```python
import torch

def _softmax(x, axis=-1):
    return torch.softmax(x, dim=axis)

def _gelu(x):
    return torch.nn.functional.gelu(x, approximate='tanh')

def _silu(x):
    return torch.nn.functional.silu(x)

def _sigmoid(x):
    return torch.sigmoid(x)

def _layernorm(x, axis=-1, eps=1e-05):
    m = x.mean(dim=axis, keepdim=True)
    v = x.var(dim=axis, keepdim=True, unbiased=False)
    return (x - m) / torch.sqrt(v + eps)

def _scatter_add(tgt, idx, src, axis=0):
    return tgt.clone().index_add_(axis, idx, src)

def _segment_sum(src, seg, n):
    out = torch.zeros((n,) + src.shape[1:], dtype=src.dtype)
    return out.index_add_(0, seg, src)

def _pad(x, pw):
    flat = tuple((v for pair in reversed(pw) for v in pair))
    return torch.nn.functional.pad(x, flat)
kernel_name = 'adaLN_cond__grouped_kv_mqa__dense_activation_silu__prompt'

def setup_inputs():

    _w = lambda a: (lambda t: t.to(torch.bfloat16) if t.is_floating_point() else t)(torch.as_tensor(a))

    def _A_setup():
        B, S, D, H = (1, 2048, 1024, 32)
        Dh = D // H
        return {'x': _w((lambda *s: torch.randn(*s, dtype=torch.bfloat16) * 0.05)(B, S, D)), 'wq': _w((lambda *s: torch.randn(*s, dtype=torch.bfloat16) * 0.05)(D, H * Dh)), 'wk': _w((lambda *s: torch.randn(*s, dtype=torch.bfloat16) * 0.05)(D, Dh)), 'wv': _w((lambda *s: torch.randn(*s, dtype=torch.bfloat16) * 0.05)(D, Dh)), 'wo': _w((lambda *s: torch.randn(*s, dtype=torch.bfloat16) * 0.05)(H * Dh, D))}
    _A_in = _A_setup()

    def _B_setup():
        B, S, D, F = (1, 2048, 1024, 3584)
        return {'x': _w((lambda *s: torch.randn(*s, dtype=torch.bfloat16) * 0.05)(B, S, D)), 'w1': _w((lambda *s: torch.randn(*s, dtype=torch.bfloat16) * 3.2)(D, F) * D ** (-0.5)), 'w2': _w((lambda *s: torch.randn(*s, dtype=torch.bfloat16) * 0.8)(F, D) * F ** (-0.5))}
    _B_in = _B_setup()
    return {'A_x': _A_in['x'], 'A_wq': _A_in['wq'], 'A_wk': _A_in['wk'], 'A_wv': _A_in['wv'], 'A_wo': _A_in['wo'], 'B_x': _B_in['x'], 'B_w1': _B_in['w1'], 'B_w2': _B_in['w2'], 'c': _w((lambda *s: torch.randn(*s, dtype=torch.bfloat16) * 0.1)(1, 1024) * 8.0), 'w_mod': _w((lambda *s: torch.randn(*s, dtype=torch.bfloat16) * 0.1)(1024, 6 * 1024) * 1024 ** (-0.5) * 8.0)}

def reference(A_x, A_wq, A_wk, A_wv, A_wo, B_x, B_w1, B_w2, c, w_mod):

    def _A_ref(x, wq, wk, wv, wo):
        B, S, D = x.shape
        H = 32
        Dh = D // H
        q = (lambda x, *d: x.permute(*d))((x @ wq).reshape(B, S, H, Dh), 0, 2, 1, 3)
        k = x @ wk
        v = x @ wv
        scores = torch.einsum('bhqd,bkd->bhqk', q, k) * Dh ** (-0.5)
        p = _softmax(scores, axis=-1)
        o = torch.einsum('bhqk,bkd->bhqd', p, v)
        return (lambda x, *d: x.permute(*d))(o, 0, 2, 1, 3).reshape(B, S, H * Dh) @ wo

    def _B_ref(x, w1, w2):
        h = torch.einsum('bsd,df->bsf', x, w1)
        h = _silu(h)
        out = torch.einsum('bsf,fd->bsd', h, w2)
        return out
    x = A_x
    mod = (_silu(c) @ w_mod).reshape(c.shape[0], 6, 1, -1)
    sa, ca, ga, sb, cb, gb = (mod[:, 0].clone(), mod[:, 1].clone(), mod[:, 2].clone(), mod[:, 3].clone(), mod[:, 4].clone(), mod[:, 5].clone())
    h = _layernorm(x, axis=-1) * (1 + ca) + sa
    x = x + ga * _A_ref(h, A_wq, A_wk, A_wv, A_wo)
    h = _layernorm(x, axis=-1) * (1 + cb) + sb
    x = x + gb * _B_ref(h, B_w1, B_w2)
    return x
```

```cpp
#include <hip/hip_runtime.h>
#include <hip/hip_bf16.h>
#include <stdint.h>

typedef unsigned short u16;
typedef __bf16 bf16x8 __attribute__((ext_vector_type(8)));
typedef __bf16 bf16x2 __attribute__((ext_vector_type(2)));
typedef short s16x4 __attribute__((ext_vector_type(4)));
typedef short s16x8 __attribute__((ext_vector_type(8)));
typedef float f32x16 __attribute__((ext_vector_type(16)));
typedef float f32x4 __attribute__((ext_vector_type(4)));
typedef float f32x2 __attribute__((ext_vector_type(2)));
typedef unsigned u32x4 __attribute__((ext_vector_type(4)));
typedef unsigned u32x2 __attribute__((ext_vector_type(2)));
#define LDSP __attribute__((address_space(3)))
typedef LDSP char* lptr;
#define DI __device__ __forceinline__

constexpr int S_ = 2048, D_ = 1024, F_ = 3584;
constexpr size_t WS_MOD = 0;
constexpr size_t WS_UT = 32768;
constexpr size_t WS_KT = 65536;
constexpr size_t WS_VT = WS_KT + 131072;
constexpr size_t WS_ST = WS_VT + 131072;
constexpr size_t WS_H = 1u << 20;
constexpr size_t WS_O = WS_H + (4u << 20);
constexpr size_t WS_X2 = WS_O + (4u << 20);
constexpr size_t WS_Y = WS_X2 + (4u << 20);
constexpr size_t WS_G = WS_Y + (4u << 20);

DI float bf2f(u16 v) { return __uint_as_float((unsigned)v << 16); }
DI float bflo(unsigned v) { return __uint_as_float(v << 16); }
DI float bfhi(unsigned v) { return __uint_as_float(v & 0xffff0000u); }
DI unsigned pk2(float a, float b) { f32x2 v = {a, b}; return __builtin_bit_cast(unsigned, __builtin_convertvector(v, bf16x2)); }
DI float rbf(float a) { return bflo(pk2(a, 0.f)); }
DI f32x16 mfma32(bf16x8 a, bf16x8 b, f32x16 c) { return __builtin_amdgcn_mfma_f32_32x32x16_bf16(a, b, c, 0, 0, 0); }
DI f32x4 mfma16(bf16x8 a, bf16x8 b, f32x4 c) { return __builtin_amdgcn_mfma_f32_16x16x32_bf16(a, b, c, 0, 0, 0); }
DI bf16x8 lds_b128(lptr p) { return *(const LDSP bf16x8*)p; }
DI s16x4 lds_tr(lptr p) { return __builtin_amdgcn_ds_read_tr16_b64_v4i16((LDSP s16x4*)p); }
DI bf16x8 cat(s16x4 a, s16x4 b) { return __builtin_bit_cast(bf16x8, __builtin_shufflevector(a, b, 0, 1, 2, 3, 4, 5, 6, 7)); }
#define SBAR() __builtin_amdgcn_sched_barrier(0)
#define PIN(x) asm volatile("" : "+v"(x))
template <int N> DI void wait_vm() { asm volatile("s_waitcnt vmcnt(%0)" ::"i"(N) : "memory"); }
DI void bar() { asm volatile("s_barrier" ::: "memory"); }
DI void lgkm_bar() { asm volatile("s_waitcnt lgkmcnt(0)\n\ts_barrier" ::: "memory"); }
DI float wave_sum(float v) {
#pragma unroll
    for (int o = 32; o; o >>= 1) v += __shfl_xor(v, o);
    return v;
}
DI float silu_f(float z) { return z * __builtin_amdgcn_rcpf(1.f + __builtin_amdgcn_exp2f(z * -1.4426950408889634f)); }

#define GLDS(v, s, off) "global_load_lds_dwordx4 %[" #v "], %[" #s "] offset:" #off "\n\t"
#define SETM0(m) "s_mov_b32 m0, %[" #m "]\n\ts_nop 0\n\t"

template <int NV>
DI void gemv_cols(const u16* __restrict__ W, int ldw, int col0, const float* cv0, const float* cv1, int lanes_per_row_log2,
                  float* red, float (&res)[NV], int& rescol, bool& active) {}

__global__ void __launch_bounds__(256) k_mod(const u16* __restrict__ c, const u16* __restrict__ wmod, float* __restrict__ mod) {
    __shared__ float red[4][32];
    const int t = threadIdx.x, ch = t & 3, rg = t >> 2, col0 = blockIdx.x * 32;
    u32x4 w[16];
#pragma unroll
    for (int i = 0; i < 16; ++i) w[i] = __builtin_nontemporal_load((const u32x4*)(wmod + (size_t)(i * 64 + rg) * 6144 + col0 + ch * 8));
    float acc[8] = {0, 0, 0, 0, 0, 0, 0, 0};
#pragma unroll
    for (int i = 0; i < 16; ++i) {
        const float cv = bf2f(c[i * 64 + rg]);
        const float s = rbf(cv / (1.f + __expf(-cv)));
#pragma unroll
        for (int j = 0; j < 4; ++j) { acc[2 * j] += s * bflo(w[i][j]); acc[2 * j + 1] += s * bfhi(w[i][j]); }
    }
#pragma unroll
    for (int j = 0; j < 8; ++j) {
#pragma unroll
        for (int o = 4; o < 64; o <<= 1) acc[j] += __shfl_xor(acc[j], o);
    }
    if ((t & 63) < 4) {
#pragma unroll
        for (int j = 0; j < 8; ++j) red[t >> 6][ch * 8 + j] = acc[j];
    }
    __syncthreads();
    if (t < 32) {
        float v = rbf(red[0][t] + red[1][t] + red[2][t] + red[3][t]);
        const int col = col0 + t, part = col >> 10;
        if (part == 1 || part == 4) v = rbf(1.f + v);
        mod[col] = v;
    }
}

__global__ void __launch_bounds__(256) k_ln_kv(const u16* __restrict__ x, const u16* __restrict__ wk, const u16* __restrict__ wv,
                                               const u16* __restrict__ w1, char* __restrict__ ws) {
    extern __shared__ __attribute__((aligned(16))) char smem[];
    const lptr lds = (lptr)smem;
    const int t = threadIdx.x, lane = t & 63, wid = __builtin_amdgcn_readfirstlane(t >> 6);
    const int b = blockIdx.x, tg = b >> 1, which = b & 1;
    const float* mod = (const float*)(ws + WS_MOD);
    {
        const u16* W = which ? wv : wk;
        const unsigned ldsb = (unsigned)(uintptr_t)lds + 32768 + wid * 16384;
        const int row = lane >> 2, chn = (lane & 3) ^ (((row >> 3) & 1) << 1);
        const unsigned vo = (unsigned)(wid * 16384 + row * 64 + chn * 16);
#pragma unroll
        for (int q = 0; q < 4; ++q) {
            const unsigned m = ldsb + q * 4096;
            const u16* sb_ = W + q * 2048;
            asm volatile(SETM0(m) GLDS(v, s, 0) GLDS(v, s, 1024) GLDS(v, s, 2048) GLDS(v, s, 3072)::[m] "s"(m), [v] "v"(vo), [s] "s"(sb_) : "memory");
        }
    }
    u32x4 xr[4][2];
#pragma unroll
    for (int r = 0; r < 4; ++r) {
        const u16* xp = x + (size_t)(tg * 16 + wid * 4 + r) * D_;
        xr[r][0] = *(const u32x4*)(xp + lane * 8);
        xr[r][1] = *(const u32x4*)(xp + 512 + lane * 8);
    }
    f32x4 sa[2][2], a1[2][2];
#pragma unroll
    for (int h = 0; h < 2; ++h)
#pragma unroll
        for (int q = 0; q < 2; ++q) {
            sa[h][q] = *(const f32x4*)(mod + h * 512 + lane * 8 + q * 4);
            a1[h][q] = *(const f32x4*)(mod + 1024 + h * 512 + lane * 8 + q * 4);
        }
    u32x4 w1r[8];
    const bool doUT = b < 224;
    if (doUT) {
#pragma unroll
        for (int i = 0; i < 8; ++i) w1r[i] = *(const u32x4*)(w1 + (size_t)((t >> 1) + 128 * i) * F_ + b * 16 + (t & 1) * 8);
    }
    char* hAT = ws + WS_H + (size_t)(tg >> 3) * 262144;
#pragma unroll
    for (int r = 0; r < 4; ++r) {
        float v[16];
#pragma unroll
        for (int h = 0; h < 2; ++h)
#pragma unroll
            for (int j = 0; j < 4; ++j) { v[h * 8 + 2 * j] = bflo(xr[r][h][j]); v[h * 8 + 2 * j + 1] = bfhi(xr[r][h][j]); }
        float s = 0;
#pragma unroll
        for (int j = 0; j < 16; ++j) s += v[j];
        const float mean = wave_sum(s) * (1.f / 1024.f);
        float q = 0;
#pragma unroll
        for (int j = 0; j < 16; ++j) { v[j] -= mean; q += v[j] * v[j]; }
        const float rstd = __builtin_amdgcn_rsqf(wave_sum(q) * (1.f / 1024.f) + 1e-5f);
        const int tok = wid * 4 + r;
#pragma unroll
        for (int h = 0; h < 2; ++h) {
            u32x4 o;
#pragma unroll
            for (int j = 0; j < 4; ++j) {
                const int e = 2 * j;
                const float h0 = v[h * 8 + e] * rstd * a1[h][e >> 2][e & 3] + sa[h][e >> 2][e & 3];
                const float h1 = v[h * 8 + e + 1] * rstd * a1[h][e >> 2][(e & 3) + 1] + sa[h][e >> 2][(e & 3) + 1];
                o[j] = pk2(h0, h1);
            }
            const int kc = h * 64 + lane;
            *(LDSP u32x4*)(lds + kc * 256 + ((tok ^ (kc & 7)) * 16)) = o;
            if (h == which) *(u32x4*)(hAT + (size_t)kc * 2048 + ((tg & 7) * 16 + tok) * 16) = o;
        }
    }
    float ut[2][8];
    if (doUT) {
#pragma unroll
        for (int j = 0; j < 8; ++j) ut[0][j] = ut[1][j] = 0.f;
#pragma unroll
        for (int i = 0; i < 8; ++i) {
            const int k = (t >> 1) + 128 * i;
            const float a2 = mod[4096 + k], sb = mod[3072 + k];
#pragma unroll
            for (int j = 0; j < 4; ++j) {
                const float lo = bflo(w1r[i][j]), hi = bfhi(w1r[i][j]);
                ut[0][2 * j] += a2 * lo; ut[0][2 * j + 1] += a2 * hi;
                ut[1][2 * j] += sb * lo; ut[1][2 * j + 1] += sb * hi;
            }
        }
#pragma unroll
        for (int v = 0; v < 2; ++v)
#pragma unroll
            for (int j = 0; j < 8; ++j) {
#pragma unroll
                for (int o = 2; o < 64; o <<= 1) ut[v][j] += __shfl_xor(ut[v][j], o);
            }
    }
    wait_vm<0>();
    __syncthreads();
    f32x4 acc[2] = {{0, 0, 0, 0}, {0, 0, 0, 0}};
    {
        const int g = lane >> 4, tk = lane & 15, q = (lane & 15) >> 2, p = lane & 3;
#pragma unroll
        for (int s = 0; s < 8; ++s) {
            const int kc = wid * 32 + s * 4 + g;
            const bf16x8 hf = lds_b128(lds + kc * 256 + ((tk ^ (kc & 7)) * 16));
            const int row = kc * 8 + q;
#pragma unroll
            for (int nb = 0; nb < 2; ++nb) {
                const lptr wp = lds + 32768 + row * 64 + ((nb ^ (kc & 1)) * 32) + p * 8;
                const bf16x8 wf = cat(lds_tr(wp), lds_tr(wp + 256));
                acc[nb] = which ? mfma16(hf, wf, acc[nb]) : mfma16(wf, hf, acc[nb]);
            }
        }
    }
    __syncthreads();
    float* red = (float*)smem;
#pragma unroll
    for (int nb = 0; nb < 2; ++nb) *(f32x4*)(red + ((wid * 2 + nb) * 64 + lane) * 4) = acc[nb];
    if (doUT && lane < 2) {
#pragma unroll
        for (int v = 0; v < 2; ++v)
#pragma unroll
            for (int j = 0; j < 8; ++j) red[2048 + (wid * 2 + v) * 16 + lane * 8 + j] = ut[v][j];
    }
    __syncthreads();
    if (t < 128) {
        const int nb = t >> 6;
        f32x4 r = *(const f32x4*)(red + (nb * 64 + lane) * 4);
#pragma unroll
        for (int w = 1; w < 4; ++w) r += *(const f32x4*)(red + ((w * 2 + nb) * 64 + lane) * 4);
        u32x2 o = {pk2(r[0], r[1]), pk2(r[2], r[3])};
        const int g = lane >> 4, c = lane & 15, kt = tg >> 1;
        if (which) {
            *(u32x2*)(ws + WS_VT + kt * 2048 + ((tg & 1) * 2 + (g & 1)) * 512 + (nb * 16 + c) * 16 + (g >> 1) * 8) = o;
        } else {
            *(u32x2*)(ws + WS_KT + kt * 2048 + (nb * 2 + (g & 1)) * 512 + ((tg & 1) * 16 + c) * 16 + (g >> 1) * 8) = o;
        }
    } else if (doUT && t < 160) {
        const int i = t - 128, v = i >> 4, c = i & 15;
        float* UT = (float*)(ws + WS_UT);
        UT[v * F_ + b * 16 + c] = red[2048 + v * 16 + c] + red[2048 + 32 + v * 16 + c] + red[2048 + 64 + v * 16 + c] + red[2048 + 96 + v * 16 + c];
    }
}

constexpr int G64_STAGE = 24576;
template <int NST, int NS>
DI void g64_loader(const char* act, const u16* W, int ldw, unsigned ldsb, int lw, int lane) {
    const unsigned va = lw * 4096 + lane * 16;
    const int row = lw * 16 + (lane >> 3), chn = (lane & 7) ^ (((row >> 1) & 1) << 2);
    const unsigned vw0 = (unsigned)(row * ldw * 2 + chn * 16), vw1 = vw0 + 8 * ldw * 2 - 1024;
    const char* sa = act;
    const char* sw = (const char*)W;
    auto issue = [&](int slot) {
        const unsigned ma = ldsb + slot * G64_STAGE + lw * 4096, mw = ldsb + slot * G64_STAGE + 16384 + lw * 2048;
        asm volatile(SETM0(ma) GLDS(va, sa, 0) GLDS(va, sa, 1024) GLDS(va, sa, 2048) GLDS(va, sa, 3072) SETM0(mw) GLDS(vw0, sw, 0) GLDS(vw1, sw, 1024)
                     ::[ma] "s"(ma), [mw] "s"(mw), [va] "v"(va), [vw0] "v"(vw0), [vw1] "v"(vw1), [sa] "s"(sa), [sw] "s"(sw) : "memory");
        sa += 16384; sw += (size_t)64 * ldw * 2;
    };
#pragma unroll
    for (int i = 0; i < NS; ++i) issue(i);
    wait_vm<6 * (NS - 1)>(); bar();
    int slot = 0;
    for (int i = 0; i < NST - NS; ++i) {
        wait_vm<6 * (NS - 2)>(); bar();
        issue(slot); slot = (slot + 1 == NS) ? 0 : slot + 1;
    }
    [&]<int... I>(std::integer_sequence<int, I...>) { ((wait_vm<6 * (NS - 2 - I)>(), bar()), ...); }(std::make_integer_sequence<int, NS - 1>{});
}

struct G64Frag { bf16x8 a[2][2]; s16x4 w[2][2][2]; };
template <int NST, int NS, bool PERM>
DI void g64_compute(lptr lds, int cw, int lane, f32x16 (&acc)[2][2]) {
    const int th = cw & 1, kh = cw >> 1, hi = lane >> 5;
    const lptr pa = lds + (kh * 4 + hi) * 2048 + (th * 64 + (lane & 31)) * 16;
    const int q = (lane & 15) >> 2, p = lane & 3, nsub = (lane >> 4) & 1;
    const int row0 = kh * 32 + (PERM ? 4 : 8) * hi + q;
    const int chn = (nsub * 2 + (p >> 1)) ^ (((q >> 1) & 1) << 2);
    const lptr pw0 = lds + 16384 + row0 * 128 + chn * 16 + (p & 1) * 8;
    const lptr pw1 = lds + 16384 + row0 * 128 + (chn ^ 4) * 16 + (p & 1) * 8;
    constexpr int TS = (PERM ? 8 : 4) * 128;
#define G64_RA(F, so, s, tb) F.a[s][tb] = lds_b128(pa + (so) + (s) * 4096 + (tb) * 512)
#define G64_RW(F, so, s, nb) do { F.w[s][nb][0] = lds_tr((nb ? pw1 : pw0) + (so) + (s) * 2048); F.w[s][nb][1] = lds_tr((nb ? pw1 : pw0) + (so) + (s) * 2048 + TS); } while (0)
#define G64_MM(F, s, nb, tb) acc[nb][tb] = mfma32(cat(F.w[s][nb][0], F.w[s][nb][1]), F.a[s][tb], acc[nb][tb])
#define G64_STEP(C, N, so, RD) do { \
        G64_MM(C, 0, 0, 0); if (RD) { G64_RA(N, so, 0, 0); G64_RW(N, so, 0, 0); } SBAR(); \
        G64_MM(C, 0, 0, 1); if (RD) { G64_RA(N, so, 0, 1); G64_RW(N, so, 0, 1); } SBAR(); \
        G64_MM(C, 0, 1, 0); if (RD) { G64_RA(N, so, 1, 0); G64_RW(N, so, 1, 0); } SBAR(); \
        G64_MM(C, 0, 1, 1); if (RD) { G64_RA(N, so, 1, 1); G64_RW(N, so, 1, 1); } SBAR(); \
        G64_MM(C, 1, 0, 0); G64_MM(C, 1, 0, 1); G64_MM(C, 1, 1, 0); G64_MM(C, 1, 1, 1); SBAR(); } while (0)
    G64Frag A, B;
    bar();
    G64_RA(A, 0, 0, 0); G64_RW(A, 0, 0, 0); G64_RA(A, 0, 0, 1); G64_RW(A, 0, 0, 1);
    G64_RA(A, 0, 1, 0); G64_RW(A, 0, 1, 0); G64_RA(A, 0, 1, 1); G64_RW(A, 0, 1, 1);
    int so = 0;
    static_assert(NST % 2 == 0, "");
    for (int i = 0; i < NST - 2; i += 2) {
        so = (so + G64_STAGE == NS * G64_STAGE) ? 0 : so + G64_STAGE;
        lgkm_bar(); SBAR();
        G64_STEP(A, B, so, true);
        so = (so + G64_STAGE == NS * G64_STAGE) ? 0 : so + G64_STAGE;
        lgkm_bar(); SBAR();
        G64_STEP(B, A, so, true);
    }
    so = (so + G64_STAGE == NS * G64_STAGE) ? 0 : so + G64_STAGE;
    lgkm_bar(); SBAR();
    G64_STEP(A, B, so, true);
    G64_STEP(B, A, 0, false);
}
DI void g64_reduce_send(lptr scratch, int cw, int lane, const f32x16 (&acc)[2][2]) {
    const int kh = cw >> 1;
#pragma unroll
    for (int tb = 0; tb < 2; ++tb)
#pragma unroll
        for (int i = 0; i < 4; ++i) {
            const f32x16& a = kh ? acc[0][tb] : acc[1][tb];
            *(LDSP f32x4*)(scratch + ((cw * 2 + tb) * 4 + i) * 1024 + lane * 16) = (f32x4){a[4 * i], a[4 * i + 1], a[4 * i + 2], a[4 * i + 3]};
        }
}
DI void g64_reduce_recv(lptr scratch, int cw, int lane, const f32x16 (&acc)[2][2], f32x16 (&r)[2]) {
    const int kh = cw >> 1;
#pragma unroll
    for (int tb = 0; tb < 2; ++tb) {
        r[tb] = kh ? acc[1][tb] : acc[0][tb];
#pragma unroll
        for (int i = 0; i < 4; ++i) {
            const f32x4 v = *(const LDSP f32x4*)(scratch + (((cw ^ 2) * 2 + tb) * 4 + i) * 1024 + lane * 16);
#pragma unroll
            for (int j = 0; j < 4; ++j) r[tb][4 * i + j] += v[j];
        }
    }
}
DI void tile_map(int b, int& p, int& j) { const int x = b & 7, i = b >> 3; p = (x >> 1) * 4 + (i >> 3); j = (x & 1) * 8 + (i & 7); }

constexpr int QA_NS = 4;
constexpr int QA_KV0 = QA_NS * G64_STAGE;
constexpr int QA_LDS = QA_KV0 + 65536;
constexpr float QSCALE = 0.17677669529663687f * 1.4426950408889634f;

__global__ void __launch_bounds__(512) k_qattn(const u16* __restrict__ wq, char* __restrict__ ws) {
    extern __shared__ __attribute__((aligned(16))) char smem[];
    const lptr lds = (lptr)smem;
    const int t = threadIdx.x, lane = t & 63, wid = __builtin_amdgcn_readfirstlane(t >> 6);
    int p, j; tile_map(blockIdx.x, p, j);
    const unsigned ldsb = (unsigned)(uintptr_t)lds;
    const char* KT = ws + WS_KT; const char* VT = ws + WS_VT;
    const unsigned vkv = lane * 16;
    auto kv_piece = [&](int chunk, unsigned buf, int i) {
        const char* s = ((i & 4) ? VT : KT) + chunk * 32768 + wid * 4096 + (i & 3) * 1024;
        const unsigned m = ldsb + buf + (i >> 2) * 32768 + wid * 4096 + (i & 3) * 1024;
        asm volatile(SETM0(m) GLDS(v, s, 0)::[m] "s"(m), [v] "v"(vkv), [s] "s"(s) : "memory");
    };
    if (wid >= 4) {
        g64_loader<16, QA_NS>(ws + WS_H + (size_t)p * 262144, wq + j * 64, D_, ldsb, wid - 4, lane);
#pragma unroll
        for (int i = 0; i < 8; ++i) kv_piece(0, QA_KV0, i);
        bar();
        bar();
    } else {
#pragma unroll
        for (int i = 0; i < 8; ++i) kv_piece(0, QA_KV0, i);
        f32x16 acc[2][2] = {};
        g64_compute<16, QA_NS, false>(lds, wid, lane, acc);
        g64_reduce_send(lds, wid, lane, acc);
        lgkm_bar();
        f32x16 r[2];
        g64_reduce_recv(lds, wid, lane, acc, r);
#pragma unroll
        for (int tb = 0; tb < 2; ++tb)
#pragma unroll
            for (int s = 0; s < 2; ++s) {
                u32x4 o;
#pragma unroll
                for (int i = 0; i < 4; ++i) o[i] = pk2(r[tb][8 * s + 2 * i] * QSCALE, r[tb][8 * s + 2 * i + 1] * QSCALE);
                *(LDSP u32x4*)(lds + 32768 + ((wid * 2 + tb) * 2 + s) * 1024 + lane * 16) = o;
            }
        lgkm_bar();
    }
    const int qg = wid & 3, kh = wid >> 2;
    bf16x8 qA0, qA1, qB0, qB1;
    {
        const lptr qp = lds + 32768 + qg * 4096 + lane * 16;
        qA0 = lds_b128(qp); qA1 = lds_b128(qp + 1024); qB0 = lds_b128(qp + 2048); qB1 = lds_b128(qp + 3072);
    }
    bf16x8 sel;
    {
        const unsigned on = ((lane & 15) == ((lane >> 4) & 1)) ? 0x3f803f80u : 0u;
        sel = __builtin_bit_cast(bf16x8, (u32x4){on, on, on, on});
    }
    f32x16 oA = {}, oB = {}, sA, sB;
    f32x4 lA = {}, lB = {};
    const f32x16 zero = {};
    u32x4 pA0, pA1, pB0, pB1;
    bf16x8 k0, k1, v0, v1, vp0, vp1;
    wait_vm<0>(); lgkm_bar();
#define EXP4(S, i) do { S[i] = __builtin_amdgcn_exp2f(S[i]); S[i + 1] = __builtin_amdgcn_exp2f(S[i + 1]); S[i + 2] = __builtin_amdgcn_exp2f(S[i + 2]); S[i + 3] = __builtin_amdgcn_exp2f(S[i + 3]); } while (0)
#define PK2(P, d, S, i) do { P[d] = pk2(S[i], S[i + 1]); P[d + 1] = pk2(S[i + 2], S[i + 3]); } while (0)
#define BF(x) __builtin_bit_cast(bf16x8, x)
#define HALF(SX, PX0, PX1, SY, KY0, KY1, QY0, QY1, OZ, LZ, VZ0, VZ1, PZ0, PZ1, DOY, DOZ, RD0, RD1) do { \
        if (DOZ) OZ = mfma32(VZ0, BF(PZ0), OZ); EXP4(SX, 0); RD0; PK2(PX0, 0, SX, 0); PIN(PX0); SBAR(); \
        if (DOY) SY = mfma32(KY0, QY0, zero); EXP4(SX, 4); if (DOZ) LZ = mfma16(sel, BF(PZ0), LZ); PK2(PX0, 2, SX, 4); PIN(PX0); SBAR(); \
        if (DOZ) OZ = mfma32(VZ1, BF(PZ1), OZ); EXP4(SX, 8); RD1; PK2(PX1, 0, SX, 8); PIN(PX1); SBAR(); \
        if (DOY) SY = mfma32(KY1, QY1, SY); EXP4(SX, 12); if (DOZ) LZ = mfma16(sel, BF(PZ1), LZ); PK2(PX1, 2, SX, 12); PIN(PX1); SBAR(); } while (0)
    unsigned buf = QA_KV0, nbuf = 0;
    const int lo = kh * 16384 + lane * 16;
    {
        const lptr kp = lds + buf + lo;
        k0 = lds_b128(kp); k1 = lds_b128(kp + 1024);
        sA = mfma32(k0, qA0, zero); sA = mfma32(k1, qA1, sA);
        pB0 = (u32x4){0, 0, 0, 0}; pB1 = pB0; vp0 = k0; vp1 = k1;
    }
    for (int c = 0; c < 4; ++c) {
        const lptr kp = lds + buf + lo;
#pragma unroll
        for (int it = 0; it < 8; ++it) {
            if (c < 3) kv_piece(c + 1, nbuf, it);
            HALF(sA, pA0, pA1, sB, k0, k1, qB0, qB1, oB, lB, vp0, vp1, pB0, pB1, true, true,
                 v0 = lds_b128(kp + 32768 + it * 2048), v1 = lds_b128(kp + 32768 + it * 2048 + 1024));
            if (it < 7) {
                k0 = lds_b128(kp + (it + 1) * 2048); k1 = lds_b128(kp + (it + 1) * 2048 + 1024);
                HALF(sB, pB0, pB1, sA, k0, k1, qA0, qA1, oA, lA, v0, v1, pA0, pA1, true, true, (void)0, (void)0);
            } else {
                if (c < 3) {
                    wait_vm<0>(); lgkm_bar();
                    const lptr kn = lds + nbuf + lo;
                    k0 = lds_b128(kn); k1 = lds_b128(kn + 1024);
                }
                HALF(sB, pB0, pB1, sA, k0, k1, qA0, qA1, oA, lA, v0, v1, pA0, pA1, true, true, (void)0, (void)0);
            }
            vp0 = v0; vp1 = v1;
        }
        const unsigned tmp = buf; buf = nbuf; nbuf = tmp;
    }
    oB = mfma32(vp0, BF(pB0), oB); oB = mfma32(vp1, BF(pB1), oB);
    lB = mfma16(sel, BF(pB0), lB); lB = mfma16(sel, BF(pB1), lB);
    lgkm_bar();
    {
        const f32x16& so = kh ? oA : oB;
        const f32x4& sl = kh ? lA : lB;
#pragma unroll
        for (int i = 0; i < 4; ++i) *(LDSP f32x4*)(lds + wid * 4096 + i * 1024 + lane * 16) = (f32x4){so[4 * i], so[4 * i + 1], so[4 * i + 2], so[4 * i + 3]};
        if (lane < 16) { *(LDSP float*)(lds + 32768 + wid * 128 + lane * 4) = sl[0]; *(LDSP float*)(lds + 32768 + wid * 128 + 64 + lane * 4) = sl[1]; }
    }
    lgkm_bar();
    f32x16 o = kh ? oB : oA;
    float l;
    {
        const f32x4 ml = kh ? lB : lA;
        const float own0 = __shfl(ml[0], lane & 15), own1 = __shfl(ml[1], lane & 15);
        l = ((lane & 16) ? own1 : own0) + *(const LDSP float*)(lds + 32768 + (wid ^ 4) * 128 + (lane & 31) * 4);
#pragma unroll
        for (int i = 0; i < 4; ++i) {
            const f32x4 v = *(const LDSP f32x4*)(lds + (wid ^ 4) * 4096 + i * 1024 + lane * 16);
#pragma unroll
            for (int e = 0; e < 4; ++e) o[4 * i + e] += v[e];
        }
    }
    float chk = l * 0.f;
#pragma unroll
    for (int i = 0; i < 16; ++i) chk = __builtin_fmaf(o[i], 0.f, chk);
    const bool bad = !(chk == 0.f) || !(l >= 0x1p-90f);
    if (__builtin_expect(__any(bad), 0)) {
        const bf16x8 q0 = kh ? qB0 : qA0, q1 = kh ? qB1 : qA1;
        float m = -INFINITY; l = 0.f; o = zero;
        for (int kt = 0; kt < 64; ++kt) {
            const bf16x8 a0 = *(const bf16x8*)(KT + kt * 2048 + lane * 16), a1 = *(const bf16x8*)(KT + kt * 2048 + 1024 + lane * 16);
            const bf16x8 b0 = *(const bf16x8*)(VT + kt * 2048 + lane * 16), b1 = *(const bf16x8*)(VT + kt * 2048 + 1024 + lane * 16);
            f32x16 s = mfma32(a0, q0, zero); s = mfma32(a1, q1, s);
            float mx = s[0];
#pragma unroll
            for (int i = 1; i < 16; ++i) mx = fmaxf(mx, s[i]);
            mx = fmaxf(mx, __shfl_xor(mx, 32));
            const float mn = fmaxf(m, mx), al = __builtin_amdgcn_exp2f(m - mn);
            m = mn;
            u32x4 p0, p1; float rs = 0.f;
#pragma unroll
            for (int i = 0; i < 4; ++i) {
                p0[i] = pk2(__builtin_amdgcn_exp2f(s[2 * i] - m), __builtin_amdgcn_exp2f(s[2 * i + 1] - m));
                p1[i] = pk2(__builtin_amdgcn_exp2f(s[8 + 2 * i] - m), __builtin_amdgcn_exp2f(s[9 + 2 * i] - m));
                rs += bflo(p0[i]) + bfhi(p0[i]) + bflo(p1[i]) + bfhi(p1[i]);
            }
            rs += __shfl_xor(rs, 32);
            l = l * al + rs;
#pragma unroll
            for (int i = 0; i < 16; ++i) o[i] *= al;
            o = mfma32(b0, BF(p0), o); o = mfma32(b1, BF(p1), o);
        }
    }
    {
        const float inv = 1.f / l;
        char* oAT = ws + WS_O + (size_t)p * 262144 + (size_t)((2 * j + (qg >> 1)) * 4 + (lane >> 5)) * 2048 + (((qg & 1) * 2 + kh) * 32 + (lane & 31)) * 16;
#pragma unroll
        for (int s = 0; s < 2; ++s) {
            u32x4 w;
#pragma unroll
            for (int i = 0; i < 4; ++i) w[i] = pk2(o[8 * s + 2 * i] * inv, o[8 * s + 2 * i + 1] * inv);
            *(u32x4*)(oAT + s * 4096) = w;
        }
    }
}

constexpr int OP_NS = 6;
__global__ void __launch_bounds__(512) k_oproj(const u16* __restrict__ x, const u16* __restrict__ wo, char* __restrict__ ws) {
    extern __shared__ __attribute__((aligned(16))) char smem[];
    const lptr lds = (lptr)smem;
    const int t = threadIdx.x, lane = t & 63, wid = __builtin_amdgcn_readfirstlane(t >> 6);
    int p, j; tile_map(blockIdx.x, p, j);
    if (wid >= 4) {
        g64_loader<16, OP_NS>(ws + WS_O + (size_t)p * 262144, wo + j * 64, D_, (unsigned)(uintptr_t)lds, wid - 4, lane);
        bar(); bar();
        return;
    }
    const int th = wid & 1, nb = wid >> 1, hi = lane >> 5;
    const int n0 = j * 64 + nb * 32 + 4 * hi;
    const float* mod = (const float*)(ws + WS_MOD);
    u32x2 xr[2][4]; f32x4 ga[4], a2[4];
#pragma unroll
    for (int i = 0; i < 4; ++i) {
#pragma unroll
        for (int tb = 0; tb < 2; ++tb) xr[tb][i] = *(const u32x2*)(x + (size_t)(p * 128 + th * 64 + tb * 32 + (lane & 31)) * D_ + n0 + 8 * i);
        ga[i] = *(const f32x4*)(mod + 2048 + n0 + 8 * i);
        a2[i] = *(const f32x4*)(mod + 4096 + n0 + 8 * i);
    }
    f32x16 acc[2][2] = {};
    g64_compute<16, OP_NS, true>(lds, wid, lane, acc);
    g64_reduce_send(lds, wid, lane, acc);
    lgkm_bar();
    f32x16 r[2];
    g64_reduce_recv(lds, wid, lane, acc, r);
    f32x2 st[2];
#pragma unroll
    for (int tb = 0; tb < 2; ++tb) {
        u32x4 xo[2], yo[2]; float v[16]; float s = 0.f;
#pragma unroll
        for (int i = 0; i < 4; ++i) {
            const float x0 = bflo(xr[tb][i][0]), x1 = bfhi(xr[tb][i][0]), x2 = bflo(xr[tb][i][1]), x3 = bfhi(xr[tb][i][1]);
            const unsigned a = pk2(x0 + ga[i][0] * r[tb][4 * i], x1 + ga[i][1] * r[tb][4 * i + 1]);
            const unsigned b = pk2(x2 + ga[i][2] * r[tb][4 * i + 2], x3 + ga[i][3] * r[tb][4 * i + 3]);
            xo[i >> 1][(i & 1) * 2] = a; xo[i >> 1][(i & 1) * 2 + 1] = b;
            v[4 * i] = bflo(a); v[4 * i + 1] = bfhi(a); v[4 * i + 2] = bflo(b); v[4 * i + 3] = bfhi(b);
            yo[i >> 1][(i & 1) * 2] = pk2(v[4 * i] * a2[i][0], v[4 * i + 1] * a2[i][1]);
            yo[i >> 1][(i & 1) * 2 + 1] = pk2(v[4 * i + 2] * a2[i][2], v[4 * i + 3] * a2[i][3]);
            s += (v[4 * i] + v[4 * i + 1]) + (v[4 * i + 2] + v[4 * i + 3]);
        }
        const size_t off = (size_t)p * 262144 + (size_t)((j * 2 + nb) * 4 + hi) * 2048 + (th * 64 + tb * 32 + (lane & 31)) * 16;
#pragma unroll
        for (int h = 0; h < 2; ++h) { *(u32x4*)(ws + WS_X2 + off + h * 4096) = xo[h]; *(u32x4*)(ws + WS_Y + off + h * 4096) = yo[h]; }
        s += __shfl_xor(s, 32);
        const float mean = s * (1.f / 32.f);
        float q = 0.f;
#pragma unroll
        for (int i = 0; i < 16; ++i) { const float d = v[i] - mean; q += d * d; }
        q += __shfl_xor(q, 32);
        st[tb] = (f32x2){mean, q};
    }
    if (nb == 1 && lane < 32) { *(LDSP f32x2*)(lds + 65536 + (th * 64 + lane) * 8) = st[0]; *(LDSP f32x2*)(lds + 65536 + (th * 64 + 32 + lane) * 8) = st[1]; }
    lgkm_bar();
    if (nb == 0) {
        const f32x2 a = hi ? st[1] : st[0];
        const f32x2 b = *(const LDSP f32x2*)(lds + 65536 + (th * 64 + lane) * 8);
        const float d = a[0] - b[0];
        *(f32x2*)(ws + WS_ST + ((size_t)j * S_ + p * 128 + th * 64 + lane) * 8) = (f32x2){0.5f * (a[0] + b[0]), a[1] + b[1] + 16.f * d * d};
    }
}

constexpr int F1_NS = 7, F1_STAGE = 22528, F1_NST = 32;
template <int NW>
DI void f1_loader(const char* act, const u16* W, unsigned ldsb, int lw, int lane) {
    const unsigned va = lw * 2048 + lane * 16;
    const int pc0 = lw < 2 ? lw * 4 : 8 + (lw - 2) * 3;
    unsigned vw[4];
#pragma unroll
    for (int i = 0; i < 4; ++i) {
        const int u = (pc0 + (i < NW ? i : 0)) * 64 + lane, row = u / 28, chn = (u - row * 28) ^ (((row >> 2) & 1) << 1);
        vw[i] = (unsigned)(row * F_ * 2 + chn * 16 + 4096 - i * 1024);
    }
    const char* sa = act;
    const char* sw = (const char*)W - 4096;
    auto issue = [&](int slot) {
        const unsigned ma = ldsb + slot * F1_STAGE + lw * 2048, mw = ldsb + slot * F1_STAGE + 8192 + pc0 * 1024;
        if constexpr (NW == 4)
            asm volatile(SETM0(ma) GLDS(va, sa, 0) GLDS(va, sa, 1024) SETM0(mw) GLDS(v0, sw, 0) GLDS(v1, sw, 1024) GLDS(v2, sw, 2048) GLDS(v3, sw, 3072)
                         ::[ma] "s"(ma), [mw] "s"(mw), [va] "v"(va), [v0] "v"(vw[0]), [v1] "v"(vw[1]), [v2] "v"(vw[2]), [v3] "v"(vw[3]), [sa] "s"(sa), [sw] "s"(sw) : "memory");
        else
            asm volatile(SETM0(ma) GLDS(va, sa, 0) GLDS(va, sa, 1024) SETM0(mw) GLDS(v0, sw, 0) GLDS(v1, sw, 1024) GLDS(v2, sw, 2048)
                         ::[ma] "s"(ma), [mw] "s"(mw), [va] "v"(va), [v0] "v"(vw[0]), [v1] "v"(vw[1]), [v2] "v"(vw[2]), [sa] "s"(sa), [sw] "s"(sw) : "memory");
        sa += 8192; sw += 32 * F_ * 2;
    };
    constexpr int PER = NW + 2;
#pragma unroll
    for (int i = 0; i < F1_NS; ++i) issue(i);
    wait_vm<PER*(F1_NS - 1)>(); bar();
    int slot = 0;
    for (int i = 0; i < F1_NST - F1_NS; ++i) {
        wait_vm<PER*(F1_NS - 2)>(); bar();
        issue(slot); slot = (slot + 1 == F1_NS) ? 0 : slot + 1;
    }
    [&]<int... I>(std::integer_sequence<int, I...>) { ((wait_vm<PER*(F1_NS - 2 - I)>(), bar()), ...); }(std::make_integer_sequence<int, F1_NS - 1>{});
}
struct F1Frag { bf16x8 a[4]; s16x4 w[7][2]; };
__global__ void __launch_bounds__(512) k_ffn1(const u16* __restrict__ w1, char* __restrict__ ws) {
    extern __shared__ __attribute__((aligned(16))) char smem[];
    const lptr lds = (lptr)smem;
    const int t = threadIdx.x, lane = t & 63, wid = __builtin_amdgcn_readfirstlane(t >> 6);
    int p, j; tile_map(blockIdx.x, p, j);
    if (wid >= 4) {
        const char* act = ws + WS_Y + (size_t)p * 262144;
        if (wid < 6) f1_loader<4>(act, w1 + j * 224, (unsigned)(uintptr_t)lds, wid - 4, lane);
        else f1_loader<3>(act, w1 + j * 224, (unsigned)(uintptr_t)lds, wid - 4, lane);
        return;
    }
    const int th = wid & 1, nh = wid >> 1, g = lane >> 4;
    float mu, rs;
    {
        f32x2 sp[16];
#pragma unroll
        for (int i = 0; i < 16; ++i) sp[i] = *(const f32x2*)(ws + WS_ST + ((size_t)i * S_ + p * 128 + th * 64 + lane) * 8);
        float s = 0.f;
#pragma unroll
        for (int i = 0; i < 16; ++i) s += sp[i][0];
        mu = s * (1.f / 16.f);
        float q = 0.f;
#pragma unroll
        for (int i = 0; i < 16; ++i) { const float d = sp[i][0] - mu; q += sp[i][1] + 64.f * d * d; }
        rs = __builtin_amdgcn_rsqf(q * (1.f / 1024.f) + 1e-5f);
    }
    const lptr pa = lds + g * 2048 + (th * 64 + (lane & 15)) * 16;
    const int q = (lane & 15) >> 2, pp = lane & 3;
    const int row0 = 16 * (g >> 1) + 4 * (g & 1) + q;
    const lptr pwe = lds + 8192 + row0 * 448 + nh * 224 + pp * 8;
    const int sw = g & 1;
    const lptr pw0 = pwe + (((nh * 7) & 1) ? (sw ? -32 : 0) : (sw ? 32 : 0));
    const lptr pw1 = pwe + (((nh * 7) & 1) ? (sw ? 32 : 0) : (sw ? -32 : 0));
    f32x4 acc[7][4] = {};
#define F1_RA(F, so, tb) F.a[tb] = lds_b128(pa + (so) + (tb) * 256)
#define F1_RW(F, so, b) do { F.w[b][0] = lds_tr(((b & 1) ? pw1 : pw0) + (so) + (b) * 32); F.w[b][1] = lds_tr(((b & 1) ? pw1 : pw0) + (so) + (b) * 32 + 8 * 448); } while (0)
#define F1_MM(F, b, tb) acc[b][tb] = mfma16(cat(F.w[b][0], F.w[b][1]), F.a[tb], acc[b][tb])
#define F1_ROW(C, N, so, RD, b, EXTRA) do { F1_MM(C, b, 0); if (RD) { EXTRA; } SBAR(); F1_MM(C, b, 1); SBAR(); F1_MM(C, b, 2); if (RD) F1_RW(N, so, b); SBAR(); F1_MM(C, b, 3); SBAR(); } while (0)
#define F1_STEP(C, N, so, RD) do { \
        F1_ROW(C, N, so, RD, 0, F1_RA(N, so, 0)); F1_ROW(C, N, so, RD, 1, F1_RA(N, so, 1)); F1_ROW(C, N, so, RD, 2, F1_RA(N, so, 2)); F1_ROW(C, N, so, RD, 3, F1_RA(N, so, 3)); \
        F1_ROW(C, N, so, RD, 4, F1_RW(N, so, 6)); F1_MM(C, 5, 0); if (RD) F1_RW(N, so, 5); SBAR(); F1_MM(C, 5, 1); F1_MM(C, 5, 2); F1_MM(C, 5, 3); \
        F1_MM(C, 6, 0); F1_MM(C, 6, 1); F1_MM(C, 6, 2); F1_MM(C, 6, 3); SBAR(); } while (0)
    F1Frag A, B;
    bar();
#pragma unroll
    for (int tb = 0; tb < 4; ++tb) F1_RA(A, 0, tb);
    F1_RW(A, 0, 0); F1_RW(A, 0, 1); F1_RW(A, 0, 2); F1_RW(A, 0, 3); F1_RW(A, 0, 4); F1_RW(A, 0, 5); F1_RW(A, 0, 6);
    int so = 0;
    for (int i = 0; i < F1_NST - 2; i += 2) {
        so = (so + F1_STAGE == F1_NS * F1_STAGE) ? 0 : so + F1_STAGE;
        lgkm_bar(); SBAR();
        F1_STEP(A, B, so, true);
        so = (so + F1_STAGE == F1_NS * F1_STAGE) ? 0 : so + F1_STAGE;
        lgkm_bar(); SBAR();
        F1_STEP(B, A, so, true);
    }
    so = (so + F1_STAGE == F1_NS * F1_STAGE) ? 0 : so + F1_STAGE;
    lgkm_bar(); SBAR();
    F1_STEP(A, B, so, true);
    F1_STEP(B, A, 0, false);
    float m4[4], r4[4];
#pragma unroll
    for (int tb = 0; tb < 4; ++tb) { m4[tb] = __shfl(mu, tb * 16 + (lane & 15)); r4[tb] = __shfl(rs, tb * 16 + (lane & 15)); }
    const float* UT = (const float*)(ws + WS_UT) + j * 224 + nh * 112 + 4 * g;
    char* gp = ws + WS_G + (size_t)p * (F_ / 8 * 2048) + (size_t)((j * 14 + nh * 7) * 2 + (g & 1)) * 2048 + (th * 64 + (lane >> 5) * 16 + (lane & 15)) * 16;
#pragma unroll
    for (int b = 0; b < 7; ++b) {
        const f32x4 u = *(const f32x4*)(UT + b * 16), tt = *(const f32x4*)(UT + F_ + b * 16);
        unsigned w[4][2];
#pragma unroll
        for (int tb = 0; tb < 4; ++tb) {
            float z[4];
#pragma unroll
            for (int e = 0; e < 4; ++e) z[e] = silu_f(__builtin_fmaf(r4[tb], __builtin_fmaf(-m4[tb], u[e], acc[b][tb][e]), tt[e]));
            w[tb][0] = pk2(z[0], z[1]); w[tb][1] = pk2(z[2], z[3]);
        }
#pragma unroll
        for (int tp = 0; tp < 2; ++tp) {
            const auto s0 = __builtin_amdgcn_permlane32_swap(w[2 * tp][0], w[2 * tp + 1][0], false, false);
            const auto s1 = __builtin_amdgcn_permlane32_swap(w[2 * tp][1], w[2 * tp + 1][1], false, false);
            *(u32x4*)(gp + b * 4096 + tp * 512) = (u32x4){s0[0], s1[0], s0[1], s1[1]};
        }
    }
}

constexpr int F2_NS = 6;
__global__ void __launch_bounds__(512) k_ffn2(const u16* __restrict__ w2, char* __restrict__ ws, u16* __restrict__ out) {
    extern __shared__ __attribute__((aligned(16))) char smem[];
    const lptr lds = (lptr)smem;
    const int t = threadIdx.x, lane = t & 63, wid = __builtin_amdgcn_readfirstlane(t >> 6);
    int p, j; tile_map(blockIdx.x, p, j);
    if (wid >= 4) {
        g64_loader<56, F2_NS>(ws + WS_G + (size_t)p * (F_ / 8 * 2048), w2 + j * 64, D_, (unsigned)(uintptr_t)lds, wid - 4, lane);
        bar();
        return;
    }
    const int th = wid & 1, nb = wid >> 1, hi = lane >> 5;
    const float* mod = (const float*)(ws + WS_MOD);
    u32x4 xr[2][2]; f32x4 gb[4];
#pragma unroll
    for (int tb = 0; tb < 2; ++tb)
#pragma unroll
        for (int h = 0; h < 2; ++h)
            xr[tb][h] = *(const u32x4*)(ws + WS_X2 + (size_t)p * 262144 + (size_t)((j * 2 + nb) * 4 + hi) * 2048 + (th * 64 + tb * 32 + (lane & 31)) * 16 + h * 4096);
#pragma unroll
    for (int i = 0; i < 4; ++i) gb[i] = *(const f32x4*)(mod + 5120 + j * 64 + nb * 32 + 4 * hi + 8 * i);
    f32x16 acc[2][2] = {};
    g64_compute<56, F2_NS, true>(lds, wid, lane, acc);
    g64_reduce_send(lds + 2 * G64_STAGE, wid, lane, acc);
    lgkm_bar();
    f32x16 r[2];
    g64_reduce_recv(lds + 2 * G64_STAGE, wid, lane, acc, r);
#pragma unroll
    for (int tb = 0; tb < 2; ++tb) {
        unsigned w[8];
#pragma unroll
        for (int i = 0; i < 4; ++i) {
            const unsigned a = xr[tb][i >> 1][(i & 1) * 2], b = xr[tb][i >> 1][(i & 1) * 2 + 1];
            w[2 * i] = pk2(bflo(a) + gb[i][0] * r[tb][4 * i], bfhi(a) + gb[i][1] * r[tb][4 * i + 1]);
            w[2 * i + 1] = pk2(bflo(b) + gb[i][2] * r[tb][4 * i + 2], bfhi(b) + gb[i][3] * r[tb][4 * i + 3]);
        }
        u16* op = out + (size_t)(p * 128 + th * 64 + tb * 32 + (lane & 31)) * D_ + j * 64 + nb * 32 + hi * 8;
#pragma unroll
        for (int h = 0; h < 2; ++h) {
            const auto s0 = __builtin_amdgcn_permlane32_swap(w[4 * h], w[4 * h + 2], false, false);
            const auto s1 = __builtin_amdgcn_permlane32_swap(w[4 * h + 1], w[4 * h + 3], false, false);
            *(u32x4*)(op + h * 16) = (u32x4){s0[0], s1[0], s0[1], s1[1]};
        }
    }
}

extern "C" void kernel_launch(void* const* d_in, const int* in_sizes, int n_in, void* d_out, int out_size, void* d_ws, size_t ws_size,
                              hipStream_t stream) {
    const u16* x = (const u16*)d_in[0];
    const u16* wq = (const u16*)d_in[1];
    const u16* wk = (const u16*)d_in[2];
    const u16* wv = (const u16*)d_in[3];
    const u16* wo = (const u16*)d_in[4];
    const u16* w1 = (const u16*)d_in[6];
    const u16* w2 = (const u16*)d_in[7];
    const u16* c = (const u16*)d_in[8];
    const u16* wmod = (const u16*)d_in[9];
    char* ws = (char*)d_ws;
    (void)hipFuncSetAttribute((const void*)k_ln_kv, hipFuncAttributeMaxDynamicSharedMemorySize, 98304);
    (void)hipFuncSetAttribute((const void*)k_qattn, hipFuncAttributeMaxDynamicSharedMemorySize, QA_LDS);
    (void)hipFuncSetAttribute((const void*)k_oproj, hipFuncAttributeMaxDynamicSharedMemorySize, OP_NS * G64_STAGE);
    (void)hipFuncSetAttribute((const void*)k_ffn1, hipFuncAttributeMaxDynamicSharedMemorySize, F1_NS * F1_STAGE);
    (void)hipFuncSetAttribute((const void*)k_ffn2, hipFuncAttributeMaxDynamicSharedMemorySize, F2_NS * G64_STAGE);
    k_mod<<<192, 256, 0, stream>>>(c, wmod, (float*)(ws + WS_MOD));
    k_ln_kv<<<256, 256, 98304, stream>>>(x, wk, wv, w1, ws);
    k_qattn<<<256, 512, QA_LDS, stream>>>(wq, ws);
    k_oproj<<<256, 512, OP_NS * G64_STAGE, stream>>>(x, wo, ws);
    k_ffn1<<<256, 512, F1_NS * F1_STAGE, stream>>>(w1, ws);
    k_ffn2<<<256, 512, F2_NS * G64_STAGE, stream>>>(w2, ws, (u16*)d_out);
}
```

```cpp
#include <hip/hip_runtime.h>
#include <hip/hip_bf16.h>
#include <stdint.h>

typedef unsigned short u16;
typedef __bf16 bf16x8 __attribute__((ext_vector_type(8)));
typedef __bf16 bf16x2 __attribute__((ext_vector_type(2)));
typedef short s16x4 __attribute__((ext_vector_type(4)));
typedef short s16x8 __attribute__((ext_vector_type(8)));
typedef float f32x16 __attribute__((ext_vector_type(16)));
typedef float f32x4 __attribute__((ext_vector_type(4)));
typedef float f32x2 __attribute__((ext_vector_type(2)));
typedef unsigned u32x4 __attribute__((ext_vector_type(4)));
typedef unsigned u32x2 __attribute__((ext_vector_type(2)));
#define LDSP __attribute__((address_space(3)))
typedef LDSP char* lptr;
#define DI __device__ __forceinline__

constexpr int S_ = 2048, D_ = 1024, F_ = 3584;
constexpr size_t WS_MOD = 0;
constexpr size_t WS_UT = 32768;
constexpr size_t WS_KT = 65536;
constexpr size_t WS_VT = WS_KT + 131072;
constexpr size_t WS_ST = WS_VT + 131072;
constexpr size_t WS_MODP = WS_ST + 262144;
constexpr size_t WS_BUT = WS_MODP + 98304;
constexpr size_t WS_UTP = WS_BUT + 4096;
constexpr size_t WS_H = 1u << 20;
constexpr size_t WS_O = WS_H + (4u << 20);
constexpr size_t WS_X2 = WS_O + (4u << 20);
constexpr size_t WS_Y = WS_X2 + (4u << 20);
constexpr size_t WS_G = WS_Y + (4u << 20);
constexpr size_t WS_WQ = 32u << 20;
constexpr size_t WS_WO = WS_WQ + (2u << 20);
constexpr size_t WS_W1 = WS_WO + (2u << 20);
constexpr size_t WS_W2 = WS_W1 + (7u << 20);
static_assert(WS_UTP + 4 * 2 * 3584 * 4 <= WS_H, "");

constexpr size_t WS_DBG = 64u << 20;
#define STAMP(ws, kid, i) do {} while (0)
struct TS {};
#define TCAP(T, i) do {} while (0)
#define TDUMP(T, ws, kid, n) do {} while (0)
DI float bf2f(u16 v) { return __uint_as_float((unsigned)v << 16); }
DI float bflo(unsigned v) { return __uint_as_float(v << 16); }
DI float bfhi(unsigned v) { return __uint_as_float(v & 0xffff0000u); }
DI unsigned pk2(float a, float b) { f32x2 v = {a, b}; return __builtin_bit_cast(unsigned, __builtin_convertvector(v, bf16x2)); }
DI float rbf(float a) { return bflo(pk2(a, 0.f)); }
DI f32x16 mfma32(bf16x8 a, bf16x8 b, f32x16 c) { return __builtin_amdgcn_mfma_f32_32x32x16_bf16(a, b, c, 0, 0, 0); }
DI f32x4 mfma16(bf16x8 a, bf16x8 b, f32x4 c) { return __builtin_amdgcn_mfma_f32_16x16x32_bf16(a, b, c, 0, 0, 0); }
DI bf16x8 lds_b128(lptr p) { return *(const LDSP bf16x8*)p; }
DI s16x4 lds_tr(lptr p) { return __builtin_amdgcn_ds_read_tr16_b64_v4i16((LDSP s16x4*)p); }
DI bf16x8 cat(s16x4 a, s16x4 b) { return __builtin_bit_cast(bf16x8, __builtin_shufflevector(a, b, 0, 1, 2, 3, 4, 5, 6, 7)); }
#define WT 16
typedef __amdgpu_buffer_rsrc_t rsrc_t;
DI rsrc_t mk_rsrc(void* base) { return __builtin_amdgcn_make_buffer_rsrc(base, 0, 0x7fffffff, 0x00020000); }
DI void st16(rsrc_t r, unsigned voff, unsigned soff, u32x4 v) { __builtin_amdgcn_raw_buffer_store_b128(v, r, voff + soff, 0, WT); }
#define SBAR() __builtin_amdgcn_sched_barrier(0)
#define PIN(x) asm volatile("" : "+v"(x))
template <int N> DI void wait_vm() { asm volatile("s_waitcnt vmcnt(%0)" ::"i"(N) : "memory"); }
DI void bar() { asm volatile("s_barrier" ::: "memory"); }
DI void lgkm_bar() { asm volatile("s_waitcnt lgkmcnt(0)\n\ts_barrier" ::: "memory"); }
template <int C> DI float dpp_add(float v) { return v + __builtin_bit_cast(float, __builtin_amdgcn_update_dpp(0, __builtin_bit_cast(int, v), C, 0xf, 0xf, true)); }
DI float swap16_add(float v) { const auto r = __builtin_amdgcn_permlane16_swap(__float_as_uint(v), __float_as_uint(v), false, false); return __uint_as_float(r[0]) + __uint_as_float(r[1]); }
DI float swap32_add(float v) { const auto r = __builtin_amdgcn_permlane32_swap(__float_as_uint(v), __float_as_uint(v), false, false); return __uint_as_float(r[0]) + __uint_as_float(r[1]); }
DI float wave_sum(float v) {
    v = dpp_add<0xB1>(v); v = dpp_add<0x4E>(v); v = dpp_add<0x141>(v); v = dpp_add<0x140>(v);
    return swap32_add(swap16_add(v));
}

#define ABL 0
#define GLDS(v, s, off) "global_load_lds_dwordx4 %[" #v "], %[" #s "] offset:" #off "\n\t"
#define SETM0(m) "s_mov_b32 m0, %[" #m "]\n\ts_nop 0\n\t"

DI void tile_issue(const u16* src, int ld, unsigned ldsb, int lane) {
    const unsigned vo = (unsigned)((lane >> 3) * ld * 2 + (lane & 7) * 16);
#pragma unroll
    for (int i = 0; i < 4; ++i) {
        const u16* s = src + (size_t)i * 8 * ld;
        const unsigned m = ldsb + i * 1024;
        asm volatile(SETM0(m) GLDS(v, s, 0)::[m] "s"(m), [v] "v"(vo), [s] "s"(s) : "memory");
    }
}
template <bool PERM> DI void tile_store32(lptr st, rsrc_t r, unsigned dst, int lane) {
    const lptr p = st + ((PERM ? 4 : 8) * (lane >> 5) + ((lane & 15) >> 2)) * 128 + ((lane >> 4) & 1) * 32 + (lane & 3) * 8;
    bf16x8 f[4];
#pragma unroll
    for (int i = 0; i < 4; ++i) { const lptr q = p + (i >> 1) * 2048 + (i & 1) * 64; f[i] = cat(lds_tr(q), lds_tr(q + (PERM ? 1024 : 512))); }
#pragma unroll
    for (int i = 0; i < 4; ++i) st16(r, i * 1024 + lane * 16, dst, __builtin_bit_cast(u32x4, f[i]));
}

__global__ void __launch_bounds__(512) k_prep(const u16* __restrict__ c, const u16* __restrict__ wmod, const u16* __restrict__ wq, char* __restrict__ ws) {
    extern __shared__ __attribute__((aligned(16))) char smem[];
    const lptr lds = (lptr)smem;
    const int t = threadIdx.x, lane = t & 63, wid = __builtin_amdgcn_readfirstlane(t >> 6), b = blockIdx.x;
    if (wid == 0) STAMP(ws, 0, 0);
    if (b >= 192) {
        const int j = (b - 192) & 15, kg = (b - 192) >> 4;
        tile_issue(wq + (size_t)(kg * 256 + wid * 32) * D_ + j * 64, D_, (unsigned)(uintptr_t)lds + wid * 4096, lane);
        wait_vm<0>();
        tile_store32<false>(lds + wid * 4096, mk_rsrc(ws), WS_WQ + j * 131072 + (kg * 8 + wid) * 4096, lane);
        if (wid == 0) STAMP(ws, 0, 1);
        return;
    }
    const int cg = b % 48, kg = b / 48, ch = t & 15, rg = t >> 4;
    u32x4 w[8]; u16 cv[8];
#pragma unroll
    for (int i = 0; i < 8; ++i) w[i] = __builtin_nontemporal_load((const u32x4*)(wmod + (size_t)(kg * 256 + i * 32 + rg) * 6144 + cg * 128 + ch * 8));
#pragma unroll
    for (int i = 0; i < 8; ++i) cv[i] = c[kg * 256 + i * 32 + rg];
    float acc[8] = {0, 0, 0, 0, 0, 0, 0, 0};
#pragma unroll
    for (int i = 0; i < 8; ++i) {
        const float f = bf2f(cv[i]);
        const float s = rbf(f / (1.f + __expf(-f)));
#pragma unroll
        for (int j = 0; j < 4; ++j) { acc[2 * j] += s * bflo(w[i][j]); acc[2 * j + 1] += s * bfhi(w[i][j]); }
    }
#pragma unroll
    for (int j = 0; j < 8; ++j) acc[j] = swap32_add(swap16_add(acc[j]));
    if (lane < 16) {
        *(LDSP f32x4*)(lds + wid * 512 + ch * 32) = (f32x4){acc[0], acc[1], acc[2], acc[3]};
        *(LDSP f32x4*)(lds + wid * 512 + ch * 32 + 16) = (f32x4){acc[4], acc[5], acc[6], acc[7]};
    }
    lgkm_bar();
    if (t < 128) {
        float v = 0.f;
#pragma unroll
        for (int i = 0; i < 8; ++i) v += *(const LDSP float*)(lds + i * 512 + t * 4);
        ((float*)(ws + WS_MODP))[kg * 6144 + cg * 128 + t] = v;
    }
    if (wid == 0) STAMP(ws, 0, 1);
}

constexpr int K2_W = 32768, K2_MOD = K2_W + 65536, K2_RED = K2_MOD + 8192, K2_LDS = K2_RED + 16384;
__global__ void __launch_bounds__(512) k_ln_kv(const u16* __restrict__ x, const u16* __restrict__ wk, const u16* __restrict__ wv, char* __restrict__ ws) {
    extern __shared__ __attribute__((aligned(16))) char smem[];
    const lptr lds = (lptr)smem;
    const int t = threadIdx.x, lane = t & 63, wid = __builtin_amdgcn_readfirstlane(t >> 6);
    const int b = blockIdx.x, tg = ((b >> 4) << 3) | (b & 7), which = (b >> 3) & 1;
    const float* modp = (const float*)(ws + WS_MODP);
    if (wid == 0) STAMP(ws, 1, 0);
    u32x4 xr[2][2]; f32x4 mp[4];
    {
        const u16* xp = x + (size_t)(tg * 16 + wid * 2) * D_;
        const unsigned vx = lane * 16, vm = t * 16;
        asm volatile("global_load_dwordx4 %0, %8, %10\n\tglobal_load_dwordx4 %1, %8, %10 offset:1024\n\t"
                     "global_load_dwordx4 %2, %8, %10 offset:2048\n\tglobal_load_dwordx4 %3, %8, %10 offset:3072\n\t"
                     "global_load_dwordx4 %4, %9, %11\n\tglobal_load_dwordx4 %5, %9, %12\n\tglobal_load_dwordx4 %6, %9, %13\n\tglobal_load_dwordx4 %7, %9, %14"
                     : "=&v"(xr[0][0]), "=&v"(xr[0][1]), "=&v"(xr[1][0]), "=&v"(xr[1][1]), "=&v"(mp[0]), "=&v"(mp[1]), "=&v"(mp[2]), "=&v"(mp[3])
                     : "v"(vx), "v"(vm), "s"(xp), "s"(modp), "s"(modp + 6144), "s"(modp + 2 * 6144), "s"(modp + 3 * 6144) : "memory");
    }
    float mf[4] = {0, 0, 0, 0};
    if (t < 24) {
#pragma unroll
        for (int i = 0; i < 4; ++i) mf[i] = modp[i * 6144 + b * 24 + t];
    }
    {
        const u16* W = (which ? wv : wk) + wid * 4096;
        const int row = lane >> 2, chn = (lane & 3) ^ (((row >> 3) & 1) << 1);
        const unsigned vo = (unsigned)(row * 64 + chn * 16);
#pragma unroll
        for (int q = 0; q < 2; ++q) {
            const unsigned m = (unsigned)(uintptr_t)lds + K2_W + wid * 8192 + q * 4096;
            const u16* sb_ = W + q * 2048;
            asm volatile(SETM0(m) GLDS(v, s, 0) GLDS(v, s, 1024) GLDS(v, s, 2048) GLDS(v, s, 3072)::[m] "s"(m), [v] "v"(vo), [s] "s"(sb_) : "memory");
        }
    }
    asm volatile("s_waitcnt vmcnt(8)" : "+v"(xr[0][0]), "+v"(xr[0][1]), "+v"(xr[1][0]), "+v"(xr[1][1]), "+v"(mp[0]), "+v"(mp[1]), "+v"(mp[2]), "+v"(mp[3])::"memory");
    {
        f32x4 m = (mp[0] + mp[1]) + (mp[2] + mp[3]);
#pragma unroll
        for (int e = 0; e < 4; ++e) { m[e] = rbf(m[e]); if (t >= 256) m[e] = rbf(1.f + m[e]); }
        *(LDSP f32x4*)(lds + K2_MOD + t * 16) = m;
    }
    float v[2][16], rstd[2];
#pragma unroll
    for (int r = 0; r < 2; ++r) {
#pragma unroll
        for (int h = 0; h < 2; ++h)
#pragma unroll
            for (int j = 0; j < 4; ++j) { v[r][h * 8 + 2 * j] = bflo(xr[r][h][j]); v[r][h * 8 + 2 * j + 1] = bfhi(xr[r][h][j]); }
        float s = 0;
#pragma unroll
        for (int j = 0; j < 16; ++j) s += v[r][j];
        const float mean = wave_sum(s) * (1.f / 1024.f);
        float q = 0;
#pragma unroll
        for (int j = 0; j < 16; ++j) { v[r][j] -= mean; q += v[r][j] * v[r][j]; }
        rstd[r] = __builtin_amdgcn_rsqf(wave_sum(q) * (1.f / 1024.f) + 1e-5f);
    }
    if (wid == 0) STAMP(ws, 1, 1);
    lgkm_bar();
#pragma unroll
    for (int h = 0; h < 2; ++h) {
        f32x4 sa[2], a1[2];
#pragma unroll
        for (int q = 0; q < 2; ++q) {
            sa[q] = *(const LDSP f32x4*)(lds + K2_MOD + (h * 512 + lane * 8 + q * 4) * 4);
            a1[q] = *(const LDSP f32x4*)(lds + K2_MOD + 4096 + (h * 512 + lane * 8 + q * 4) * 4);
        }
#pragma unroll
        for (int r = 0; r < 2; ++r) {
            u32x4 o;
#pragma unroll
            for (int j = 0; j < 4; ++j) {
                const int e = 2 * j;
                o[j] = pk2(v[r][h * 8 + e] * rstd[r] * a1[e >> 2][e & 3] + sa[e >> 2][e & 3], v[r][h * 8 + e + 1] * rstd[r] * a1[e >> 2][(e & 3) + 1] + sa[e >> 2][(e & 3) + 1]);
            }
            const int kc = h * 64 + lane;
            *(LDSP u32x4*)(lds + kc * 256 + (((wid * 2 + r) ^ (kc & 7)) * 16)) = o;
        }
    }
    wait_vm<0>();
    lgkm_bar();
    if (wid == 0) STAMP(ws, 1, 2);
    if (t < 24) {
        const int col = b * 24 + t, part = col >> 10, k = col & 1023;
        float v = rbf((mf[0] + mf[1]) + (mf[2] + mf[3]));
        if (part == 1 || part == 4) v = rbf(1.f + v);
        ((float*)(ws + WS_MOD))[col] = v;
        if (part == 3 || part == 4)
            ((u16*)(ws + WS_BUT))[(((k >> 5) * 2 + (part == 3)) * 4 + 2 * ((k >> 4) & 1) + ((k >> 2) & 1)) * 8 + (k & 3) + 4 * ((k >> 3) & 1)] = (u16)(__float_as_uint(v) >> 16);
    }
    {
        const unsigned hAT = WS_H + (tg >> 3) * 262144 + (tg & 7) * 256;
#pragma unroll
        for (int i = 0; i < 2; ++i) {
            const int kc = which * 64 + i * 32 + (t >> 4), tk = t & 15;
            st16(mk_rsrc(ws), kc * 2048 + tk * 16, hAT, *(const LDSP u32x4*)(lds + kc * 256 + ((tk ^ (kc & 7)) * 16)));
        }
    }
    f32x4 acc[2] = {{0, 0, 0, 0}, {0, 0, 0, 0}};
    {
        const int g = lane >> 4, tk = lane & 15, q = (lane & 15) >> 2, p = lane & 3;
#pragma unroll
        for (int s = 0; s < 4; ++s) {
            const int kc = wid * 16 + s * 4 + g;
            const bf16x8 hf = lds_b128(lds + kc * 256 + ((tk ^ (kc & 7)) * 16));
            const int row = kc * 8 + q;
#pragma unroll
            for (int nb = 0; nb < 2; ++nb) {
                const lptr wp = lds + K2_W + row * 64 + ((nb ^ (kc & 1)) * 32) + p * 8;
                const bf16x8 wf = cat(lds_tr(wp), lds_tr(wp + 256));
                acc[nb] = which ? mfma16(hf, wf, acc[nb]) : mfma16(wf, hf, acc[nb]);
            }
        }
    }
#pragma unroll
    for (int nb = 0; nb < 2; ++nb) *(LDSP f32x4*)(lds + K2_RED + ((wid * 2 + nb) * 64 + lane) * 16) = acc[nb];
    lgkm_bar();
    if (t < 128) {
        const int nb = t >> 6;
        f32x4 r = *(const LDSP f32x4*)(lds + K2_RED + (nb * 64 + lane) * 16);
#pragma unroll
        for (int w = 1; w < 8; ++w) r += *(const LDSP f32x4*)(lds + K2_RED + ((w * 2 + nb) * 64 + lane) * 16);
        u32x2 o = {pk2(r[0], r[1]), pk2(r[2], r[3])};
        const int g = lane >> 4, c = lane & 15, kt = tg >> 1;
        if (which) {
            *(u32x2*)(ws + WS_VT + kt * 2048 + ((tg & 1) * 2 + (g & 1)) * 512 + (nb * 16 + c) * 16 + (g >> 1) * 8) = o;
        } else {
            *(u32x2*)(ws + WS_KT + kt * 2048 + (nb * 2 + (g & 1)) * 512 + ((tg & 1) * 16 + c) * 16 + (g >> 1) * 8) = o;
        }
    }
    if (wid == 0) STAMP(ws, 1, 3);
}

constexpr int ring_issued(int i, int NST, int NS) { int n = 2; for (int k = 0; k < i; ++k) { n += 2; if (n > k + NS) n = k + NS; if (n > NST) n = NST; } return n; }
constexpr int ring_ramp(int NST, int NS) { int i = 0; while (ring_issued(i, NST, NS) != i + NS - 1) ++i; return i; }
template <int I> DI void tcap(TS& T) { TCAP(T, I); }
template <int NST, int NS, int PER, class F>
DI void ring_loader(F issue, char* ws = nullptr, int kid = -1) {
    constexpr int R = ring_ramp(NST, NS);
    TS T; tcap<0>(T);
    issue(0); issue(1);
    tcap<1>(T);
    [&]<int... I>(std::integer_sequence<int, I...>) {
        ((wait_vm<PER*(ring_issued(I, NST, NS) - 1 - I)>(), tcap<2 + 2 * I>(T), bar(), tcap<3 + 2 * I>(T), issue(ring_issued(I, NST, NS) % NS),
          (ring_issued(I + 1, NST, NS) - ring_issued(I, NST, NS) > 1 ? issue((ring_issued(I, NST, NS) + 1) % NS) : (void)0)), ...);
    }(std::make_integer_sequence<int, R>{});
    int slot = (R + NS - 1) % NS;
    for (int i = R; i <= NST - NS; ++i) {
        wait_vm<PER*(NS - 2)>(); bar();
        issue(slot); slot = (slot + 1 == NS) ? 0 : slot + 1;
    }
    tcap<2 + 2 * R>(T);
    [&]<int... I>(std::integer_sequence<int, I...>) { ((wait_vm<PER*(NS - 2 - I)>(), bar(), tcap<3 + 2 * R + (I < 4 ? I : 4)>(T)), ...); }(std::make_integer_sequence<int, NS - 1>{});
    if (kid >= 0 && (threadIdx.x >> 6) == 4) TDUMP(T, ws, kid, 3 + 2 * R + (NS - 1 < 5 ? NS - 1 : 5));
}
constexpr int G64_STAGE = 24576;
template <int NST, int NS>
DI void g64_loader(const char* sa, const char* sw, unsigned ldsb, int lw, int lane, char* ws = nullptr, int kid = -1) {
    const unsigned va = lw * 4096 + lane * 16, vw = lw * 2048 + lane * 16;
    ring_loader<NST, NS, 6>([&](int slot) {
        const unsigned ma = ldsb + slot * G64_STAGE + lw * 4096, mw = ldsb + slot * G64_STAGE + 16384 + lw * 2048;
        if (ABL != 2) asm volatile(SETM0(ma) GLDS(va, sa, 0) GLDS(va, sa, 1024) GLDS(va, sa, 2048) GLDS(va, sa, 3072) SETM0(mw) GLDS(vw, sw, 0) GLDS(vw, sw, 1024)
                     ::[ma] "s"(ma), [mw] "s"(mw), [va] "v"(va), [vw] "v"(vw), [sa] "s"(sa), [sw] "s"(sw) : "memory");
        sa += 16384; sw += 8192;
    }, ws, kid);
}

struct G64Frag { bf16x8 a[2][2], w[2][2]; };
template <int NST, int NS>
DI void g64_compute(lptr lds, int cw, int lane, f32x16 (&acc)[2][2]) {
    const int th = cw & 1, kh = cw >> 1, hi = lane >> 5;
    const lptr pa = lds + (kh * 4 + hi) * 2048 + (th * 64 + (lane & 31)) * 16;
    const lptr pw = lds + 16384 + kh * 4096 + lane * 16;
#define G64_RA(F, so, s, tb) F.a[s][tb] = lds_b128(pa + (so) + (s) * 4096 + (tb) * 512)
#define G64_RW(F, so, s, nb) F.w[s][nb] = lds_b128(pw + (so) + (s) * 2048 + (nb) * 1024)
#define G64_MM(F, s, nb, tb) acc[nb][tb] = mfma32(F.w[s][nb], F.a[s][tb], acc[nb][tb])
#define G64_STEP(C, N, so, RD) do { if (ABL == 1) break; \
        G64_MM(C, 0, 0, 0); if (RD) { G64_RA(N, so, 0, 0); G64_RW(N, so, 0, 0); } SBAR(); \
        G64_MM(C, 0, 0, 1); if (RD) { G64_RA(N, so, 0, 1); G64_RW(N, so, 0, 1); } SBAR(); \
        G64_MM(C, 0, 1, 0); if (RD) { G64_RA(N, so, 1, 0); G64_RW(N, so, 1, 0); } SBAR(); \
        G64_MM(C, 0, 1, 1); if (RD) { G64_RA(N, so, 1, 1); G64_RW(N, so, 1, 1); } SBAR(); \
        G64_MM(C, 1, 0, 0); G64_MM(C, 1, 0, 1); G64_MM(C, 1, 1, 0); G64_MM(C, 1, 1, 1); SBAR(); } while (0)
    G64Frag A, B;
    bar();
    G64_RA(A, 0, 0, 0); G64_RW(A, 0, 0, 0); G64_RA(A, 0, 0, 1); G64_RW(A, 0, 0, 1);
    G64_RA(A, 0, 1, 0); G64_RW(A, 0, 1, 0); G64_RA(A, 0, 1, 1); G64_RW(A, 0, 1, 1);
    int so = 0;
    static_assert(NST % 2 == 0, "");
    for (int i = 0; i < NST - 2; i += 2) {
        so = (so + G64_STAGE == NS * G64_STAGE) ? 0 : so + G64_STAGE;
        lgkm_bar(); SBAR();
        G64_STEP(A, B, so, true);
        so = (so + G64_STAGE == NS * G64_STAGE) ? 0 : so + G64_STAGE;
        lgkm_bar(); SBAR();
        G64_STEP(B, A, so, true);
    }
    so = (so + G64_STAGE == NS * G64_STAGE) ? 0 : so + G64_STAGE;
    lgkm_bar(); SBAR();
    G64_STEP(A, B, so, true);
    G64_STEP(B, A, 0, false);
}
DI void g64_reduce_send(lptr scratch, int cw, int lane, const f32x16 (&acc)[2][2]) {
    const int kh = cw >> 1;
#pragma unroll
    for (int tb = 0; tb < 2; ++tb)
#pragma unroll
        for (int i = 0; i < 4; ++i) {
            const f32x16& a = kh ? acc[0][tb] : acc[1][tb];
            *(LDSP f32x4*)(scratch + ((cw * 2 + tb) * 4 + i) * 1024 + lane * 16) = (f32x4){a[4 * i], a[4 * i + 1], a[4 * i + 2], a[4 * i + 3]};
        }
}
DI void g64_reduce_recv(lptr scratch, int cw, int lane, const f32x16 (&acc)[2][2], f32x16 (&r)[2]) {
    const int kh = cw >> 1;
#pragma unroll
    for (int tb = 0; tb < 2; ++tb) {
        r[tb] = kh ? acc[1][tb] : acc[0][tb];
#pragma unroll
        for (int i = 0; i < 4; ++i) {
            const f32x4 v = *(const LDSP f32x4*)(scratch + (((cw ^ 2) * 2 + tb) * 4 + i) * 1024 + lane * 16);
#pragma unroll
            for (int j = 0; j < 4; ++j) r[tb][4 * i + j] += v[j];
        }
    }
}
DI void tile_map(int b, int& p, int& j) { const int x = b & 7, i = b >> 3; p = (x >> 1) * 4 + (i >> 3); j = (x & 1) * 8 + (i & 7); }

constexpr int QA_NS = 4;
constexpr int QA_KV0 = QA_NS * G64_STAGE;
constexpr int QA_LDS = QA_KV0 + 65536;
constexpr float QSCALE = 0.17677669529663687f * 1.4426950408889634f;

__global__ void __launch_bounds__(512) k_qattn(const u16* __restrict__ wo, const u16* __restrict__ w1, const u16* __restrict__ w2, char* __restrict__ ws) {
    extern __shared__ __attribute__((aligned(16))) char smem[];
    const lptr lds = (lptr)smem;
    const int t = threadIdx.x, lane = t & 63, wid = __builtin_amdgcn_readfirstlane(t >> 6);
    int p, j; tile_map(blockIdx.x, p, j);
    const unsigned ldsb = (unsigned)(uintptr_t)lds;
    const char* KT = ws + WS_KT; const char* VT = ws + WS_VT;
    const unsigned vkv = lane * 16;
    auto kv_piece = [&](int chunk, unsigned buf, int i) {
        const char* s = ((i & 4) ? VT : KT) + chunk * 32768 + wid * 4096 + (i & 3) * 1024;
        const unsigned m = ldsb + buf + (i >> 2) * 32768 + wid * 4096 + (i & 3) * 1024;
        asm volatile(SETM0(m) GLDS(v, s, 0)::[m] "s"(m), [v] "v"(vkv), [s] "s"(s) : "memory");
    };
    if (wid == 0) STAMP(ws, 2, 0);
    if (wid >= 4) {
        g64_loader<16, QA_NS>(ws + WS_H + (size_t)p * 262144, ws + WS_WQ + (size_t)j * 131072, ldsb, wid - 4, lane);
#pragma unroll
        for (int i = 0; i < 8; ++i) kv_piece(0, QA_KV0, i);
        bar();
        bar();
    } else {
#pragma unroll
        for (int i = 0; i < 8; ++i) kv_piece(0, QA_KV0, i);
        f32x16 acc[2][2] = {};
        g64_compute<16, QA_NS>(lds, wid, lane, acc);
        if (wid == 0) STAMP(ws, 2, 1);
        g64_reduce_send(lds, wid, lane, acc);
        lgkm_bar();
        f32x16 r[2];
        g64_reduce_recv(lds, wid, lane, acc, r);
#pragma unroll
        for (int tb = 0; tb < 2; ++tb)
#pragma unroll
            for (int s = 0; s < 2; ++s) {
                u32x4 o;
#pragma unroll
                for (int i = 0; i < 4; ++i) o[i] = pk2(r[tb][8 * s + 2 * i] * QSCALE, r[tb][8 * s + 2 * i + 1] * QSCALE);
                *(LDSP u32x4*)(lds + 32768 + ((wid * 2 + tb) * 2 + s) * 1024 + lane * 16) = o;
            }
        lgkm_bar();
    }
    const int b = blockIdx.x;
    const bool big = b < 224;
    const rsrc_t rws = mk_rsrc(ws);
    const lptr stg = lds + 65536 + wid * 4096;
    const int jA = b & 15, gA = big ? (b >> 4) : ((b - 224) >> 4);
    const int gB = big ? (b / 56) : 2 + ((b - 224) >> 4), cB = big ? (b % 56) : jA;
    bf16x8 but = {};
    if (big && (lane & 15) < 2) but = *(const bf16x8*)(ws + WS_BUT + (((gB * 8 + wid) * 2 + (lane & 15)) * 4 + (lane >> 4)) * 16);
    const int qg = wid & 3, kh = wid >> 2;
    bf16x8 qA0, qA1, qB0, qB1;
    {
        const lptr qp = lds + 32768 + qg * 4096 + lane * 16;
        qA0 = lds_b128(qp); qA1 = lds_b128(qp + 1024); qB0 = lds_b128(qp + 2048); qB1 = lds_b128(qp + 3072);
    }
    bf16x8 sel;
    {
        const unsigned on = ((lane & 15) == ((lane >> 4) & 1)) ? 0x3f803f80u : 0u;
        sel = __builtin_bit_cast(bf16x8, (u32x4){on, on, on, on});
    }
    f32x16 oA = {}, oB = {}, sA, sB;
    f32x4 lA = {}, lB = {};
    const f32x16 zero = {};
    u32x4 pA0, pA1, pB0, pB1;
    bf16x8 k0, k1, v0, v1, vp0, vp1;
    PIN(but);
    wait_vm<0>(); lgkm_bar();
    if (wid == 0) STAMP(ws, 2, 2);
#define EXP4(S, i) do { S[i] = __builtin_amdgcn_exp2f(S[i]); S[i + 1] = __builtin_amdgcn_exp2f(S[i + 1]); S[i + 2] = __builtin_amdgcn_exp2f(S[i + 2]); S[i + 3] = __builtin_amdgcn_exp2f(S[i + 3]); } while (0)
#define PK2(P, d, S, i) do { P[d] = pk2(S[i], S[i + 1]); P[d + 1] = pk2(S[i + 2], S[i + 3]); } while (0)
#define BF(x) __builtin_bit_cast(bf16x8, x)
#define HALF(SX, PX0, PX1, SY, KY0, KY1, QY0, QY1, OZ, LZ, VZ0, VZ1, PZ0, PZ1, DOY, DOZ, RD0, RD1) do { \
        if (DOZ) OZ = mfma32(VZ0, BF(PZ0), OZ); EXP4(SX, 0); RD0; PK2(PX0, 0, SX, 0); PIN(PX0); SBAR(); \
        if (DOY) SY = mfma32(KY0, QY0, zero); EXP4(SX, 4); if (DOZ) LZ = mfma16(sel, BF(PZ0), LZ); PK2(PX0, 2, SX, 4); PIN(PX0); SBAR(); \
        if (DOZ) OZ = mfma32(VZ1, BF(PZ1), OZ); EXP4(SX, 8); RD1; PK2(PX1, 0, SX, 8); PIN(PX1); SBAR(); \
        if (DOY) SY = mfma32(KY1, QY1, SY); EXP4(SX, 12); if (DOZ) LZ = mfma16(sel, BF(PZ1), LZ); PK2(PX1, 2, SX, 12); PIN(PX1); SBAR(); } while (0)
    unsigned buf = QA_KV0, nbuf = 0;
    const int lo = kh * 16384 + lane * 16;
    {
        const lptr kp = lds + buf + lo;
        k0 = lds_b128(kp); k1 = lds_b128(kp + 1024);
        sA = mfma32(k0, qA0, zero); sA = mfma32(k1, qA1, sA);
        pB0 = (u32x4){0, 0, 0, 0}; pB1 = pB0; vp0 = k0; vp1 = k1;
    }
    for (int c = 0; c < 4; ++c) {
        const lptr kp = lds + buf + lo;
#pragma unroll
        for (int it = 0; it < 8; ++it) {
            if (it == 4) {
                if (c == 1 || c == 2) wait_vm<8>();
                if (c == 0) tile_issue((big ? w2 : wo) + (size_t)(gA * 256 + wid * 32) * D_ + jA * 64, D_, ldsb + 65536 + wid * 4096, lane);
                if (c == 1) {
                    tile_store32<true>(stg, rws, (big ? WS_W2 + jA * 458752 : WS_WO + jA * 131072) + (gA * 8 + wid) * 4096, lane);
                    asm volatile("s_waitcnt lgkmcnt(0)" ::: "memory");
                    if (big) {
                        tile_issue(w1 + (size_t)(gB * 256 + wid * 32) * F_ + cB * 64, F_, ldsb + 65536 + wid * 4096, lane);
                    } else tile_issue(wo + (size_t)(gB * 256 + wid * 32) * D_ + cB * 64, D_, ldsb + 65536 + wid * 4096, lane);
                }
                if (c == 2) {
                    if (big) {
                        const int g = lane >> 4;
                        const lptr tp = stg + (16 * (g >> 1) + 4 * (g & 1) + ((lane & 15) >> 2)) * 128 + (lane & 3) * 8;
                        bf16x8 f[4];
#pragma unroll
                        for (int i = 0; i < 4; ++i) f[i] = cat(lds_tr(tp + i * 32), lds_tr(tp + i * 32 + 1024));
#pragma unroll
                        for (int i = 0; i < 4; ++i) {
                            const int nbk = cB * 4 + i, jt = nbk / 14;
                            st16(rws, lane * 16, WS_W1 + jt * 458752 + (gB * 8 + wid) * 14336 + (nbk - jt * 14) * 1024, __builtin_bit_cast(u32x4, f[i]));
                            const f32x4 ut = mfma16(f[i], but, (f32x4){0, 0, 0, 0});
                            if ((lane & 15) < 2) *(LDSP f32x4*)(stg + (lane & 15) * 256 + i * 64 + g * 16) = ut;
                        }
                    } else tile_store32<true>(stg, rws, WS_WO + cB * 131072 + (gB * 8 + wid) * 4096, lane);
                }
                SBAR();
            }
            if (c < 3 && it < 4) { kv_piece(c + 1, nbuf, 2 * it); kv_piece(c + 1, nbuf, 2 * it + 1); }
            HALF(sA, pA0, pA1, sB, k0, k1, qB0, qB1, oB, lB, vp0, vp1, pB0, pB1, true, true,
                 v0 = lds_b128(kp + 32768 + it * 2048), v1 = lds_b128(kp + 32768 + it * 2048 + 1024));
            if (it < 7) {
                k0 = lds_b128(kp + (it + 1) * 2048); k1 = lds_b128(kp + (it + 1) * 2048 + 1024);
                HALF(sB, pB0, pB1, sA, k0, k1, qA0, qA1, oA, lA, v0, v1, pA0, pA1, true, true, (void)0, (void)0);
            } else {
                if (c < 3) {
                    if (c == 1) wait_vm<8>(); else wait_vm<4>();
                    lgkm_bar();
                    const lptr kn = lds + nbuf + lo;
                    k0 = lds_b128(kn); k1 = lds_b128(kn + 1024);
                }
                HALF(sB, pB0, pB1, sA, k0, k1, qA0, qA1, oA, lA, v0, v1, pA0, pA1, true, true, (void)0, (void)0);
            }
            vp0 = v0; vp1 = v1;
        }
        const unsigned tmp = buf; buf = nbuf; nbuf = tmp;
    }
    oB = mfma32(vp0, BF(pB0), oB); oB = mfma32(vp1, BF(pB1), oB);
    lB = mfma16(sel, BF(pB0), lB); lB = mfma16(sel, BF(pB1), lB);
    if (wid == 0) STAMP(ws, 2, 3);
    lgkm_bar();
    if (big && t < 128) {
        float s = 0.f;
#pragma unroll
        for (int w = 0; w < 8; ++w) s += *(const LDSP float*)(lds + 65536 + w * 4096 + t * 4);
        ((float*)(ws + WS_UTP))[(gB * 2 + (t >> 6)) * F_ + cB * 64 + (t & 63)] = s;
    }
    {
        const f32x16& so = kh ? oA : oB;
        const f32x4& sl = kh ? lA : lB;
#pragma unroll
        for (int i = 0; i < 4; ++i) *(LDSP f32x4*)(lds + wid * 4096 + i * 1024 + lane * 16) = (f32x4){so[4 * i], so[4 * i + 1], so[4 * i + 2], so[4 * i + 3]};
        if (lane < 16) { *(LDSP float*)(lds + 32768 + wid * 128 + lane * 4) = sl[0]; *(LDSP float*)(lds + 32768 + wid * 128 + 64 + lane * 4) = sl[1]; }
    }
    lgkm_bar();
    f32x16 o = kh ? oB : oA;
    float l;
    {
        const f32x4 ml = kh ? lB : lA;
        const float own0 = __shfl(ml[0], lane & 15), own1 = __shfl(ml[1], lane & 15);
        l = ((lane & 16) ? own1 : own0) + *(const LDSP float*)(lds + 32768 + (wid ^ 4) * 128 + (lane & 31) * 4);
#pragma unroll
        for (int i = 0; i < 4; ++i) {
            const f32x4 v = *(const LDSP f32x4*)(lds + (wid ^ 4) * 4096 + i * 1024 + lane * 16);
#pragma unroll
            for (int e = 0; e < 4; ++e) o[4 * i + e] += v[e];
        }
    }
    float chk = l * 0.f;
#pragma unroll
    for (int i = 0; i < 16; ++i) chk = __builtin_fmaf(o[i], 0.f, chk);
    const bool bad = !(chk == 0.f) || !(l >= 0x1p-90f);
    if (__builtin_expect(__any(bad), 0)) {
        const bf16x8 q0 = kh ? qB0 : qA0, q1 = kh ? qB1 : qA1;
        float m = -INFINITY; l = 0.f; o = zero;
        for (int kt = 0; kt < 64; ++kt) {
            const bf16x8 a0 = *(const bf16x8*)(KT + kt * 2048 + lane * 16), a1 = *(const bf16x8*)(KT + kt * 2048 + 1024 + lane * 16);
            const bf16x8 b0 = *(const bf16x8*)(VT + kt * 2048 + lane * 16), b1 = *(const bf16x8*)(VT + kt * 2048 + 1024 + lane * 16);
            f32x16 s = mfma32(a0, q0, zero); s = mfma32(a1, q1, s);
            float mx = s[0];
#pragma unroll
            for (int i = 1; i < 16; ++i) mx = fmaxf(mx, s[i]);
            mx = fmaxf(mx, __shfl_xor(mx, 32));
            const float mn = fmaxf(m, mx), al = __builtin_amdgcn_exp2f(m - mn);
            m = mn;
            u32x4 p0, p1; float rs = 0.f;
#pragma unroll
            for (int i = 0; i < 4; ++i) {
                p0[i] = pk2(__builtin_amdgcn_exp2f(s[2 * i] - m), __builtin_amdgcn_exp2f(s[2 * i + 1] - m));
                p1[i] = pk2(__builtin_amdgcn_exp2f(s[8 + 2 * i] - m), __builtin_amdgcn_exp2f(s[9 + 2 * i] - m));
                rs += bflo(p0[i]) + bfhi(p0[i]) + bflo(p1[i]) + bfhi(p1[i]);
            }
            rs += __shfl_xor(rs, 32);
            l = l * al + rs;
#pragma unroll
            for (int i = 0; i < 16; ++i) o[i] *= al;
            o = mfma32(b0, BF(p0), o); o = mfma32(b1, BF(p1), o);
        }
    }
    {
        const float inv = 1.f / l;
        const unsigned oAT = WS_O + p * 262144 + (2 * j + (qg >> 1)) * 8192 + ((qg & 1) * 2 + kh) * 512, vo = (lane >> 5) * 2048 + (lane & 31) * 16;
#pragma unroll
        for (int s = 0; s < 2; ++s) {
            u32x4 w;
#pragma unroll
            for (int i = 0; i < 4; ++i) w[i] = pk2(o[8 * s + 2 * i] * inv, o[8 * s + 2 * i + 1] * inv);
            st16(rws, vo, oAT + s * 4096, w);
        }
    }
    if (wid == 0) STAMP(ws, 2, 4);
}

constexpr int OP_NS = 6;
__global__ void __launch_bounds__(512) k_oproj(const u16* __restrict__ x, char* __restrict__ ws) {
    extern __shared__ __attribute__((aligned(16))) char smem[];
    const lptr lds = (lptr)smem;
    const int t = threadIdx.x, lane = t & 63, wid = __builtin_amdgcn_readfirstlane(t >> 6);
    int p, j; tile_map(blockIdx.x, p, j);
    if (wid == 0) STAMP(ws, 3, 0);
    if (wid >= 4) {
        g64_loader<16, OP_NS>(ws + WS_O + (size_t)p * 262144, ws + WS_WO + (size_t)j * 131072, (unsigned)(uintptr_t)lds, wid - 4, lane, ws, 6);
        bar(); bar();
        if (wid == 4 && lane < 28) {
            const float* up = (const float*)(ws + WS_UTP) + blockIdx.x * 28 + lane;
            ((float*)(ws + WS_UT))[blockIdx.x * 28 + lane] = (up[0] + up[2 * F_]) + (up[4 * F_] + up[6 * F_]);
        }
        return;
    }
    const int th = wid & 1, nb = wid >> 1, hi = lane >> 5;
    const int n0 = j * 64 + nb * 32 + 4 * hi;
    const float* mod = (const float*)(ws + WS_MOD);
    u32x4 xq[2][2]; f32x4 ga[4], a2[4];
#pragma unroll
    for (int tb = 0; tb < 2; ++tb)
#pragma unroll
        for (int h = 0; h < 2; ++h) xq[tb][h] = *(const u32x4*)(x + (size_t)(p * 128 + th * 64 + tb * 32 + (lane & 31)) * D_ + j * 64 + nb * 32 + h * 16 + hi * 8);
#pragma unroll
    for (int i = 0; i < 4; ++i) {
        ga[i] = *(const f32x4*)(mod + 2048 + n0 + 8 * i);
        a2[i] = *(const f32x4*)(mod + 4096 + n0 + 8 * i);
    }
    f32x16 acc[2][2] = {};
    if (wid == 0) STAMP(ws, 3, 1);
    g64_compute<16, OP_NS>(lds, wid, lane, acc);
    if (wid == 0) STAMP(ws, 3, 2);
    g64_reduce_send(lds, wid, lane, acc);
    lgkm_bar();
    f32x16 r[2];
    g64_reduce_recv(lds, wid, lane, acc, r);
    f32x2 st[2];
#pragma unroll
    for (int tb = 0; tb < 2; ++tb) {
        u32x4 xo[2], yo[2]; float v[16]; float s = 0.f;
        u32x2 xr[2][4];
#pragma unroll
        for (int h = 0; h < 2; ++h) {
            const auto s0 = __builtin_amdgcn_permlane32_swap(xq[tb][h][0], xq[tb][h][2], false, false);
            const auto s1 = __builtin_amdgcn_permlane32_swap(xq[tb][h][1], xq[tb][h][3], false, false);
            xr[tb][2 * h] = (u32x2){s0[0], s1[0]}; xr[tb][2 * h + 1] = (u32x2){s0[1], s1[1]};
        }
#pragma unroll
        for (int i = 0; i < 4; ++i) {
            const float x0 = bflo(xr[tb][i][0]), x1 = bfhi(xr[tb][i][0]), x2 = bflo(xr[tb][i][1]), x3 = bfhi(xr[tb][i][1]);
            const unsigned a = pk2(x0 + ga[i][0] * r[tb][4 * i], x1 + ga[i][1] * r[tb][4 * i + 1]);
            const unsigned b = pk2(x2 + ga[i][2] * r[tb][4 * i + 2], x3 + ga[i][3] * r[tb][4 * i + 3]);
            xo[i >> 1][(i & 1) * 2] = a; xo[i >> 1][(i & 1) * 2 + 1] = b;
            v[4 * i] = bflo(a); v[4 * i + 1] = bfhi(a); v[4 * i + 2] = bflo(b); v[4 * i + 3] = bfhi(b);
            yo[i >> 1][(i & 1) * 2] = pk2(v[4 * i] * a2[i][0], v[4 * i + 1] * a2[i][1]);
            yo[i >> 1][(i & 1) * 2 + 1] = pk2(v[4 * i + 2] * a2[i][2], v[4 * i + 3] * a2[i][3]);
            s += (v[4 * i] + v[4 * i + 1]) + (v[4 * i + 2] + v[4 * i + 3]);
        }
        const unsigned off = p * 262144 + (j * 2 + nb) * 8192 + (th * 64 + tb * 32) * 16, vo = hi * 2048 + (lane & 31) * 16;
#pragma unroll
        for (int h = 0; h < 2; ++h) { st16(mk_rsrc(ws), vo, WS_Y + off + h * 4096, yo[h]); st16(mk_rsrc(ws), vo, WS_X2 + off + h * 4096, xo[h]); }
        s += __shfl_xor(s, 32);
        const float mean = s * (1.f / 32.f);
        float q = 0.f;
#pragma unroll
        for (int i = 0; i < 16; ++i) { const float d = v[i] - mean; q += d * d; }
        q += __shfl_xor(q, 32);
        st[tb] = (f32x2){mean, q};
    }
    if (nb == 1 && lane < 32) { *(LDSP f32x2*)(lds + 65536 + (th * 64 + lane) * 8) = st[0]; *(LDSP f32x2*)(lds + 65536 + (th * 64 + 32 + lane) * 8) = st[1]; }
    lgkm_bar();
    if (nb == 0) {
        const f32x2 a = hi ? st[1] : st[0];
        const f32x2 b = *(const LDSP f32x2*)(lds + 65536 + (th * 64 + lane) * 8);
        const float d = a[0] - b[0];
        *(f32x2*)(ws + WS_ST + ((size_t)j * S_ + p * 128 + th * 64 + lane) * 8) = (f32x2){0.5f * (a[0] + b[0]), a[1] + b[1] + 16.f * d * d};
    }
    if (wid == 0) STAMP(ws, 3, 3);
}

constexpr int F1_NS = 7, F1_STAGE = 22528, F1_NST = 32;
template <int NW>
DI void f1_loader(const char* sa, const char* sw, unsigned ldsb, int lw, int lane) {
    const int pc0 = lw < 2 ? lw * 4 : 8 + (lw - 2) * 3;
    const unsigned va = lw * 2048 + lane * 16, vw = pc0 * 1024 + lane * 16;
    ring_loader<F1_NST, F1_NS, NW + 2>([&](int slot) {
        const unsigned ma = ldsb + slot * F1_STAGE + lw * 2048, mw = ldsb + slot * F1_STAGE + 8192 + pc0 * 1024;
        if constexpr (ABL == 2) {} else if constexpr (NW == 4)
            asm volatile(SETM0(ma) GLDS(va, sa, 0) GLDS(va, sa, 1024) SETM0(mw) GLDS(vw, sw, 0) GLDS(vw, sw, 1024) GLDS(vw, sw, 2048) GLDS(vw, sw, 3072)
                         ::[ma] "s"(ma), [mw] "s"(mw), [va] "v"(va), [vw] "v"(vw), [sa] "s"(sa), [sw] "s"(sw) : "memory");
        else
            asm volatile(SETM0(ma) GLDS(va, sa, 0) GLDS(va, sa, 1024) SETM0(mw) GLDS(vw, sw, 0) GLDS(vw, sw, 1024) GLDS(vw, sw, 2048)
                         ::[ma] "s"(ma), [mw] "s"(mw), [va] "v"(va), [vw] "v"(vw), [sa] "s"(sa), [sw] "s"(sw) : "memory");
        sa += 8192; sw += 14336;
    });
}
struct F1Frag { bf16x8 a[4], w[7]; };
__global__ void __launch_bounds__(512) k_ffn1(char* __restrict__ ws) {
    extern __shared__ __attribute__((aligned(16))) char smem[];
    const lptr lds = (lptr)smem;
    const int t = threadIdx.x, lane = t & 63, wid = __builtin_amdgcn_readfirstlane(t >> 6);
    int p, j; tile_map(blockIdx.x, p, j);
    if (wid == 0) STAMP(ws, 4, 0);
    if (wid >= 4) {
        const char* act = ws + WS_Y + (size_t)p * 262144;
        const char* w = ws + WS_W1 + (size_t)j * 458752;
        if (wid < 6) f1_loader<4>(act, w, (unsigned)(uintptr_t)lds, wid - 4, lane);
        else f1_loader<3>(act, w, (unsigned)(uintptr_t)lds, wid - 4, lane);
        return;
    }
    const int th = wid & 1, nh = wid >> 1, g = lane >> 4;
    f32x4 acc[7][4];
    float sg[4], rl[4];
    {
        f32x2 sp[16];
#pragma unroll
        for (int i = 0; i < 16; ++i) sp[i] = *(const f32x2*)(ws + WS_ST + ((size_t)i * S_ + p * 128 + th * 64 + lane) * 8);
        const float* UT = (const float*)(ws + WS_UT) + j * 224 + nh * 112 + 4 * g;
        f32x4 u[7], tt[7];
#pragma unroll
        for (int b = 0; b < 7; ++b) { u[b] = *(const f32x4*)(UT + b * 16); tt[b] = *(const f32x4*)(UT + F_ + b * 16); }
        float s = 0.f;
#pragma unroll
        for (int i = 0; i < 16; ++i) s += sp[i][0];
        const float mu = s * (1.f / 16.f);
        float q = 0.f;
#pragma unroll
        for (int i = 0; i < 16; ++i) { const float d = sp[i][0] - mu; q += sp[i][1] + 64.f * d * d; }
        const float sigma = __builtin_sqrtf(q * (1.f / 1024.f) + 1e-5f);
#pragma unroll
        for (int tb = 0; tb < 4; ++tb) {
            const float m = __shfl(mu, tb * 16 + (lane & 15));
            sg[tb] = __shfl(sigma, tb * 16 + (lane & 15));
            rl[tb] = -1.4426950408889634f / sg[tb];
#pragma unroll
            for (int b = 0; b < 7; ++b) acc[b][tb] = tt[b] * sg[tb] - u[b] * m;
        }
    }
    const lptr pa = lds + g * 2048 + (th * 64 + (lane & 15)) * 16;
    const lptr pw = lds + 8192 + nh * 7168 + lane * 16;
#define F1_RA(F, so, tb) F.a[tb] = lds_b128(pa + (so) + (tb) * 256)
#define F1_RW(F, so, b) F.w[b] = lds_b128(pw + (so) + (b) * 1024)
#define F1_MM(F, b, tb) acc[b][tb] = mfma16(F.w[b], F.a[tb], acc[b][tb])
#define F1_ROW(C, RD, b, E0, E1) do { F1_MM(C, b, 0); if (RD) { E0; } SBAR(); F1_MM(C, b, 1); SBAR(); F1_MM(C, b, 2); if (RD) { E1; } SBAR(); F1_MM(C, b, 3); SBAR(); } while (0)
#define F1_STEP(C, N, so, RD) do { if (ABL == 1) break; \
        F1_ROW(C, RD, 0, F1_RA(N, so, 0), F1_RW(N, so, 0)); F1_ROW(C, RD, 1, F1_RA(N, so, 1), F1_RA(N, so, 2)); F1_ROW(C, RD, 2, F1_RA(N, so, 3), F1_RW(N, so, 1)); \
        F1_ROW(C, RD, 3, F1_RW(N, so, 2), F1_RW(N, so, 3)); F1_ROW(C, RD, 4, F1_RW(N, so, 4), F1_RW(N, so, 5)); F1_ROW(C, RD, 5, F1_RW(N, so, 6), (void)0); \
        F1_ROW(C, false, 6, (void)0, (void)0); } while (0)
    F1Frag A, B;
    if (wid == 0) STAMP(ws, 4, 1);
    bar();
    if (wid == 0) STAMP(ws, 4, 2);
#pragma unroll
    for (int tb = 0; tb < 4; ++tb) F1_RA(A, 0, tb);
    F1_RW(A, 0, 0); F1_RW(A, 0, 1); F1_RW(A, 0, 2); F1_RW(A, 0, 3); F1_RW(A, 0, 4); F1_RW(A, 0, 5); F1_RW(A, 0, 6);
    int so = 0;
    for (int i = 0; i < F1_NST - 2; i += 2) {
        so = (so + F1_STAGE == F1_NS * F1_STAGE) ? 0 : so + F1_STAGE;
        lgkm_bar(); SBAR();
        F1_STEP(A, B, so, true);
        so = (so + F1_STAGE == F1_NS * F1_STAGE) ? 0 : so + F1_STAGE;
        lgkm_bar(); SBAR();
        F1_STEP(B, A, so, true);
    }
    so = (so + F1_STAGE == F1_NS * F1_STAGE) ? 0 : so + F1_STAGE;
    lgkm_bar(); SBAR();
    F1_STEP(A, B, so, true);
    F1_STEP(B, A, 0, false);
    if (wid == 0) STAMP(ws, 4, 3);
    const unsigned gp = WS_G + p * (F_ / 8 * 2048) + (j * 14 + nh * 7) * 4096 + th * 1024, vo = (g & 1) * 2048 + ((lane >> 5) * 16 + (lane & 15)) * 16;
#pragma unroll
    for (int b = 0; b < 7; ++b) {
        unsigned w[4][2];
#pragma unroll
        for (int tb = 0; tb < 4; ++tb)
#pragma unroll
            for (int e = 0; e < 2; ++e) {
                const f32x2 a = {acc[b][tb][2 * e], acc[b][tb][2 * e + 1]};
                const f32x2 zl = a * rl[tb];
                const f32x2 ex = {__builtin_amdgcn_exp2f(zl[0]), __builtin_amdgcn_exp2f(zl[1])};
                const f32x2 dn = __builtin_elementwise_fma(ex, (f32x2){sg[tb], sg[tb]}, (f32x2){sg[tb], sg[tb]});
                const f32x2 o = a * (f32x2){__builtin_amdgcn_rcpf(dn[0]), __builtin_amdgcn_rcpf(dn[1])};
                w[tb][e] = pk2(o[0], o[1]);
            }
#pragma unroll
        for (int tp = 0; tp < 2; ++tp) {
            const auto s0 = __builtin_amdgcn_permlane32_swap(w[2 * tp][0], w[2 * tp + 1][0], false, false);
            const auto s1 = __builtin_amdgcn_permlane32_swap(w[2 * tp][1], w[2 * tp + 1][1], false, false);
            st16(mk_rsrc(ws), vo + tp * 512, gp + b * 4096, (u32x4){s0[0], s1[0], s0[1], s1[1]});
        }
    }
    if (wid == 0) STAMP(ws, 4, 4);
}

constexpr int F2_NS = 6;
__global__ void __launch_bounds__(512) k_ffn2(char* __restrict__ ws, u16* __restrict__ out) {
    extern __shared__ __attribute__((aligned(16))) char smem[];
    const lptr lds = (lptr)smem;
    const int t = threadIdx.x, lane = t & 63, wid = __builtin_amdgcn_readfirstlane(t >> 6);
    int p, j; tile_map(blockIdx.x, p, j);
    if (wid == 0) STAMP(ws, 5, 0);
    if (wid >= 4) {
        g64_loader<56, F2_NS>(ws + WS_G + (size_t)p * (F_ / 8 * 2048), ws + WS_W2 + (size_t)j * 458752, (unsigned)(uintptr_t)lds, wid - 4, lane, ws, 7);
        bar();
        return;
    }
    const int th = wid & 1, nb = wid >> 1, hi = lane >> 5;
    const float* mod = (const float*)(ws + WS_MOD);
    u32x4 xr[2][2]; f32x4 gb[4];
#pragma unroll
    for (int tb = 0; tb < 2; ++tb)
#pragma unroll
        for (int h = 0; h < 2; ++h)
            xr[tb][h] = *(const u32x4*)(ws + WS_X2 + (size_t)p * 262144 + (size_t)((j * 2 + nb) * 4 + hi) * 2048 + (th * 64 + tb * 32 + (lane & 31)) * 16 + h * 4096);
#pragma unroll
    for (int i = 0; i < 4; ++i) gb[i] = *(const f32x4*)(mod + 5120 + j * 64 + nb * 32 + 4 * hi + 8 * i);
    f32x16 acc[2][2] = {};
    if (wid == 0) STAMP(ws, 5, 1);
    g64_compute<56, F2_NS>(lds, wid, lane, acc);
    if (wid == 0) STAMP(ws, 5, 2);
    g64_reduce_send(lds + 2 * G64_STAGE, wid, lane, acc);
    lgkm_bar();
    f32x16 r[2];
    g64_reduce_recv(lds + 2 * G64_STAGE, wid, lane, acc, r);
#pragma unroll
    for (int tb = 0; tb < 2; ++tb) {
        unsigned w[8];
#pragma unroll
        for (int i = 0; i < 4; ++i) {
            const unsigned a = xr[tb][i >> 1][(i & 1) * 2], b = xr[tb][i >> 1][(i & 1) * 2 + 1];
            w[2 * i] = pk2(bflo(a) + gb[i][0] * r[tb][4 * i], bfhi(a) + gb[i][1] * r[tb][4 * i + 1]);
            w[2 * i + 1] = pk2(bflo(b) + gb[i][2] * r[tb][4 * i + 2], bfhi(b) + gb[i][3] * r[tb][4 * i + 3]);
        }
        const unsigned op = ((p * 128 + th * 64 + tb * 32) * D_ + j * 64 + nb * 32) * 2, vo = (lane & 31) * (D_ * 2) + hi * 16;
#pragma unroll
        for (int h = 0; h < 2; ++h) {
            const auto s0 = __builtin_amdgcn_permlane32_swap(w[4 * h], w[4 * h + 2], false, false);
            const auto s1 = __builtin_amdgcn_permlane32_swap(w[4 * h + 1], w[4 * h + 3], false, false);
            st16(mk_rsrc(out), vo + h * 32, op, (u32x4){s0[0], s1[0], s0[1], s1[1]});
        }
    }
    if (wid == 0) STAMP(ws, 5, 3);
}

extern "C" void kernel_launch(void* const* d_in, const int* in_sizes, int n_in, void* d_out, int out_size, void* d_ws, size_t ws_size,
                              hipStream_t stream) {
    const u16* x = (const u16*)d_in[0];
    const u16* wq = (const u16*)d_in[1];
    const u16* wk = (const u16*)d_in[2];
    const u16* wv = (const u16*)d_in[3];
    const u16* wo = (const u16*)d_in[4];
    const u16* w1 = (const u16*)d_in[6];
    const u16* w2 = (const u16*)d_in[7];
    const u16* c = (const u16*)d_in[8];
    const u16* wmod = (const u16*)d_in[9];
    char* ws = (char*)d_ws;
    (void)hipFuncSetAttribute((const void*)k_ln_kv, hipFuncAttributeMaxDynamicSharedMemorySize, K2_LDS);
    (void)hipFuncSetAttribute((const void*)k_qattn, hipFuncAttributeMaxDynamicSharedMemorySize, QA_LDS);
    (void)hipFuncSetAttribute((const void*)k_oproj, hipFuncAttributeMaxDynamicSharedMemorySize, OP_NS * G64_STAGE);
    (void)hipFuncSetAttribute((const void*)k_ffn1, hipFuncAttributeMaxDynamicSharedMemorySize, F1_NS * F1_STAGE);
    (void)hipFuncSetAttribute((const void*)k_ffn2, hipFuncAttributeMaxDynamicSharedMemorySize, F2_NS * G64_STAGE);
    k_prep<<<256, 512, 32768, stream>>>(c, wmod, wq, ws);
    k_ln_kv<<<256, 512, K2_LDS, stream>>>(x, wk, wv, ws);
    k_qattn<<<256, 512, QA_LDS, stream>>>(wo, w1, w2, ws);
    k_oproj<<<256, 512, OP_NS * G64_STAGE, stream>>>(x, ws);
    k_ffn1<<<256, 512, F1_NS * F1_STAGE, stream>>>(ws);
    k_ffn2<<<256, 512, F2_NS * G64_STAGE, stream>>>(ws, (u16*)d_out);
}
```

```cpp
#pragma clang diagnostic ignored "-Wc++20-extensions"
#include <hip/hip_runtime.h>
#include <hip/hip_bf16.h>
#include <stdint.h>

typedef unsigned short u16;
typedef __bf16 bf16x8 __attribute__((ext_vector_type(8)));
typedef __bf16 bf16x2 __attribute__((ext_vector_type(2)));
typedef short s16x4 __attribute__((ext_vector_type(4)));
typedef short s16x8 __attribute__((ext_vector_type(8)));
typedef float f32x16 __attribute__((ext_vector_type(16)));
typedef float f32x4 __attribute__((ext_vector_type(4)));
typedef float f32x2 __attribute__((ext_vector_type(2)));
typedef unsigned u32x4 __attribute__((ext_vector_type(4)));
typedef unsigned u32x2 __attribute__((ext_vector_type(2)));
#define LDSP __attribute__((address_space(3)))
typedef LDSP char* lptr;
#define DI __device__ __forceinline__

constexpr int S_ = 2048, D_ = 1024, F_ = 3584;
constexpr size_t WS_MOD = 0;
constexpr size_t WS_UT = 32768;
constexpr size_t WS_KT = 65536;
constexpr size_t WS_VT = WS_KT + 131072;
constexpr size_t WS_ST = WS_VT + 131072;
constexpr size_t WS_MODP = WS_ST + 262144;
constexpr size_t WS_BUT = WS_MODP + 98304;
constexpr size_t WS_UTP = WS_BUT + 4096;
constexpr size_t WS_H = 1u << 20;
constexpr size_t WS_O = WS_H + (4u << 20);
constexpr size_t WS_X2 = WS_O + (4u << 20);
constexpr size_t WS_Y = WS_X2 + (4u << 20);
constexpr size_t WS_G = WS_Y + (4u << 20);
constexpr size_t WS_WQ = 32u << 20;
constexpr size_t WS_WO = WS_WQ + (2u << 20);
constexpr size_t WS_W1 = WS_WO + (2u << 20);
constexpr size_t WS_W2 = WS_W1 + (7u << 20);
constexpr size_t WS_FLAG = WS_UTP + 131072;
constexpr size_t WS_EX = 52u << 20;
static_assert(WS_FLAG + 32768 <= WS_H, "");
static_assert(WS_UTP + 4 * 2 * 3584 * 4 <= WS_H, "");

constexpr size_t WS_DBG = 64u << 20;
#define STAMP(ws, kid, i) do {} while (0)
struct TS {};
#define TCAP(T, i) do {} while (0)
#define TDUMP(T, ws, kid, n) do {} while (0)
DI float bf2f(u16 v) { return __uint_as_float((unsigned)v << 16); }
DI float bflo(unsigned v) { return __uint_as_float(v << 16); }
DI float bfhi(unsigned v) { return __uint_as_float(v & 0xffff0000u); }
DI unsigned pk2(float a, float b) { f32x2 v = {a, b}; return __builtin_bit_cast(unsigned, __builtin_convertvector(v, bf16x2)); }
DI float rbf(float a) { return bflo(pk2(a, 0.f)); }
DI f32x16 mfma32(bf16x8 a, bf16x8 b, f32x16 c) { return __builtin_amdgcn_mfma_f32_32x32x16_bf16(a, b, c, 0, 0, 0); }
DI f32x4 mfma16(bf16x8 a, bf16x8 b, f32x4 c) { return __builtin_amdgcn_mfma_f32_16x16x32_bf16(a, b, c, 0, 0, 0); }
DI bf16x8 lds_b128(lptr p) { return *(const LDSP bf16x8*)p; }
DI s16x4 lds_tr(lptr p) { return __builtin_amdgcn_ds_read_tr16_b64_v4i16((LDSP s16x4*)p); }
DI bf16x8 cat(s16x4 a, s16x4 b) { return __builtin_bit_cast(bf16x8, __builtin_shufflevector(a, b, 0, 1, 2, 3, 4, 5, 6, 7)); }
#define WT 16
typedef __amdgpu_buffer_rsrc_t rsrc_t;
DI rsrc_t mk_rsrc(void* base) { return __builtin_amdgcn_make_buffer_rsrc(base, 0, 0x7fffffff, 0x00020000); }
template <int POL = WT> DI void st16(rsrc_t r, unsigned voff, unsigned soff, u32x4 v) { __builtin_amdgcn_raw_buffer_store_b128(v, r, voff + soff, 0, POL); }
struct LP { lptr b[3]; };
DI LP mk_lp(lptr p) { LP r = {{p, p + 65536, p + 131072}}; asm volatile("" : "+v"(r.b[0]), "+v"(r.b[1]), "+v"(r.b[2])); return r; }
template <int OFF> DI bf16x8 lds_at(const LP& p) { return *(const LDSP bf16x8*)(p.b[OFF >> 16] + (OFF & 65535)); }
#define SBAR() __builtin_amdgcn_sched_barrier(0)
#define PIN(x) asm volatile("" : "+v"(x))
template <int N> DI void wait_vm() { asm volatile("s_waitcnt vmcnt(%0)" ::"i"(N) : "memory"); }
DI void bar() { asm volatile("s_barrier" ::: "memory"); }
DI void lgkm_bar() { asm volatile("s_waitcnt lgkmcnt(0)\n\ts_barrier" ::: "memory"); }
template <int C> DI float dpp_add(float v) { return v + __builtin_bit_cast(float, __builtin_amdgcn_update_dpp(0, __builtin_bit_cast(int, v), C, 0xf, 0xf, true)); }
DI float swap16_add(float v) { const auto r = __builtin_amdgcn_permlane16_swap(__float_as_uint(v), __float_as_uint(v), false, false); return __uint_as_float(r[0]) + __uint_as_float(r[1]); }
DI float swap32_add(float v) { const auto r = __builtin_amdgcn_permlane32_swap(__float_as_uint(v), __float_as_uint(v), false, false); return __uint_as_float(r[0]) + __uint_as_float(r[1]); }
DI float wave_sum(float v) {
    v = dpp_add<0xB1>(v); v = dpp_add<0x4E>(v); v = dpp_add<0x141>(v); v = dpp_add<0x140>(v);
    return swap32_add(swap16_add(v));
}

#define ABL 0
#define AA 0
#define PRIO 1
#define GLDS(v, s, off) "global_load_lds_dwordx4 %[" #v "], %[" #s "] offset:" #off "\n\t"
#define SETM0(m) "s_mov_b32 m0, %[" #m "]\n\ts_nop 0\n\t"

DI void tile_issue(const u16* src, int ld, unsigned ldsb, int lane) {
    const unsigned vo = (unsigned)((lane >> 3) * ld * 2 + (lane & 7) * 16);
#pragma unroll
    for (int i = 0; i < 4; ++i) {
        const u16* s = src + (size_t)i * 8 * ld;
        const unsigned m = ldsb + i * 1024;
        asm volatile(SETM0(m) GLDS(v, s, 0)::[m] "s"(m), [v] "v"(vo), [s] "s"(s) : "memory");
    }
}
template <bool PERM> DI void tile_store32(lptr st, rsrc_t r, unsigned dst, int lane) {
    const lptr p = st + ((PERM ? 4 : 8) * (lane >> 5) + ((lane & 15) >> 2)) * 128 + ((lane >> 4) & 1) * 32 + (lane & 3) * 8;
    bf16x8 f[4];
#pragma unroll
    for (int i = 0; i < 4; ++i) { const lptr q = p + (i >> 1) * 2048 + (i & 1) * 64; f[i] = cat(lds_tr(q), lds_tr(q + (PERM ? 1024 : 512))); }
#pragma unroll
    for (int i = 0; i < 4; ++i) st16(r, i * 1024 + lane * 16, dst, __builtin_bit_cast(u32x4, f[i]));
}

__global__ void __launch_bounds__(512) k_prep(const u16* __restrict__ c, const u16* __restrict__ wmod, const u16* __restrict__ wq, char* __restrict__ ws) {
    extern __shared__ __attribute__((aligned(16))) char smem[];
    const lptr lds = (lptr)smem;
    const int t = threadIdx.x, lane = t & 63, wid = __builtin_amdgcn_readfirstlane(t >> 6), b = blockIdx.x;
    if (wid == 0) STAMP(ws, 0, 0);
    if (t == 0) *(unsigned*)(ws + WS_FLAG + b * 128) = 0u;
    if (b >= 192) {
        const int j = (b - 192) & 15, kg = (b - 192) >> 4;
        tile_issue(wq + (size_t)(kg * 256 + wid * 32) * D_ + j * 64, D_, (unsigned)(uintptr_t)lds + wid * 4096, lane);
        wait_vm<0>();
        tile_store32<false>(lds + wid * 4096, mk_rsrc(ws), WS_WQ + j * 131072 + (kg * 8 + wid) * 4096, lane);
        if (wid == 0) STAMP(ws, 0, 1);
        return;
    }
    const int cg = b % 48, kg = b / 48, ch = t & 15, rg = t >> 4;
    u32x4 w[8]; u16 cv[8];
#pragma unroll
    for (int i = 0; i < 8; ++i) w[i] = __builtin_nontemporal_load((const u32x4*)(wmod + (size_t)(kg * 256 + i * 32 + rg) * 6144 + cg * 128 + ch * 8));
#pragma unroll
    for (int i = 0; i < 8; ++i) cv[i] = c[kg * 256 + i * 32 + rg];
    float acc[8] = {0, 0, 0, 0, 0, 0, 0, 0};
#pragma unroll
    for (int i = 0; i < 8; ++i) {
        const float f = bf2f(cv[i]);
        const float s = rbf(f / (1.f + __expf(-f)));
#pragma unroll
        for (int j = 0; j < 4; ++j) { acc[2 * j] += s * bflo(w[i][j]); acc[2 * j + 1] += s * bfhi(w[i][j]); }
    }
#pragma unroll
    for (int j = 0; j < 8; ++j) acc[j] = swap32_add(swap16_add(acc[j]));
    if (lane < 16) {
        *(LDSP f32x4*)(lds + wid * 512 + ch * 32) = (f32x4){acc[0], acc[1], acc[2], acc[3]};
        *(LDSP f32x4*)(lds + wid * 512 + ch * 32 + 16) = (f32x4){acc[4], acc[5], acc[6], acc[7]};
    }
    lgkm_bar();
    if (t < 128) {
        float v = 0.f;
#pragma unroll
        for (int i = 0; i < 8; ++i) v += *(const LDSP float*)(lds + i * 512 + t * 4);
        ((float*)(ws + WS_MODP))[kg * 6144 + cg * 128 + t] = v;
    }
    if (wid == 0) STAMP(ws, 0, 1);
}

constexpr int K2_W = 32768, K2_MOD = K2_W + 65536, K2_RED = K2_MOD + 8192, K2_LDS = K2_RED + 16384;
__global__ void __launch_bounds__(512) k_ln_kv(const u16* __restrict__ x, const u16* __restrict__ wk, const u16* __restrict__ wv, char* __restrict__ ws) {
    extern __shared__ __attribute__((aligned(16))) char smem[];
    const lptr lds = (lptr)smem;
    const int t = threadIdx.x, lane = t & 63, wid = __builtin_amdgcn_readfirstlane(t >> 6);
    const int b = blockIdx.x, tg = ((b >> 4) << 3) | (b & 7), which = (b >> 3) & 1;
    const float* modp = (const float*)(ws + WS_MODP);
    if (wid == 0) STAMP(ws, 1, 0);
    u32x4 xr[2][2]; f32x4 mp[4];
    {
        const u16* xp = x + (size_t)(tg * 16 + wid * 2) * D_;
        const unsigned vx = lane * 16, vm = t * 16;
        asm volatile("global_load_dwordx4 %0, %8, %10\n\tglobal_load_dwordx4 %1, %8, %10 offset:1024\n\t"
                     "global_load_dwordx4 %2, %8, %10 offset:2048\n\tglobal_load_dwordx4 %3, %8, %10 offset:3072\n\t"
                     "global_load_dwordx4 %4, %9, %11\n\tglobal_load_dwordx4 %5, %9, %12\n\tglobal_load_dwordx4 %6, %9, %13\n\tglobal_load_dwordx4 %7, %9, %14"
                     : "=&v"(xr[0][0]), "=&v"(xr[0][1]), "=&v"(xr[1][0]), "=&v"(xr[1][1]), "=&v"(mp[0]), "=&v"(mp[1]), "=&v"(mp[2]), "=&v"(mp[3])
                     : "v"(vx), "v"(vm), "s"(xp), "s"(modp), "s"(modp + 6144), "s"(modp + 2 * 6144), "s"(modp + 3 * 6144) : "memory");
    }
    float mf[4] = {0, 0, 0, 0};
    if (t < 24) {
#pragma unroll
        for (int i = 0; i < 4; ++i) mf[i] = modp[i * 6144 + b * 24 + t];
    }
    {
        const u16* W = (which ? wv : wk) + wid * 4096;
        const int row = lane >> 2, chn = (lane & 3) ^ (((row >> 3) & 1) << 1);
        const unsigned vo = (unsigned)(row * 64 + chn * 16);
#pragma unroll
        for (int q = 0; q < 2; ++q) {
            const unsigned m = (unsigned)(uintptr_t)lds + K2_W + wid * 8192 + q * 4096;
            const u16* sb_ = W + q * 2048;
            asm volatile(SETM0(m) GLDS(v, s, 0) GLDS(v, s, 1024) GLDS(v, s, 2048) GLDS(v, s, 3072)::[m] "s"(m), [v] "v"(vo), [s] "s"(sb_) : "memory");
        }
    }
    asm volatile("s_waitcnt vmcnt(8)" : "+v"(xr[0][0]), "+v"(xr[0][1]), "+v"(xr[1][0]), "+v"(xr[1][1]), "+v"(mp[0]), "+v"(mp[1]), "+v"(mp[2]), "+v"(mp[3])::"memory");
    {
        f32x4 m = (mp[0] + mp[1]) + (mp[2] + mp[3]);
#pragma unroll
        for (int e = 0; e < 4; ++e) { m[e] = rbf(m[e]); if (t >= 256) m[e] = rbf(1.f + m[e]); }
        *(LDSP f32x4*)(lds + K2_MOD + t * 16) = m;
    }
    float v[2][16], rstd[2];
#pragma unroll
    for (int r = 0; r < 2; ++r) {
#pragma unroll
        for (int h = 0; h < 2; ++h)
#pragma unroll
            for (int j = 0; j < 4; ++j) { v[r][h * 8 + 2 * j] = bflo(xr[r][h][j]); v[r][h * 8 + 2 * j + 1] = bfhi(xr[r][h][j]); }
        float s = 0;
#pragma unroll
        for (int j = 0; j < 16; ++j) s += v[r][j];
        const float mean = wave_sum(s) * (1.f / 1024.f);
        float q = 0;
#pragma unroll
        for (int j = 0; j < 16; ++j) { v[r][j] -= mean; q += v[r][j] * v[r][j]; }
        rstd[r] = __builtin_amdgcn_rsqf(wave_sum(q) * (1.f / 1024.f) + 1e-5f);
    }
    if (wid == 0) STAMP(ws, 1, 1);
    lgkm_bar();
#pragma unroll
    for (int h = 0; h < 2; ++h) {
        f32x4 sa[2], a1[2];
#pragma unroll
        for (int q = 0; q < 2; ++q) {
            sa[q] = *(const LDSP f32x4*)(lds + K2_MOD + (h * 512 + lane * 8 + q * 4) * 4);
            a1[q] = *(const LDSP f32x4*)(lds + K2_MOD + 4096 + (h * 512 + lane * 8 + q * 4) * 4);
        }
#pragma unroll
        for (int r = 0; r < 2; ++r) {
            u32x4 o;
#pragma unroll
            for (int j = 0; j < 4; ++j) {
                const int e = 2 * j;
                o[j] = pk2(v[r][h * 8 + e] * rstd[r] * a1[e >> 2][e & 3] + sa[e >> 2][e & 3], v[r][h * 8 + e + 1] * rstd[r] * a1[e >> 2][(e & 3) + 1] + sa[e >> 2][(e & 3) + 1]);
            }
            const int kc = h * 64 + lane;
            *(LDSP u32x4*)(lds + kc * 256 + (((wid * 2 + r) ^ (kc & 7)) * 16)) = o;
        }
    }
    wait_vm<0>();
    lgkm_bar();
    if (wid == 0) STAMP(ws, 1, 2);
    if (t < 24) {
        const int col = b * 24 + t, part = col >> 10, k = col & 1023;
        float v = rbf((mf[0] + mf[1]) + (mf[2] + mf[3]));
        if (part == 1 || part == 4) v = rbf(1.f + v);
        ((float*)(ws + WS_MOD))[col] = v;
        if (part == 3 || part == 4)
            ((u16*)(ws + WS_BUT))[(((k >> 5) * 2 + (part == 3)) * 4 + 2 * ((k >> 4) & 1) + ((k >> 2) & 1)) * 8 + (k & 3) + 4 * ((k >> 3) & 1)] = (u16)(__float_as_uint(v) >> 16);
    }
    {
        const unsigned hAT = WS_H + (tg >> 3) * 262144 + (tg & 7) * 256;
#pragma unroll
        for (int i = 0; i < 2; ++i) {
            const int kc = which * 64 + i * 32 + (t >> 4), tk = t & 15;
            st16(mk_rsrc(ws), kc * 2048 + tk * 16, hAT, *(const LDSP u32x4*)(lds + kc * 256 + ((tk ^ (kc & 7)) * 16)));
        }
    }
    f32x4 acc[2] = {{0, 0, 0, 0}, {0, 0, 0, 0}};
    {
        const int g = lane >> 4, tk = lane & 15, q = (lane & 15) >> 2, p = lane & 3;
#pragma unroll
        for (int s = 0; s < 4; ++s) {
            const int kc = wid * 16 + s * 4 + g;
            const bf16x8 hf = lds_b128(lds + kc * 256 + ((tk ^ (kc & 7)) * 16));
            const int row = kc * 8 + q;
#pragma unroll
            for (int nb = 0; nb < 2; ++nb) {
                const lptr wp = lds + K2_W + row * 64 + ((nb ^ (kc & 1)) * 32) + p * 8;
                const bf16x8 wf = cat(lds_tr(wp), lds_tr(wp + 256));
                acc[nb] = which ? mfma16(hf, wf, acc[nb]) : mfma16(wf, hf, acc[nb]);
            }
        }
    }
#pragma unroll
    for (int nb = 0; nb < 2; ++nb) *(LDSP f32x4*)(lds + K2_RED + ((wid * 2 + nb) * 64 + lane) * 16) = acc[nb];
    lgkm_bar();
    if (t < 128) {
        const int nb = t >> 6;
        f32x4 r = *(const LDSP f32x4*)(lds + K2_RED + (nb * 64 + lane) * 16);
#pragma unroll
        for (int w = 1; w < 8; ++w) r += *(const LDSP f32x4*)(lds + K2_RED + ((w * 2 + nb) * 64 + lane) * 16);
        u32x2 o = {pk2(r[0], r[1]), pk2(r[2], r[3])};
        const int g = lane >> 4, c = lane & 15, kt = tg >> 1;
        if (which) {
            *(u32x2*)(ws + WS_VT + kt * 2048 + ((tg & 1) * 2 + (g & 1)) * 512 + (nb * 16 + c) * 16 + (g >> 1) * 8) = o;
        } else {
            *(u32x2*)(ws + WS_KT + kt * 2048 + (nb * 2 + (g & 1)) * 512 + ((tg & 1) * 16 + c) * 16 + (g >> 1) * 8) = o;
        }
    }
    if (wid == 0) STAMP(ws, 1, 3);
}

#define R0 3
constexpr int ring_issued(int i, int NST, int NS) { int n = R0 < NS - 1 ? R0 : NS - 1; for (int k = 0; k < i; ++k) { n += 2; if (n > k + NS) n = k + NS; if (n > NST) n = NST; } return n; }
constexpr int ring_ramp(int NST, int NS) { int i = 0; while (ring_issued(i, NST, NS) != i + NS - 1) ++i; return i; }
template <int I> DI void tcap(TS& T) { TCAP(T, I); }
template <int NST, int NS, int PER, class F>
DI void ring_loader(F issue, char* ws = nullptr, int kid = -1) {
    constexpr int R = ring_ramp(NST, NS);
    TS T; tcap<0>(T);
#pragma unroll
    for (int i = 0; i < ring_issued(0, NST, NS); ++i) issue(i);
    tcap<1>(T);
    [&]<int... I>(std::integer_sequence<int, I...>) {
        ((wait_vm<PER*(ring_issued(I, NST, NS) - 1 - I)>(), tcap<2 + 2 * I>(T), bar(), tcap<3 + 2 * I>(T), issue(ring_issued(I, NST, NS) % NS),
          (ring_issued(I + 1, NST, NS) - ring_issued(I, NST, NS) > 1 ? issue((ring_issued(I, NST, NS) + 1) % NS) : (void)0)), ...);
    }(std::make_integer_sequence<int, R>{});
    int slot = (R + NS - 1) % NS;
    for (int i = R; i <= NST - NS; ++i) {
        wait_vm<PER*(NS - 2)>(); bar();
        issue(slot); slot = (slot + 1 == NS) ? 0 : slot + 1;
    }
    tcap<2 + 2 * R>(T);
    [&]<int... I>(std::integer_sequence<int, I...>) { ((wait_vm<PER*(NS - 2 - I)>(), bar(), tcap<3 + 2 * R + (I < 4 ? I : 4)>(T)), ...); }(std::make_integer_sequence<int, NS - 1>{});
    if (kid >= 0 && (threadIdx.x >> 6) == 4) TDUMP(T, ws, kid, 3 + 2 * R + (NS - 1 < 5 ? NS - 1 : 5));
}
constexpr int cmin(int a, int b) { return a < b ? a : b; }
constexpr int pair_issued(int m, int NST, int NS) {
    int n = 3;
    for (int k = -1; k < m; ++k) n = cmin(cmin(n + 4, k < 0 ? NS : 2 * k + 1 + NS), NST);
    return n;
}
template <int NST, int NS, class CNT, class F, class X>
DI void pair_loader(F issue, X extra) {
    auto range = [&]<int S0, int... I>(std::integer_sequence<int, I...>) { (issue.template operator()<S0 + I>(), ...); };
    auto sync = [&]<int M>() {
        constexpr int need = M < 0 ? 0 : cmin(2 * M + 2, NST - 1), have = pair_issued(M, NST, NS);
        constexpr int out = [] { int c = 0; for (int s = need + 1; s < have; ++s) c += CNT::n(s); return c; }();
        static_assert(out < 64 && need < have, "");
        wait_vm<out>();
        extra.template operator()<M, 0>();
        bar();
        range.template operator()<have>(std::make_integer_sequence<int, pair_issued(M + 1, NST, NS) - have>{});
        extra.template operator()<M, 1>();
    };
    range.template operator()<0>(std::make_integer_sequence<int, 3>{});
    [&]<int... I>(std::integer_sequence<int, I...>) { (sync.template operator()<I - 1>(), ...); }(std::make_integer_sequence<int, NST / 2 + 1>{});
}
constexpr int G64_STAGE = 24576;
template <int NST, int NS>
DI void g64_loader(const char* sa, const char* sw, unsigned ldsb, int lw, int lane, char* ws = nullptr, int kid = -1) {
    const unsigned va = lw * 4096 + lane * 16, vw = lw * 2048 + lane * 16;
    ring_loader<NST, NS, 6>([&](int slot) {
        const unsigned ma = ldsb + slot * G64_STAGE + lw * 4096, mw = ldsb + slot * G64_STAGE + 16384 + lw * 2048;
        if (ABL != 2) asm volatile(SETM0(ma) GLDS(va, sa, 0) GLDS(va, sa, 1024) GLDS(va, sa, 2048) GLDS(va, sa, 3072) SETM0(mw) GLDS(vw, sw, 0) GLDS(vw, sw, 1024)
                     ::[ma] "s"(ma), [mw] "s"(mw), [va] "v"(va), [vw] "v"(vw), [sa] "s"(sa), [sw] "s"(sw) : "memory");
        sa += 16384; sw += 8192;
    }, ws, kid);
}

struct G64Frag { bf16x8 a[4][2], w[4]; };
template <int NST, int NS, class H>
DI void g64_compute(lptr lds, int cw, int lane, f32x16 (&acc)[2], H hook) {
    const LP pa = mk_lp(lds + (lane >> 5) * 2048 + ((cw & 1) * 64 + (lane & 31)) * 16), pw = mk_lp(lds + (cw >> 1) * 1024 + lane * 16);
    G64Frag fr[2];
    auto rd = [&]<int S, int I>() {
        constexpr int so = (S % NS) * G64_STAGE;
        if constexpr (S < NST && I < 12) {
            if constexpr (I % 3 == 0) fr[S & 1].w[I / 3] = lds_at<so + 16384 + (I / 3) * 2048>(pw);
            else fr[S & 1].a[I / 3][I % 3 - 1] = lds_at<so + (I / 3) * 4096 + (I % 3 - 1) * 512>(pa);
        }
    };
    auto step = [&]<int S>() {
        if constexpr (S + 1 < NST) { lgkm_bar(); SBAR(); }
        if constexpr (ABL != 1)
            [&]<int... U>(std::integer_sequence<int, U...>) {
                ((acc[U % 2] = mfma32(fr[S & 1].w[U / 2], fr[S & 1].a[U / 2][U % 2], acc[U % 2]), SBAR(),
                  rd.template operator()<S + 1, 2 * U>(), rd.template operator()<S + 1, 2 * U + 1>(), SBAR()), ...);
            }(std::make_integer_sequence<int, 8>{});
        hook(S);
    };
    bar();
    [&]<int... I>(std::integer_sequence<int, I...>) { (rd.template operator()<0, I>(), ...); }(std::make_integer_sequence<int, 12>{});
    hook(-1);
    [&]<int... S>(std::integer_sequence<int, S...>) { (step.template operator()<S>(), ...); }(std::make_integer_sequence<int, NST>{});
}
DI void tile_map(int b, int& p, int& j) { const int x = b & 7, i = b >> 3; p = (x >> 1) * 4 + (i >> 3); j = (x & 1) * 8 + (i & 7); }

constexpr int QA_NS = 4;
constexpr int QA_KV0 = QA_NS * G64_STAGE;
constexpr int QA_LDS = QA_KV0 + 65536;
constexpr float QSCALE = 0.17677669529663687f * 1.4426950408889634f;

__global__ void __launch_bounds__(512) k_qattn(const u16* __restrict__ wo, const u16* __restrict__ w1, const u16* __restrict__ w2, char* __restrict__ ws) {
    extern __shared__ __attribute__((aligned(16))) char smem[];
    const lptr lds = (lptr)smem;
    const int t = threadIdx.x, lane = t & 63, wid = __builtin_amdgcn_readfirstlane(t >> 6);
    int p, j; tile_map(blockIdx.x, p, j);
    const unsigned ldsb = (unsigned)(uintptr_t)lds;
    const char* KT = ws + WS_KT; const char* VT = ws + WS_VT;
    const unsigned vkv = lane * 16;
    auto kv_piece = [&](int chunk, unsigned buf, int w, int i) {
        const char* s = ((i & 4) ? VT : KT) + chunk * 32768 + w * 4096 + (i & 3) * 1024;
        const unsigned m = ldsb + buf + (i >> 2) * 32768 + w * 4096 + (i & 3) * 1024;
        asm volatile(SETM0(m) GLDS(v, s, 0)::[m] "s"(m), [v] "v"(vkv), [s] "s"(s) : "memory");
    };
    const int b = blockIdx.x;
    const bool big = b < 224;
    const rsrc_t rws = mk_rsrc(ws);
    const lptr stg = lds + 65536 + wid * 4096;
    const int jA = b & 15, gA = big ? (b >> 4) : ((b - 224) >> 4);
    const int gB = big ? (b / 56) : 2 + ((b - 224) >> 4), cB = big ? (b % 56) : jA;
    bf16x8 but = {};
    if (big && (lane & 15) < 2) but = *(const bf16x8*)(ws + WS_BUT + (((gB * 8 + wid) * 2 + (lane & 15)) * 4 + (lane >> 4)) * 16);
    if (wid == 0) STAMP(ws, 2, 0);
    if (wid >= 4) {
        g64_loader<16, QA_NS>(ws + WS_H + (size_t)p * 262144, ws + WS_WQ + (size_t)j * 131072, ldsb, wid - 4, lane);
        bar();
    } else {
        f32x16 r[2] = {};
        g64_compute<16, QA_NS>(lds, wid, lane, r, [&](int i) {
            if (i >= 0 && i < 8) { kv_piece(0, QA_KV0, wid, i); kv_piece(0, QA_KV0, wid + 4, i); }
        });
        if (wid == 0) STAMP(ws, 2, 1);
#pragma unroll
        for (int tb = 0; tb < 2; ++tb)
#pragma unroll
            for (int s = 0; s < 2; ++s) {
                u32x4 o;
#pragma unroll
                for (int i = 0; i < 4; ++i) o[i] = pk2(r[tb][8 * s + 2 * i] * QSCALE, r[tb][8 * s + 2 * i + 1] * QSCALE);
                *(LDSP u32x4*)(lds + 32768 + ((wid * 2 + tb) * 2 + s) * 1024 + lane * 16) = o;
            }
        wait_vm<0>();
        lgkm_bar();
    }
    const int qg = wid & 3, kh = wid >> 2;
    const int lo = kh * 16384 + lane * 16;
    bf16x8 qA0, qA1, qB0, qB1, k0, k1, v0, v1;
    {
        const lptr qp = lds + 32768 + qg * 4096 + lane * 16;
        qA0 = lds_b128(qp); qA1 = lds_b128(qp + 1024); qB0 = lds_b128(qp + 2048); qB1 = lds_b128(qp + 3072);
        k0 = lds_b128(lds + QA_KV0 + lo); k1 = lds_b128(lds + QA_KV0 + lo + 1024);
    }
    bf16x8 sel;
    {
        const unsigned on = ((lane & 15) == ((lane >> 4) & 1)) ? 0x3f803f80u : 0u;
        sel = __builtin_bit_cast(bf16x8, (u32x4){on, on, on, on});
    }
    f32x16 oA = {}, oB = {}, sA, sB;
    f32x4 lA = {}, lB = {};
    const f32x16 zero = {};
    u32x4 pA0, pA1, pB0 = {0, 0, 0, 0}, pB1 = {0, 0, 0, 0};
    PIN(but);
    lgkm_bar();
    if (wid == 0) STAMP(ws, 2, 2);
#define EXP2(E, d, S, i) do { E[d] = __builtin_amdgcn_exp2f(S[i]); E[d + 1] = __builtin_amdgcn_exp2f(S[i + 1]); PIN(E); } while (0)
#define EXP4(E, S, i) do { E[0] = __builtin_amdgcn_exp2f(S[i]); E[1] = __builtin_amdgcn_exp2f(S[i + 1]); E[2] = __builtin_amdgcn_exp2f(S[i + 2]); E[3] = __builtin_amdgcn_exp2f(S[i + 3]); } while (0)
#define PK2(P, d, E) do { P[d] = pk2(E[0], E[1]); P[d + 1] = pk2(E[2], E[3]); PIN(P); } while (0)
#define BF(x) __builtin_bit_cast(bf16x8, x)
#define HALF(SX, PX0, PX1, SY, QY0, QY1, OZ, LZ, PZ0, PZ1, R0, R1, R2, R3) do { \
        SY = mfma32(k0, QY0, zero); SBAR(); PK2(PZ1, 2, e); R0; SBAR(); EXP4(e, SX, 0); PIN(e); SBAR(); \
        SY = mfma32(k1, QY1, SY); SBAR(); PK2(PX0, 0, e); R1; SBAR(); EXP2(e, 0, SX, 4); SBAR(); LZ = mfma16(sel, BF(PZ0), LZ); SBAR(); EXP2(e, 2, SX, 6); SBAR(); \
        OZ = mfma32(v0, BF(PZ0), OZ); SBAR(); PK2(PX0, 2, e); R2; SBAR(); EXP4(e, SX, 8); PIN(e); SBAR(); \
        OZ = mfma32(v1, BF(PZ1), OZ); SBAR(); PK2(PX1, 0, e); R3; SBAR(); EXP2(e, 0, SX, 12); SBAR(); LZ = mfma16(sel, BF(PZ1), LZ); SBAR(); EXP2(e, 2, SX, 14); SBAR(); } while (0)
    sA = mfma32(k0, qA0, zero); sA = mfma32(k1, qA1, sA);
    v0 = k0; v1 = k1;
    f32x4 e = {0, 0, 0, 0};
    bf16x8 w0, w1_, sf[4];
    f32x4 ut[4];
    const lptr tp32 = stg + (4 * (lane >> 5) + ((lane & 15) >> 2)) * 128 + ((lane >> 4) & 1) * 32 + (lane & 3) * 8;
    const lptr tp16 = stg + (16 * (lane >> 5) + 4 * ((lane >> 4) & 1) + ((lane & 15) >> 2)) * 128 + (lane & 3) * 8;
    unsigned dstA = big ? WS_W2 + (jA >> 1) * 917504 + (gA * 8 + wid) * 8192 + (jA & 1) * 4096 : WS_WO + jA * 131072 + (gA * 8 + wid) * 4096, dstB[4];
#pragma unroll
    for (int i = 0; i < 4; ++i) {
        const int nbk = cB * 4 + i, jt = nbk / 14;
        dstB[i] = big ? WS_W1 + jt * 458752 + (gB * 8 + wid) * 14336 + (nbk - jt * 14) * 1024 : WS_WO + cB * 131072 + (gB * 8 + wid) * 4096 + i * 1024;
        asm volatile("" : "+s"(dstB[i]));
    }
    const int ldB = big ? F_ : D_;
    const u16* srcA = (big ? w2 : wo) + jA * 64 + (size_t)(gA * 256 + wid * 32) * D_;
    const u16* srcB = (big ? w1 : wo) + cB * 64 + (size_t)(gB * 256 + wid * 32) * ldB;
    asm volatile("" : "+s"(dstA), "+s"(srcA), "+s"(srcB));
    auto side = [&]<int C, int IT, int I>() {
        if constexpr (C < 3 && IT < 2 && !(AA & 2)) kv_piece(C + 1, (C & 1) ? QA_KV0 : 0, wid, IT * 4 + I);
        if constexpr (AA & 1) return;
        if constexpr ((C == 0 && IT == 2) || (C == 1 && IT == 4)) {
            const int ld = C == 0 ? D_ : ldB;
            const u16* s = (C == 0 ? srcA : srcB) + I * 8 * ld;
            const unsigned vo = (unsigned)((lane >> 3) * ld * 2 + (lane & 7) * 16), m = ldsb + 65536 + wid * 4096 + I * 1024;
            asm volatile(SETM0(m) GLDS(v, s, 0)::[m] "s"(m), [v] "v"(vo), [s] "s"(s) : "memory");
        }
        if constexpr ((C == 1 || C == 2) && IT == 2) {
            if constexpr (I == 0) wait_vm<8>();
            const lptr q = !big ? tp32 + (I >> 1) * 2048 + (I & 1) * 64 : (C == 2 ? tp16 + I * 32 : tp16 + (lane & 3) * 8 + (I >> 1) * 64 + (I & 1) * 8);
            sf[I] = cat(lds_tr(q), lds_tr(q + 1024));
        }
        if constexpr ((C == 1 || C == 2) && IT == 3) {
            const unsigned dst = C == 1 ? dstA + I * 1024 : dstB[I];
            if constexpr (C == 2) ut[I] = mfma16(sf[I], but, (f32x4){0, 0, 0, 0});
            st16(rws, lane * 16, dst, __builtin_bit_cast(u32x4, sf[I]));
        }
        if constexpr (C == 2 && IT == 4) *(LDSP f32x4*)(stg + I * 1024 + lane * 16) = ut[I];
    };
#define SIDE(I) side.template operator()<C, IT, I>()
    auto step = [&]<int C, int IT>() {
        const lptr kp = lds + ((C & 1) ? 0 : QA_KV0) + lo, kn = lds + ((C & 1) ? QA_KV0 : 0) + lo;
        if constexpr (PRIO && IT == 5) __builtin_amdgcn_s_setprio(2);
        if constexpr (PRIO && IT == 6) __builtin_amdgcn_s_setprio(1);
        if constexpr (IT < 7) {
            HALF(sA, pA0, pA1, sB, qB0, qB1, oB, lB, pB0, pB1, SIDE(0), k0 = lds_b128(kp + (IT + 1) * 2048),
                 k1 = lds_b128(kp + (IT + 1) * 2048 + 1024), v0 = lds_b128(kp + 32768 + IT * 2048));
            if constexpr (PRIO && IT == 6) __builtin_amdgcn_s_setprio(0);
            HALF(sB, pB0, pB1, sA, qA0, qA1, oA, lA, pA0, pA1, v1 = lds_b128(kp + 32768 + IT * 2048 + 1024),
                 SIDE(1); if constexpr (IT == 6) w0 = lds_b128(kp + 32768 + 7 * 2048), SIDE(2); if constexpr (IT == 6) w1_ = lds_b128(kp + 32768 + 7 * 2048 + 1024), SIDE(3));
        } else {
            if constexpr (C < 3 && !(AA & 4)) {
                wait_vm<(AA & 1) ? 0 : (C == 1 ? 8 : 4)>();
                lgkm_bar();
                if constexpr (PRIO) __builtin_amdgcn_s_setprio(3);
            }
            HALF(sA, pA0, pA1, sB, qB0, qB1, oB, lB, pB0, pB1, (void)0, if constexpr (C < 3) k0 = lds_b128(kn), if constexpr (C < 3) k1 = lds_b128(kn + 1024), (void)0);
            v0 = w0; v1 = w1_;
            HALF(sB, pB0, pB1, sA, qA0, qA1, oA, lA, pA0, pA1, (void)0, (void)0, (void)0, (void)0);
        }
    };
    if constexpr (PRIO) __builtin_amdgcn_s_setprio(3);
    [&]<int... N>(std::integer_sequence<int, N...>) { (step.template operator()<N / 8, N % 8>(), ...); }(std::make_integer_sequence<int, 32>{});
    PK2(pB1, 2, e);
    oB = mfma32(v0, BF(pB0), oB); oB = mfma32(v1, BF(pB1), oB);
    lB = mfma16(sel, BF(pB0), lB); lB = mfma16(sel, BF(pB1), lB);
    PIN(lA); PIN(lB); PIN(oA); PIN(oB);
    if (wid == 0) STAMP(ws, 2, 3);
    const lptr ex = lds + QA_KV0;
    {
        const f32x16& so = kh ? oA : oB;
        const f32x4& sl = kh ? lA : lB;
#pragma unroll
        for (int i = 0; i < 4; ++i) *(LDSP f32x4*)(ex + wid * 4096 + i * 1024 + lane * 16) = (f32x4){so[4 * i], so[4 * i + 1], so[4 * i + 2], so[4 * i + 3]};
        const f32x4& ml = kh ? lB : lA;
        if (lane < 16) {
            *(LDSP float*)(ex + 32768 + wid * 256 + lane * 4) = sl[0]; *(LDSP float*)(ex + 32768 + wid * 256 + 64 + lane * 4) = sl[1];
            *(LDSP float*)(ex + 32768 + wid * 256 + 128 + lane * 4) = ml[0]; *(LDSP float*)(ex + 32768 + wid * 256 + 192 + lane * 4) = ml[1];
        }
    }
    lgkm_bar();
    f32x16 o = kh ? oB : oA;
    float l;
    {
        l = *(const LDSP float*)(ex + 32768 + wid * 256 + 128 + (lane & 31) * 4) + *(const LDSP float*)(ex + 32768 + (wid ^ 4) * 256 + (lane & 31) * 4);
#pragma unroll
        for (int i = 0; i < 4; ++i) {
            const f32x4 v = *(const LDSP f32x4*)(ex + (wid ^ 4) * 4096 + i * 1024 + lane * 16);
#pragma unroll
            for (int e = 0; e < 4; ++e) o[4 * i + e] += v[e];
        }
    }
    float chk = l * 0.f;
#pragma unroll
    for (int i = 0; i < 16; ++i) chk = __builtin_fmaf(o[i], 0.f, chk);
    const bool bad = AA ? false : (!(chk == 0.f) || !(l >= 0x1p-90f));
    if (__builtin_expect(__any(bad), 0)) {
        const bf16x8 q0 = kh ? qB0 : qA0, q1 = kh ? qB1 : qA1;
        float m = -INFINITY; l = 0.f; o = zero;
        for (int kt = 0; kt < 64; ++kt) {
            const bf16x8 a0 = *(const bf16x8*)(KT + kt * 2048 + lane * 16), a1 = *(const bf16x8*)(KT + kt * 2048 + 1024 + lane * 16);
            const bf16x8 b0 = *(const bf16x8*)(VT + kt * 2048 + lane * 16), b1 = *(const bf16x8*)(VT + kt * 2048 + 1024 + lane * 16);
            f32x16 s = mfma32(a0, q0, zero); s = mfma32(a1, q1, s);
            float mx = s[0];
#pragma unroll
            for (int i = 1; i < 16; ++i) mx = fmaxf(mx, s[i]);
            mx = fmaxf(mx, __shfl_xor(mx, 32));
            const float mn = fmaxf(m, mx), al = __builtin_amdgcn_exp2f(m - mn);
            m = mn;
            u32x4 p0, p1; float rs = 0.f;
#pragma unroll
            for (int i = 0; i < 4; ++i) {
                p0[i] = pk2(__builtin_amdgcn_exp2f(s[2 * i] - m), __builtin_amdgcn_exp2f(s[2 * i + 1] - m));
                p1[i] = pk2(__builtin_amdgcn_exp2f(s[8 + 2 * i] - m), __builtin_amdgcn_exp2f(s[9 + 2 * i] - m));
                rs += bflo(p0[i]) + bfhi(p0[i]) + bflo(p1[i]) + bfhi(p1[i]);
            }
            rs += __shfl_xor(rs, 32);
            l = l * al + rs;
#pragma unroll
            for (int i = 0; i < 16; ++i) o[i] *= al;
            o = mfma32(b0, BF(p0), o); o = mfma32(b1, BF(p1), o);
        }
    }
    {
        const float inv = __builtin_amdgcn_rcpf(l);
        const unsigned oAT = WS_O + p * 262144 + (2 * j + (qg >> 1)) * 8192 + ((qg & 1) * 2 + kh) * 512, vo = (lane >> 5) * 2048 + (lane & 31) * 16;
#pragma unroll
        for (int s = 0; s < 2; ++s) {
            u32x4 w;
#pragma unroll
            for (int i = 0; i < 4; ++i) w[i] = pk2(o[8 * s + 2 * i] * inv, o[8 * s + 2 * i + 1] * inv);
            st16(rws, vo, oAT + s * 4096, w);
        }
    }
    if (big && t < 128) {
        float s = 0.f;
#pragma unroll
        for (int w = 0; w < 8; ++w) s += *(const LDSP float*)(lds + 65536 + w * 4096 + ((t & 63) >> 4) * 1024 + (((t >> 2) & 3) * 16 + (t >> 6)) * 16 + (t & 3) * 4);
        ((float*)(ws + WS_UTP))[(gB * 2 + (t >> 6)) * F_ + cB * 64 + (t & 63)] = s;
    }
    if (wid == 0) STAMP(ws, 2, 4);
}

constexpr int OP_NS = 6;
__global__ void __launch_bounds__(512) k_oproj(const u16* __restrict__ x, char* __restrict__ ws) {
    extern __shared__ __attribute__((aligned(16))) char smem[];
    const lptr lds = (lptr)smem;
    const int t = threadIdx.x, lane = t & 63, wid = __builtin_amdgcn_readfirstlane(t >> 6);
    int p, j; tile_map(blockIdx.x, p, j);
    if (wid == 0) STAMP(ws, 3, 0);
    if (wid >= 4) {
        g64_loader<16, OP_NS>(ws + WS_O + (size_t)p * 262144, ws + WS_WO + (size_t)j * 131072, (unsigned)(uintptr_t)lds, wid - 4, lane, ws, 6);
        bar();
        if (wid == 4 && lane < 28) {
            const float* up = (const float*)(ws + WS_UTP) + blockIdx.x * 28 + lane;
            ((float*)(ws + WS_UT))[blockIdx.x * 28 + lane] = ((up[0] + up[2 * F_]) + (up[4 * F_] + up[6 * F_])) * (blockIdx.x < 128 ? 1.f : -1.4426950408889634f);
        }
        return;
    }
    const int th = wid & 1, nb = wid >> 1, hi = lane >> 5;
    const int n0 = j * 64 + nb * 32 + 4 * hi;
    const float* mod = (const float*)(ws + WS_MOD);
    u32x4 xq[2][2]; f32x4 ga[4], a2[4];
    f32x16 r[2] = {};
    if (wid == 0) STAMP(ws, 3, 1);
    g64_compute<16, OP_NS>(lds, wid, lane, r, [&](int i) {
        if (i == -1) {
#pragma unroll
            for (int tb = 0; tb < 2; ++tb)
#pragma unroll
                for (int h = 0; h < 2; ++h) xq[tb][h] = *(const u32x4*)(x + (size_t)(p * 128 + th * 64 + tb * 32 + (lane & 31)) * D_ + j * 64 + nb * 32 + h * 16 + hi * 8);
#pragma unroll
            for (int q = 0; q < 4; ++q) {
                ga[q] = *(const f32x4*)(mod + 2048 + n0 + 8 * q);
                a2[q] = *(const f32x4*)(mod + 4096 + n0 + 8 * q);
            }
            SBAR();
        }
    });
    if (wid == 0) STAMP(ws, 3, 2);
    f32x2 st[2];
#pragma unroll
    for (int tb = 0; tb < 2; ++tb) {
        u32x4 xo[2], yo[2]; float v[16]; float s = 0.f;
        u32x2 xr[2][4];
#pragma unroll
        for (int h = 0; h < 2; ++h) {
            const auto s0 = __builtin_amdgcn_permlane32_swap(xq[tb][h][0], xq[tb][h][2], false, false);
            const auto s1 = __builtin_amdgcn_permlane32_swap(xq[tb][h][1], xq[tb][h][3], false, false);
            xr[tb][2 * h] = (u32x2){s0[0], s1[0]}; xr[tb][2 * h + 1] = (u32x2){s0[1], s1[1]};
        }
#pragma unroll
        for (int i = 0; i < 4; ++i) {
            const float x0 = bflo(xr[tb][i][0]), x1 = bfhi(xr[tb][i][0]), x2 = bflo(xr[tb][i][1]), x3 = bfhi(xr[tb][i][1]);
            const unsigned a = pk2(x0 + ga[i][0] * r[tb][4 * i], x1 + ga[i][1] * r[tb][4 * i + 1]);
            const unsigned b = pk2(x2 + ga[i][2] * r[tb][4 * i + 2], x3 + ga[i][3] * r[tb][4 * i + 3]);
            xo[i >> 1][(i & 1) * 2] = a; xo[i >> 1][(i & 1) * 2 + 1] = b;
            v[4 * i] = bflo(a); v[4 * i + 1] = bfhi(a); v[4 * i + 2] = bflo(b); v[4 * i + 3] = bfhi(b);
            yo[i >> 1][(i & 1) * 2] = pk2(v[4 * i] * a2[i][0], v[4 * i + 1] * a2[i][1]);
            yo[i >> 1][(i & 1) * 2 + 1] = pk2(v[4 * i + 2] * a2[i][2], v[4 * i + 3] * a2[i][3]);
            s += (v[4 * i] + v[4 * i + 1]) + (v[4 * i + 2] + v[4 * i + 3]);
        }
        const unsigned off = p * 262144 + (j * 2 + nb) * 8192 + (th * 64 + tb * 32) * 16, vo = hi * 2048 + (lane & 31) * 16;
#pragma unroll
        for (int h = 0; h < 2; ++h) {
            st16(mk_rsrc(ws), vo, WS_Y + off + h * 4096, yo[h]);
            const auto s0 = __builtin_amdgcn_permlane32_swap(xo[h][0], xo[h][2], false, false);
            const auto s1 = __builtin_amdgcn_permlane32_swap(xo[h][1], xo[h][3], false, false);
            st16(mk_rsrc(ws), vo, WS_X2 + off + h * 4096, (u32x4){s0[0], s1[0], s0[1], s1[1]});
        }
        s += __shfl_xor(s, 32);
        const float mean = s * (1.f / 32.f);
        float q = 0.f;
#pragma unroll
        for (int i = 0; i < 16; ++i) { const float d = v[i] - mean; q += d * d; }
        q += __shfl_xor(q, 32);
        st[tb] = (f32x2){mean, q};
    }
    if (nb == 1 && lane < 32) { *(LDSP f32x2*)(lds + 65536 + (th * 64 + lane) * 8) = st[0]; *(LDSP f32x2*)(lds + 65536 + (th * 64 + 32 + lane) * 8) = st[1]; }
    lgkm_bar();
    if (nb == 0) {
        const f32x2 a = hi ? st[1] : st[0];
        const f32x2 b = *(const LDSP f32x2*)(lds + 65536 + (th * 64 + lane) * 8);
        const float d = a[0] - b[0];
        *(f32x2*)(ws + WS_ST + ((size_t)j * S_ + p * 128 + th * 64 + lane) * 8) = (f32x2){0.5f * (a[0] + b[0]), a[1] + b[1] + 16.f * d * d};
    }
    if (wid == 0) STAMP(ws, 3, 3);
}

constexpr int F1_NS = 7, F1_STAGE = 22528, F1_NST = 32, F1_AUX = F1_NS * F1_STAGE, F1_LDS = F1_AUX + 3072;
template <int N> struct CntC { static constexpr int n(int) { return N; } };
template <int NW>
DI void f1_loader(char* ws, int p, int j, lptr lds, int lw, int lane) {
    const char* sa = ws + WS_Y + (size_t)p * 262144;
    const char* sw = ws + WS_W1 + (size_t)j * 458752;
    const unsigned ldsb = (unsigned)(uintptr_t)lds;
    const int pc0 = lw < 2 ? lw * 4 : 8 + (lw - 2) * 3;
    const unsigned va = lw * 2048 + lane * 16, vw = pc0 * 1024 + lane * 16;
    f32x2 sp[8];
    {
        const char* s = ws + WS_ST + (size_t)(p * 128 + lw * 32) * 8;
        const unsigned v0 = (lane >> 5) * 8 * (S_ * 8) + (lane & 31) * 8;
#pragma unroll
        for (int i = 0; i < 8; ++i) asm volatile("global_load_dwordx2 %0, %1, %2" : "=v"(sp[i]) : "v"(v0 + i * (S_ * 8)), "s"(s) : "memory");
        if (NW == 3) {
            const char* u = ws + WS_UT + ((lw - 2) * F_ + j * 224) * 4;
            const unsigned m = ldsb + F1_AUX + (lw - 2) * 1024, vu = lane * 16;
            asm volatile(SETM0(m) GLDS(v, s, 0)::[m] "s"(m), [v] "v"(vu), [s] "s"(u) : "memory");
        }
    }
    pair_loader<F1_NST, F1_NS, CntC<NW + 2>>([&]<int S>() {
        const unsigned ma = ldsb + (S % F1_NS) * F1_STAGE + lw * 2048, mw = ldsb + (S % F1_NS) * F1_STAGE + 8192 + pc0 * 1024;
        if constexpr (ABL == 2) {} else if constexpr (NW == 4)
            asm volatile(SETM0(ma) GLDS(va, sa, 0) GLDS(va, sa, 1024) SETM0(mw) GLDS(vw, sw, 0) GLDS(vw, sw, 1024) GLDS(vw, sw, 2048) GLDS(vw, sw, 3072)
                         ::[ma] "s"(ma), [mw] "s"(mw), [va] "v"(va), [vw] "v"(vw), [sa] "s"(sa), [sw] "s"(sw) : "memory");
        else
            asm volatile(SETM0(ma) GLDS(va, sa, 0) GLDS(va, sa, 1024) SETM0(mw) GLDS(vw, sw, 0) GLDS(vw, sw, 1024) GLDS(vw, sw, 2048)
                         ::[ma] "s"(ma), [mw] "s"(mw), [va] "v"(va), [vw] "v"(vw), [sa] "s"(sa), [sw] "s"(sw) : "memory");
        sa += 8192; sw += 14336;
    }, [&]<int M, int PH>() {
        if constexpr (M == -1 && PH == 0) {
#pragma unroll
            for (int i = 0; i < 8; ++i) PIN(sp[i]);
        }
        if constexpr (M == 2 && PH == 1) {
            float s = 0.f;
#pragma unroll
            for (int i = 0; i < 8; ++i) s += sp[i][0];
            const float mu = swap32_add(s) * (1.f / 16.f);
            float q = 0.f;
#pragma unroll
            for (int i = 0; i < 8; ++i) { const float d = sp[i][0] - mu; q += sp[i][1] + 64.f * d * d; }
            const float rl = -1.4426950408889634f * __builtin_amdgcn_rsqf(swap32_add(q) * (1.f / 1024.f) + 1e-5f);
            if (lane < 32) *(LDSP f32x2*)(lds + F1_AUX + 2048 + (lw * 32 + lane) * 8) = (f32x2){mu, rl};
            asm volatile("s_waitcnt lgkmcnt(0)" ::: "memory");
        }
    });
}
struct F1Frag { bf16x8 a[4], w[7]; };
__global__ void __launch_bounds__(512) k_ffn1(char* __restrict__ ws) {
    extern __shared__ __attribute__((aligned(16))) char smem[];
    const lptr lds = (lptr)smem;
    const int t = threadIdx.x, lane = t & 63, wid = __builtin_amdgcn_readfirstlane(t >> 6);
    int p, j; tile_map(blockIdx.x, p, j);
    if (wid == 0) STAMP(ws, 4, 0);
    if (wid >= 4) {
        if (wid < 6) f1_loader<4>(ws, p, j, lds, wid - 4, lane);
        else f1_loader<3>(ws, p, j, lds, wid - 4, lane);
        return;
    }
    const int th = wid & 1, nh = wid >> 1, g = lane >> 4;
    f32x4 acc[7][4] = {};
    const LP pa = mk_lp(lds + g * 2048 + (th * 64 + (lane & 15)) * 16), pw = mk_lp(lds + nh * 7168 + lane * 16);
    F1Frag fr[2];
    auto rd = [&]<int S, int I>() {
        constexpr int so = (S % F1_NS) * F1_STAGE;
        if constexpr (S < F1_NST && I < 11) {
            if constexpr (I >= 1 && I <= 4) fr[S & 1].a[I - 1] = lds_at<so + (I - 1) * 256>(pa);
            else fr[S & 1].w[I ? I - 4 : 0] = lds_at<so + 8192 + (I ? I - 4 : 0) * 1024>(pw);
        }
    };
    auto step = [&]<int S>() {
        if constexpr (S % 2 == 0) { lgkm_bar(); SBAR(); }
        if constexpr (ABL != 1)
            [&]<int... U>(std::integer_sequence<int, U...>) {
                ((acc[U / 4][U % 4] = mfma16(fr[S & 1].w[U / 4], fr[S & 1].a[U % 4], acc[U / 4][U % 4]), SBAR(), rd.template operator()<S + 1, U>(), SBAR()), ...);
            }(std::make_integer_sequence<int, 28>{});
    };
    if (wid == 0) STAMP(ws, 4, 1);
    bar();
    if (wid == 0) STAMP(ws, 4, 2);
    [&]<int... I>(std::integer_sequence<int, I...>) { (rd.template operator()<0, I>(), ...); }(std::make_integer_sequence<int, 11>{});
    [&]<int... S>(std::integer_sequence<int, S...>) { (step.template operator()<S>(), ...); }(std::make_integer_sequence<int, F1_NST>{});
    if (wid == 0) STAMP(ws, 4, 3);
    f32x2 st[4];
#pragma unroll
    for (int tb = 0; tb < 4; ++tb) st[tb] = *(const LDSP f32x2*)(lds + F1_AUX + 2048 + (th * 64 + tb * 16 + (lane & 15)) * 8);
    const lptr pu = lds + F1_AUX + (nh * 112 + 4 * g) * 4;
    const unsigned gp = WS_G + p * (F_ / 8 * 2048) + (j * 14 + nh * 7) * 4096 + th * 1024, vo = (g & 1) * 2048 + ((lane >> 5) * 16 + (lane & 15)) * 16;
    f32x4 u = *(const LDSP f32x4*)pu, tt = *(const LDSP f32x4*)(pu + 1024);
    const f32x2 cl = {-1.4426950408889634f, -1.4426950408889634f};
#pragma unroll
    for (int b = 0; b < 7; ++b) {
        f32x4 un, tn;
        if (b < 6) { un = *(const LDSP f32x4*)(pu + (b + 1) * 64); tn = *(const LDSP f32x4*)(pu + 1024 + (b + 1) * 64); }
        unsigned w[4][2];
#pragma unroll
        for (int tb = 0; tb < 4; ++tb)
#pragma unroll
            for (int e = 0; e < 2; ++e) {
                const f32x2 a = __builtin_elementwise_fma((f32x2){u[2 * e], u[2 * e + 1]}, (f32x2){-st[tb][0], -st[tb][0]}, (f32x2){acc[b][tb][2 * e], acc[b][tb][2 * e + 1]});
                const f32x2 z = __builtin_elementwise_fma(a, (f32x2){st[tb][1], st[tb][1]}, (f32x2){tt[2 * e], tt[2 * e + 1]});
                const f32x2 dn = __builtin_elementwise_fma((f32x2){__builtin_amdgcn_exp2f(z[0]), __builtin_amdgcn_exp2f(z[1])}, cl, cl);
                const f32x2 o = z * (f32x2){__builtin_amdgcn_rcpf(dn[0]), __builtin_amdgcn_rcpf(dn[1])};
                w[tb][e] = pk2(o[0], o[1]);
            }
#pragma unroll
        for (int tp = 0; tp < 2; ++tp) {
            const auto s0 = __builtin_amdgcn_permlane32_swap(w[2 * tp][0], w[2 * tp + 1][0], false, false);
            const auto s1 = __builtin_amdgcn_permlane32_swap(w[2 * tp][1], w[2 * tp + 1][1], false, false);
            st16(mk_rsrc(ws), vo + tp * 512, gp + b * 4096, (u32x4){s0[0], s1[0], s0[1], s1[1]});
        }
        u = un; tt = tn;
        SBAR();
    }
    if (wid == 0) STAMP(ws, 4, 4);
}

#define F2_D 6
#define F2_MF 2
constexpr int F2_NS = 10, F2_STAGE = 16384, F2_MAIN = 56 - F2_D, F2_NST = 56 + F2_D;
struct F2Cnt { static constexpr int n(int s) { return s < F2_MAIN ? 4 : 3; } };
struct F2Frag { bf16x8 a[4], w[4]; };
__global__ void __launch_bounds__(512) k_ffn2(char* __restrict__ ws, u16* __restrict__ out) {
    extern __shared__ __attribute__((aligned(16))) char smem[];
    const lptr lds = (lptr)smem;
    const int t = threadIdx.x, lane = t & 63, wid = __builtin_amdgcn_readfirstlane(t >> 6);
    const int b = blockIdx.x, kh = b & 1, p = ((b & 7) >> 1) * 4 + (b >> 6), jn = (b >> 3) & 7;
    if (wid == 0) STAMP(ws, 5, 0);
    if (wid >= 4) {
        const int lw = wid - 4;
        const unsigned ldsb = (unsigned)(uintptr_t)lds;
        const char* sa = ws + WS_G + (size_t)p * (F_ / 8 * 2048) + (kh ? (56 + F2_D) * 8192 : 0);
        const char* sw = ws + WS_W2 + (size_t)jn * 917504 + (kh ? (56 + F2_D) * 8192 : 0);
        const unsigned va = lw * 2048 + lane * 16, vk = ((lw >> 1) * 4 + kh * 2 + (lw & 1)) * 1024 + lane * 16;
        pair_loader<F2_NST, F2_NS, F2Cnt>([&]<int S>() {
            const unsigned ma = ldsb + (S % F2_NS) * F2_STAGE + lw * 2048;
            if constexpr (S == F2_MAIN) { if (kh) { sa -= 112 * 8192 - F2_MAIN * 8192 + 0; sw -= 112 * 8192 - F2_MAIN * 8192 + 0; } }
            if constexpr (ABL == 2) {} else if constexpr (S < F2_MAIN) {
                const unsigned mw = ma + 8192;
                asm volatile(SETM0(ma) GLDS(va, sa, 0) GLDS(va, sa, 1024) SETM0(mw) GLDS(va, sw, 0) GLDS(va, sw, 1024)
                             ::[ma] "s"(ma), [mw] "s"(mw), [va] "v"(va), [sa] "s"(sa), [sw] "s"(sw) : "memory");
            } else {
                const unsigned mw = ldsb + (S % F2_NS) * F2_STAGE + 8192 + lw * 1024;
                asm volatile(SETM0(ma) GLDS(va, sa, 0) GLDS(va, sa, 1024) SETM0(mw) GLDS(vk, sw, 0)
                             ::[ma] "s"(ma), [mw] "s"(mw), [va] "v"(va), [vk] "v"(vk), [sa] "s"(sa), [sw] "s"(sw) : "memory");
            }
            sa += 8192; sw += 8192;
        }, [&]<int M, int PH>() {});
        return;
    }
    const int th = wid & 1, nh = wid >> 1, g = lane >> 4;
    f32x4 acc[4][4] = {};
    const LP pa = mk_lp(lds + g * 2048 + (th * 64 + (lane & 15)) * 16);
    const LP pk = mk_lp(lds + (nh * 4 + kh * 2) * 1024 + lane * 16), ps = mk_lp(lds + (nh * 4 + (kh ^ 1) * 2) * 1024 + lane * 16);
    F2Frag fr[2];
    LP pt;
    auto rd = [&]<int S, int I>() {
        constexpr int so = (S % F2_NS) * F2_STAGE;
        if constexpr (S < F2_NST && I < (S < F2_MAIN ? 8 : 6)) {
            if constexpr (I >= 1 && I <= 4) fr[S & 1].a[I - 1] = lds_at<so + (I - 1) * 256>(pa);
            else if constexpr (I >= 6) fr[S & 1].w[I - 4] = lds_at<so + 8192 + (I - 6) * 1024>(ps);
            else fr[S & 1].w[I ? 1 : 0] = lds_at<so + 8192 + (I ? 1024 : 0)>(S < F2_MAIN ? pk : pt);
        }
    };
    auto step = [&]<int S>() {
        if constexpr (ABL != 1)
            [&]<int... U>(std::integer_sequence<int, U...>) {
                ((acc[U / 4][U % 4] = mfma16(fr[S & 1].w[U / 4], fr[S & 1].a[U % 4], acc[U / 4][U % 4]), SBAR(), rd.template operator()<S + 1, U>(), SBAR()), ...);
            }(std::make_integer_sequence<int, S < F2_MAIN ? 16 : 8>{});
    };
    if (wid == 0) STAMP(ws, 5, 1);
    bar();
    [&]<int... I>(std::integer_sequence<int, I...>) { (rd.template operator()<0, I>(), ...); }(std::make_integer_sequence<int, 8>{});
    static_assert(F2_MAIN % 2 == 0 && F2_D >= 4, "");
    [&]<int... M>(std::integer_sequence<int, M...>) { ((lgkm_bar(), SBAR(), step.template operator()<2 * M>(), step.template operator()<2 * M + 1>()), ...); }
    (std::make_integer_sequence<int, F2_MAIN / 2 - 1>{});
    pt = mk_lp(lds + nh * 2048 + lane * 16);
    lgkm_bar(); SBAR();
    step.template operator()<F2_MAIN - 2>(); step.template operator()<F2_MAIN - 1>();
    if (wid == 0) STAMP(ws, 5, 2);
    const rsrc_t rws = mk_rsrc(ws);
    const unsigned exo = WS_EX + wid * 8192 + lane * 16;
#pragma unroll
    for (int i = 0; i < 8; ++i) st16<16>(rws, exo + i * 1024, b * 32768, __builtin_bit_cast(u32x4, acc[2 + (i >> 2)][i & 3]));
    SBAR();
    __attribute__((address_space(1))) unsigned* flags = (__attribute__((address_space(1))) unsigned*)(ws + WS_FLAG);
    u32x4 xr[4], pr[8]; f32x4 gb[2];
    const int c8 = jn * 16 + nh * 8 + kh * 4 + g;
    TS TT; TCAP(TT, 0);
    auto tail = [&]<int M>() {
        if constexpr (M == F2_MF) wait_vm<0>();
        if constexpr (M == F2_D - 2) {
            if (wid == 0) {
                for (int spin = 0; spin < (1 << 22) && __hip_atomic_load(flags + (b ^ 1) * 32, __ATOMIC_RELAXED, __HIP_MEMORY_SCOPE_AGENT) == 0; ++spin) __builtin_amdgcn_s_sleep(1);
            }
        }
        lgkm_bar(); SBAR();
        if constexpr (M == F2_MF) {
            if (t == 0) __hip_atomic_store(flags + b * 32, 1u, __ATOMIC_RELAXED, __HIP_MEMORY_SCOPE_AGENT);
#pragma unroll
            for (int tb = 0; tb < 4; ++tb) xr[tb] = *(const u32x4*)(ws + WS_X2 + (size_t)p * 262144 + c8 * 2048 + (th * 64 + tb * 16 + (lane & 15)) * 16);
            gb[0] = *(const f32x4*)(ws + WS_MOD + (5120 + c8 * 8) * 4); gb[1] = *(const f32x4*)(ws + WS_MOD + (5120 + c8 * 8 + 4) * 4);
            SBAR();
        }
        if constexpr (M == F2_D - 2) {
#pragma unroll
            for (int i = 0; i < 8; ++i) pr[i] = __builtin_amdgcn_raw_buffer_load_b128(rws, exo + i * 1024 + (b ^ 1) * 32768, 0, 16);
            SBAR();
        }
        step.template operator()<F2_MAIN + 2 * M>(); step.template operator()<F2_MAIN + 2 * M + 1>();
    };
    [&]<int... I>(std::integer_sequence<int, I...>) { (tail.template operator()<I>(), ...); }(std::make_integer_sequence<int, F2_D>{});
    if (wid == 0) STAMP(ws, 5, 3);
#pragma unroll
    for (int tb = 0; tb < 4; ++tb) {
        const f32x4 lo = acc[0][tb] + __builtin_bit_cast(f32x4, pr[tb]), hi = acc[1][tb] + __builtin_bit_cast(f32x4, pr[4 + tb]);
        const u32x4 xv = xr[tb];
        const u32x4 o = {pk2(bflo(xv[0]) + gb[0][0] * lo[0], bfhi(xv[0]) + gb[0][1] * lo[1]), pk2(bflo(xv[1]) + gb[0][2] * lo[2], bfhi(xv[1]) + gb[0][3] * lo[3]),
                         pk2(bflo(xv[2]) + gb[1][0] * hi[0], bfhi(xv[2]) + gb[1][1] * hi[1]), pk2(bflo(xv[3]) + gb[1][2] * hi[2], bfhi(xv[3]) + gb[1][3] * hi[3])};
        st16(mk_rsrc(out), (lane & 15) * (D_ * 2) + c8 * 16, (p * 128 + th * 64 + tb * 16) * (D_ * 2), o);
    }
    if (wid == 0) STAMP(ws, 5, 4);
    if (wid == 0) TDUMP(TT, ws, 7, F2_D + 1);
}

extern "C" void kernel_launch(void* const* d_in, const int* in_sizes, int n_in, void* d_out, int out_size, void* d_ws, size_t ws_size,
                              hipStream_t stream) {
    const u16* x = (const u16*)d_in[0];
    const u16* wq = (const u16*)d_in[1];
    const u16* wk = (const u16*)d_in[2];
    const u16* wv = (const u16*)d_in[3];
    const u16* wo = (const u16*)d_in[4];
    const u16* w1 = (const u16*)d_in[6];
    const u16* w2 = (const u16*)d_in[7];
    const u16* c = (const u16*)d_in[8];
    const u16* wmod = (const u16*)d_in[9];
    char* ws = (char*)d_ws;
    (void)hipFuncSetAttribute((const void*)k_ln_kv, hipFuncAttributeMaxDynamicSharedMemorySize, K2_LDS);
    (void)hipFuncSetAttribute((const void*)k_qattn, hipFuncAttributeMaxDynamicSharedMemorySize, QA_LDS);
    (void)hipFuncSetAttribute((const void*)k_oproj, hipFuncAttributeMaxDynamicSharedMemorySize, OP_NS * G64_STAGE);
    (void)hipFuncSetAttribute((const void*)k_ffn1, hipFuncAttributeMaxDynamicSharedMemorySize, F1_LDS);
    (void)hipFuncSetAttribute((const void*)k_ffn2, hipFuncAttributeMaxDynamicSharedMemorySize, F2_NS * F2_STAGE);
    k_prep<<<256, 512, 32768, stream>>>(c, wmod, wq, ws);
    k_ln_kv<<<256, 512, K2_LDS, stream>>>(x, wk, wv, ws);
    k_qattn<<<256, 512, QA_LDS, stream>>>(wo, w1, w2, ws);
    k_oproj<<<256, 512, OP_NS * G64_STAGE, stream>>>(x, ws);
    k_ffn1<<<256, 512, F1_LDS, stream>>>(ws);
    k_ffn2<<<256, 512, F2_NS * F2_STAGE, stream>>>(ws, (u16*)d_out);
}
```

```cpp
#pragma clang diagnostic ignored "-Wc++20-extensions"
#include <hip/hip_runtime.h>
#include <hip/hip_bf16.h>
#include <stdint.h>

typedef unsigned short u16;
typedef __bf16 bf16x8 __attribute__((ext_vector_type(8)));
typedef __bf16 bf16x2 __attribute__((ext_vector_type(2)));
typedef short s16x4 __attribute__((ext_vector_type(4)));
typedef short s16x8 __attribute__((ext_vector_type(8)));
typedef float f32x16 __attribute__((ext_vector_type(16)));
typedef float f32x4 __attribute__((ext_vector_type(4)));
typedef float f32x2 __attribute__((ext_vector_type(2)));
typedef unsigned u32x4 __attribute__((ext_vector_type(4)));
typedef unsigned u32x2 __attribute__((ext_vector_type(2)));
#define LDSP __attribute__((address_space(3)))
typedef LDSP char* lptr;
#define DI __device__ __forceinline__

constexpr int S_ = 2048, D_ = 1024, F_ = 3584;
constexpr size_t WS_MOD = 0;
constexpr size_t WS_UT = 32768;
constexpr size_t WS_KT = 65536;
constexpr size_t WS_VT = WS_KT + 131072;
constexpr size_t WS_MODP = WS_VT + 131072;
constexpr size_t WS_BUT = WS_MODP + 98304;
constexpr size_t WS_UTP = WS_BUT + 4096;
constexpr size_t WS_H = 1u << 20;
constexpr size_t WS_O = WS_H + (4u << 20);
constexpr size_t WS_X2 = WS_O + (4u << 20);
constexpr size_t WS_Y = WS_X2 + (4u << 20);
constexpr size_t WS_G = WS_Y + (4u << 20);
constexpr size_t WS_WQ = 32u << 20;
constexpr size_t WS_WO = WS_WQ + (2u << 20);
constexpr size_t WS_W1 = WS_WO + (2u << 20);
constexpr size_t WS_W2 = WS_W1 + (7u << 20);
constexpr size_t WS_FLAG = WS_UTP + 131072;
constexpr int FL_EX = 0, FL_O = 32768, FL_Y = 65536, FL_G = 98304, FL_H = 131072, FL_SIDE = 163840, FL_KV = FL_SIDE + 8192, FL_END = FL_KV + 8192;
constexpr size_t WS_ST = 60u << 20;
constexpr size_t WS_EX = 52u << 20;
static_assert(WS_FLAG + FL_END <= WS_H && FL_END % (256 * 4) == 0, "");
static_assert(WS_UTP + 4 * 2 * 3584 * 4 <= WS_H, "");

constexpr size_t WS_DBG = 64u << 20;
#define STAMP(ws, kid, i) do {} while (0)
struct TS {};
#define TCAP(T, i) do {} while (0)
#define TDUMP(T, ws, kid, n) do {} while (0)
DI float bf2f(u16 v) { return __uint_as_float((unsigned)v << 16); }
DI float bflo(unsigned v) { return __uint_as_float(v << 16); }
DI float bfhi(unsigned v) { return __uint_as_float(v & 0xffff0000u); }
DI unsigned pk2(float a, float b) { f32x2 v = {a, b}; return __builtin_bit_cast(unsigned, __builtin_convertvector(v, bf16x2)); }
DI float rbf(float a) { return bflo(pk2(a, 0.f)); }
DI f32x16 mfma32(bf16x8 a, bf16x8 b, f32x16 c) { return __builtin_amdgcn_mfma_f32_32x32x16_bf16(a, b, c, 0, 0, 0); }
DI f32x4 mfma16(bf16x8 a, bf16x8 b, f32x4 c) { return __builtin_amdgcn_mfma_f32_16x16x32_bf16(a, b, c, 0, 0, 0); }
DI bf16x8 lds_b128(lptr p) { return *(const LDSP bf16x8*)p; }
DI s16x4 lds_tr(lptr p) { return __builtin_amdgcn_ds_read_tr16_b64_v4i16((LDSP s16x4*)p); }
DI bf16x8 cat(s16x4 a, s16x4 b) { return __builtin_bit_cast(bf16x8, __builtin_shufflevector(a, b, 0, 1, 2, 3, 4, 5, 6, 7)); }
#define WT 16
typedef __amdgpu_buffer_rsrc_t rsrc_t;
DI rsrc_t mk_rsrc(void* base) { return __builtin_amdgcn_make_buffer_rsrc(base, 0, 0x7fffffff, 0x00020000); }
template <int POL = WT> DI void st16(rsrc_t r, unsigned voff, unsigned soff, u32x4 v) { __builtin_amdgcn_raw_buffer_store_b128(v, r, voff + soff, 0, POL); }
struct LP { lptr b[3]; };
DI LP mk_lp(lptr p) { LP r = {{p, p + 65536, p + 131072}}; asm volatile("" : "+v"(r.b[0]), "+v"(r.b[1]), "+v"(r.b[2])); return r; }
template <int OFF> DI bf16x8 lds_at(const LP& p) { return *(const LDSP bf16x8*)(p.b[OFF >> 16] + (OFF & 65535)); }
#define SBAR() __builtin_amdgcn_sched_barrier(0)
#define PIN(x) asm volatile("" : "+v"(x))
template <int N> DI void wait_vm() { asm volatile("s_waitcnt vmcnt(%0)" ::"i"(N) : "memory"); }
DI void bar() { asm volatile("s_barrier" ::: "memory"); }
DI void lgkm_bar() { asm volatile("s_waitcnt lgkmcnt(0)\n\ts_barrier" ::: "memory"); }
template <int C> DI float dpp_add(float v) { return v + __builtin_bit_cast(float, __builtin_amdgcn_update_dpp(0, __builtin_bit_cast(int, v), C, 0xf, 0xf, true)); }
DI float swap16_add(float v) { const auto r = __builtin_amdgcn_permlane16_swap(__float_as_uint(v), __float_as_uint(v), false, false); return __uint_as_float(r[0]) + __uint_as_float(r[1]); }
DI float swap32_add(float v) { const auto r = __builtin_amdgcn_permlane32_swap(__float_as_uint(v), __float_as_uint(v), false, false); return __uint_as_float(r[0]) + __uint_as_float(r[1]); }
DI float wave_sum(float v) {
    v = dpp_add<0xB1>(v); v = dpp_add<0x4E>(v); v = dpp_add<0x141>(v); v = dpp_add<0x140>(v);
    return swap32_add(swap16_add(v));
}

#define ABL 0
#define AA 0
#define PRIO 1
#define GLDS(v, s, off) "global_load_lds_dwordx4 %[" #v "], %[" #s "] offset:" #off "\n\t"
#define GLD1(v, s, off) "global_load_lds_dwordx4 %[" #v "], %[" #s "] offset:" #off " sc1\n\t"
#define SETM0(m) "s_mov_b32 m0, %[" #m "]\n\ts_nop 0\n\t"

typedef __attribute__((address_space(1))) unsigned* gu32;
DI void flag_st(char* ws, unsigned off, unsigned v) { __hip_atomic_store((gu32)(ws + WS_FLAG + off), v, __ATOMIC_RELAXED, __HIP_MEMORY_SCOPE_AGENT); }
DI u32x4 ld16(rsrc_t r, unsigned off) { return __builtin_amdgcn_raw_buffer_load_b128(r, off, 0, 16); }
DI f32x4 ld16f(rsrc_t r, unsigned off) { return __builtin_bit_cast(f32x4, ld16(r, off)); }
DI void async_ld(u32x4& d, unsigned v, const char* s) { asm volatile("global_load_dwordx4 %0, %1, %2 sc1" : "=v"(d) : "v"(v), "s"(s) : "memory"); }
DI void async_ld(f32x2& d, unsigned v, const char* s) { asm volatile("global_load_dwordx2 %0, %1, %2 sc1" : "=v"(d) : "v"(v), "s"(s) : "memory"); }
DI void touch(unsigned& d, unsigned v, const char* s) { asm volatile("global_load_dword %0, %1, %2 sc1" : "+v"(d) : "v"(v), "s"(s) : "memory"); }
DI void panel_post(char* ws, int fl, int p, int j, int lane) { if (lane < 16) flag_st(ws, fl + (p * 16 + lane) * 128 + j * 4, 1u); }
DI void panel_wait(char* ws, int fl, int p, int j, int lane) {
    const gu32 f = (gu32)(ws + WS_FLAG + fl + (p * 16 + j) * 128) + (lane & 15);
    for (int spin = 0; spin < (1 << 22) && !__all(__hip_atomic_load(f, __ATOMIC_RELAXED, __HIP_MEMORY_SCOPE_AGENT) != 0); ++spin) __builtin_amdgcn_s_sleep(1);
    asm volatile("" ::: "memory");
}
DI void grid_post(char* ws, int fl, unsigned v, int lane) { if (lane < 8) flag_st(ws, fl + lane * 1024 + blockIdx.x * 4, v); }
DI bool grid_ok(u32x4 f, unsigned v) { return __all(f[0] >= v && f[1] >= v && f[2] >= v && f[3] >= v); }
DI void grid_wait(char* ws, int fl, unsigned v, int lane) {
    for (int spin = 0; spin < (1 << 22) && !grid_ok(ld16(mk_rsrc(ws), WS_FLAG + fl + (blockIdx.x & 7) * 1024 + lane * 16), v); ++spin) { __builtin_amdgcn_s_sleep(1); asm volatile("" ::: "memory"); }
    asm volatile("" ::: "memory");
}

DI void tile_issue(const u16* src, int ld, unsigned ldsb, int lane) {
    const unsigned vo = (unsigned)((lane >> 3) * ld * 2 + (lane & 7) * 16);
#pragma unroll
    for (int i = 0; i < 4; ++i) {
        const u16* s = src + (size_t)i * 8 * ld;
        const unsigned m = ldsb + i * 1024;
        asm volatile(SETM0(m) GLDS(v, s, 0)::[m] "s"(m), [v] "v"(vo), [s] "s"(s) : "memory");
    }
}
template <bool PERM> DI void tile_store32(lptr st, rsrc_t r, unsigned dst, int lane) {
    const lptr p = st + ((PERM ? 4 : 8) * (lane >> 5) + ((lane & 15) >> 2)) * 128 + ((lane >> 4) & 1) * 32 + (lane & 3) * 8;
    bf16x8 f[4];
#pragma unroll
    for (int i = 0; i < 4; ++i) { const lptr q = p + (i >> 1) * 2048 + (i & 1) * 64; f[i] = cat(lds_tr(q), lds_tr(q + (PERM ? 1024 : 512))); }
#pragma unroll
    for (int i = 0; i < 4; ++i) st16(r, i * 1024 + lane * 16, dst, __builtin_bit_cast(u32x4, f[i]));
}

__global__ void __launch_bounds__(512) k_prep(const u16* __restrict__ c, const u16* __restrict__ wmod, const u16* __restrict__ wq, char* __restrict__ ws) {
    extern __shared__ __attribute__((aligned(16))) char smem[];
    const lptr lds = (lptr)smem;
    const int t = threadIdx.x, lane = t & 63, wid = __builtin_amdgcn_readfirstlane(t >> 6), b = blockIdx.x;
    if (wid == 0) STAMP(ws, 0, 0);
    if (t < FL_END / 1024) *(unsigned*)(ws + WS_FLAG + (b * (FL_END / 1024) + t) * 4) = 0u;
    if (b >= 192) {
        const int j = (b - 192) & 15, kg = (b - 192) >> 4;
        tile_issue(wq + (size_t)(kg * 256 + wid * 32) * D_ + j * 64, D_, (unsigned)(uintptr_t)lds + wid * 4096, lane);
        wait_vm<0>();
        tile_store32<false>(lds + wid * 4096, mk_rsrc(ws), WS_WQ + j * 131072 + (kg * 8 + wid) * 4096, lane);
        if (wid == 0) STAMP(ws, 0, 1);
        return;
    }
    const int cg = b % 48, kg = b / 48, ch = t & 15, rg = t >> 4;
    u32x4 w[8]; u16 cv[8];
#pragma unroll
    for (int i = 0; i < 8; ++i) w[i] = __builtin_nontemporal_load((const u32x4*)(wmod + (size_t)(kg * 256 + i * 32 + rg) * 6144 + cg * 128 + ch * 8));
#pragma unroll
    for (int i = 0; i < 8; ++i) cv[i] = c[kg * 256 + i * 32 + rg];
    float acc[8] = {0, 0, 0, 0, 0, 0, 0, 0};
#pragma unroll
    for (int i = 0; i < 8; ++i) {
        const float f = bf2f(cv[i]);
        const float s = rbf(f / (1.f + __expf(-f)));
#pragma unroll
        for (int j = 0; j < 4; ++j) { acc[2 * j] += s * bflo(w[i][j]); acc[2 * j + 1] += s * bfhi(w[i][j]); }
    }
#pragma unroll
    for (int j = 0; j < 8; ++j) acc[j] = swap32_add(swap16_add(acc[j]));
    if (lane < 16) {
        *(LDSP f32x4*)(lds + wid * 512 + ch * 32) = (f32x4){acc[0], acc[1], acc[2], acc[3]};
        *(LDSP f32x4*)(lds + wid * 512 + ch * 32 + 16) = (f32x4){acc[4], acc[5], acc[6], acc[7]};
    }
    lgkm_bar();
    if (t < 128) {
        float v = 0.f;
#pragma unroll
        for (int i = 0; i < 8; ++i) v += *(const LDSP float*)(lds + i * 512 + t * 4);
        ((float*)(ws + WS_MODP))[kg * 6144 + cg * 128 + t] = v;
    }
    if (wid == 0) STAMP(ws, 0, 1);
}

constexpr int K2_W = 32768, K2_MOD = K2_W + 65536, K2_RED = K2_MOD + 8192, K2_LDS = K2_RED + 16384;
__global__ void __launch_bounds__(512) k_ln_kv(const u16* __restrict__ x, const u16* __restrict__ wk, const u16* __restrict__ wv, char* __restrict__ ws) {
    extern __shared__ __attribute__((aligned(16))) char smem[];
    const lptr lds = (lptr)smem;
    const int t = threadIdx.x, lane = t & 63, wid = __builtin_amdgcn_readfirstlane(t >> 6);
    const int b = blockIdx.x, tg = ((b >> 4) << 3) | (b & 7), which = (b >> 3) & 1;
    const float* modp = (const float*)(ws + WS_MODP);
    if (wid == 0) STAMP(ws, 1, 0);
    u32x4 xr[2][2]; f32x4 mp[4];
    {
        const u16* xp = x + (size_t)(tg * 16 + wid * 2) * D_;
        const unsigned vx = lane * 16, vm = t * 16;
        asm volatile("global_load_dwordx4 %0, %8, %10\n\tglobal_load_dwordx4 %1, %8, %10 offset:1024\n\t"
                     "global_load_dwordx4 %2, %8, %10 offset:2048\n\tglobal_load_dwordx4 %3, %8, %10 offset:3072\n\t"
                     "global_load_dwordx4 %4, %9, %11\n\tglobal_load_dwordx4 %5, %9, %12\n\tglobal_load_dwordx4 %6, %9, %13\n\tglobal_load_dwordx4 %7, %9, %14"
                     : "=&v"(xr[0][0]), "=&v"(xr[0][1]), "=&v"(xr[1][0]), "=&v"(xr[1][1]), "=&v"(mp[0]), "=&v"(mp[1]), "=&v"(mp[2]), "=&v"(mp[3])
                     : "v"(vx), "v"(vm), "s"(xp), "s"(modp), "s"(modp + 6144), "s"(modp + 2 * 6144), "s"(modp + 3 * 6144) : "memory");
    }
    float mf[4] = {0, 0, 0, 0};
    if (t < 24) {
#pragma unroll
        for (int i = 0; i < 4; ++i) mf[i] = modp[i * 6144 + b * 24 + t];
    }
    {
        const u16* W = (which ? wv : wk) + wid * 4096;
        const int row = lane >> 2, chn = (lane & 3) ^ (((row >> 3) & 1) << 1);
        const unsigned vo = (unsigned)(row * 64 + chn * 16);
#pragma unroll
        for (int q = 0; q < 2; ++q) {
            const unsigned m = (unsigned)(uintptr_t)lds + K2_W + wid * 8192 + q * 4096;
            const u16* sb_ = W + q * 2048;
            asm volatile(SETM0(m) GLDS(v, s, 0) GLDS(v, s, 1024) GLDS(v, s, 2048) GLDS(v, s, 3072)::[m] "s"(m), [v] "v"(vo), [s] "s"(sb_) : "memory");
        }
    }
    asm volatile("s_waitcnt vmcnt(8)" : "+v"(xr[0][0]), "+v"(xr[0][1]), "+v"(xr[1][0]), "+v"(xr[1][1]), "+v"(mp[0]), "+v"(mp[1]), "+v"(mp[2]), "+v"(mp[3])::"memory");
    {
        f32x4 m = (mp[0] + mp[1]) + (mp[2] + mp[3]);
#pragma unroll
        for (int e = 0; e < 4; ++e) { m[e] = rbf(m[e]); if (t >= 256) m[e] = rbf(1.f + m[e]); }
        *(LDSP f32x4*)(lds + K2_MOD + t * 16) = m;
    }
    float v[2][16], rstd[2];
#pragma unroll
    for (int r = 0; r < 2; ++r) {
#pragma unroll
        for (int h = 0; h < 2; ++h)
#pragma unroll
            for (int j = 0; j < 4; ++j) { v[r][h * 8 + 2 * j] = bflo(xr[r][h][j]); v[r][h * 8 + 2 * j + 1] = bfhi(xr[r][h][j]); }
        float s = 0;
#pragma unroll
        for (int j = 0; j < 16; ++j) s += v[r][j];
        const float mean = wave_sum(s) * (1.f / 1024.f);
        float q = 0;
#pragma unroll
        for (int j = 0; j < 16; ++j) { v[r][j] -= mean; q += v[r][j] * v[r][j]; }
        rstd[r] = __builtin_amdgcn_rsqf(wave_sum(q) * (1.f / 1024.f) + 1e-5f);
    }
    if (wid == 0) STAMP(ws, 1, 1);
    lgkm_bar();
#pragma unroll
    for (int h = 0; h < 2; ++h) {
        f32x4 sa[2], a1[2];
#pragma unroll
        for (int q = 0; q < 2; ++q) {
            sa[q] = *(const LDSP f32x4*)(lds + K2_MOD + (h * 512 + lane * 8 + q * 4) * 4);
            a1[q] = *(const LDSP f32x4*)(lds + K2_MOD + 4096 + (h * 512 + lane * 8 + q * 4) * 4);
        }
#pragma unroll
        for (int r = 0; r < 2; ++r) {
            u32x4 o;
#pragma unroll
            for (int j = 0; j < 4; ++j) {
                const int e = 2 * j;
                o[j] = pk2(v[r][h * 8 + e] * rstd[r] * a1[e >> 2][e & 3] + sa[e >> 2][e & 3], v[r][h * 8 + e + 1] * rstd[r] * a1[e >> 2][(e & 3) + 1] + sa[e >> 2][(e & 3) + 1]);
            }
            const int kc = h * 64 + lane;
            *(LDSP u32x4*)(lds + kc * 256 + (((wid * 2 + r) ^ (kc & 7)) * 16)) = o;
        }
    }
    wait_vm<0>();
    lgkm_bar();
    if (wid == 0) STAMP(ws, 1, 2);
    if (t < 24) {
        const int col = b * 24 + t, part = col >> 10, k = col & 1023;
        float v = rbf((mf[0] + mf[1]) + (mf[2] + mf[3]));
        if (part == 1 || part == 4) v = rbf(1.f + v);
        ((float*)(ws + WS_MOD))[col] = v;
        if (part == 3 || part == 4)
            ((u16*)(ws + WS_BUT))[(((k >> 5) * 2 + (part == 3)) * 4 + 2 * ((k >> 4) & 1) + ((k >> 2) & 1)) * 8 + (k & 3) + 4 * ((k >> 3) & 1)] = (u16)(__float_as_uint(v) >> 16);
    }
    {
        const unsigned hAT = WS_H + (tg >> 3) * 262144 + (tg & 7) * 256;
#pragma unroll
        for (int i = 0; i < 2; ++i) {
            const int kc = which * 64 + i * 32 + (t >> 4), tk = t & 15;
            st16(mk_rsrc(ws), kc * 2048 + tk * 16, hAT, *(const LDSP u32x4*)(lds + kc * 256 + ((tk ^ (kc & 7)) * 16)));
        }
    }
    f32x4 acc[2] = {{0, 0, 0, 0}, {0, 0, 0, 0}};
    {
        const int g = lane >> 4, tk = lane & 15, q = (lane & 15) >> 2, p = lane & 3;
#pragma unroll
        for (int s = 0; s < 4; ++s) {
            const int kc = wid * 16 + s * 4 + g;
            const bf16x8 hf = lds_b128(lds + kc * 256 + ((tk ^ (kc & 7)) * 16));
            const int row = kc * 8 + q;
#pragma unroll
            for (int nb = 0; nb < 2; ++nb) {
                const lptr wp = lds + K2_W + row * 64 + ((nb ^ (kc & 1)) * 32) + p * 8;
                const bf16x8 wf = cat(lds_tr(wp), lds_tr(wp + 256));
                acc[nb] = which ? mfma16(hf, wf, acc[nb]) : mfma16(wf, hf, acc[nb]);
            }
        }
    }
#pragma unroll
    for (int nb = 0; nb < 2; ++nb) *(LDSP f32x4*)(lds + K2_RED + ((wid * 2 + nb) * 64 + lane) * 16) = acc[nb];
    lgkm_bar();
    if (t < 128) {
        const int nb = t >> 6;
        f32x4 r = *(const LDSP f32x4*)(lds + K2_RED + (nb * 64 + lane) * 16);
#pragma unroll
        for (int w = 1; w < 8; ++w) r += *(const LDSP f32x4*)(lds + K2_RED + ((w * 2 + nb) * 64 + lane) * 16);
        u32x2 o = {pk2(r[0], r[1]), pk2(r[2], r[3])};
        const int g = lane >> 4, c = lane & 15, kt = tg >> 1;
        if (which) {
            *(u32x2*)(ws + WS_VT + kt * 2048 + ((tg & 1) * 2 + (g & 1)) * 512 + (nb * 16 + c) * 16 + (g >> 1) * 8) = o;
        } else {
            *(u32x2*)(ws + WS_KT + kt * 2048 + (nb * 2 + (g & 1)) * 512 + ((tg & 1) * 16 + c) * 16 + (g >> 1) * 8) = o;
        }
    }
    if (wid == 0) STAMP(ws, 1, 3);
}

#define R0 3
constexpr int ring_issued(int i, int NST, int NS) { int n = R0 < NS - 1 ? R0 : NS - 1; for (int k = 0; k < i; ++k) { n += 2; if (n > k + NS) n = k + NS; if (n > NST) n = NST; } return n; }
template <int I> DI void tcap(TS& T) { TCAP(T, I); }
struct NoEx { static constexpr int n(int) { return 0; } };
template <int NST, int NS, class CNT, class EX, class F, class X>
DI void ring_loader(F issue, X extra) {
    auto range = [&]<int S0, int... I>(std::integer_sequence<int, I...>) { (issue.template operator()<S0 + I>(), ...); };
    auto sync = [&]<int I>() {
        constexpr int have = ring_issued(I, NST, NS);
        constexpr int out = [] { int c = 0; for (int s = I + 1; s < have; ++s) c += CNT::n(s); for (int i = 0; i < I; ++i) c += EX::n(i); return c; }();
        static_assert(out < 64 && I < have && (EX::n(I) == 0 || ring_issued(I + 1, NST, NS) == NST), "");
        wait_vm<out>();
        bar();
        range.template operator()<have>(std::make_integer_sequence<int, ring_issued(I + 1, NST, NS) - have>{});
        extra.template operator()<I>();
    };
    range.template operator()<0>(std::make_integer_sequence<int, ring_issued(0, NST, NS)>{});
    [&]<int... I>(std::integer_sequence<int, I...>) { (sync.template operator()<I>(), ...); }(std::make_integer_sequence<int, NST>{});
}
constexpr int cmin(int a, int b) { return a < b ? a : b; }
constexpr int pair_issued(int m, int NST, int NS) {
    int n = 3;
    for (int k = -1; k < m; ++k) n = cmin(cmin(n + 4, k < 0 ? NS : 2 * k + 1 + NS), NST);
    return n;
}
template <int NST, int NS, class CNT, class EX, class F, class X>
DI void pair_loader(F issue, X extra) {
    auto range = [&]<int S0, int... I>(std::integer_sequence<int, I...>) { (issue.template operator()<S0 + I>(), ...); };
    auto sync = [&]<int M>() {
        constexpr int need = M < 0 ? 0 : cmin(2 * M + 2, NST - 1), have = pair_issued(M, NST, NS);
        constexpr int out = [] { int c = 0; for (int s = need + 1; s < have; ++s) c += CNT::n(s); for (int i = 0; i < M; ++i) c += EX::n(i); return c; }();
        static_assert(out < 64 && need < have && (EX::n(M) == 0 || pair_issued(M + 1, NST, NS) == NST), "");
        wait_vm<out>();
        extra.template operator()<M, 0>();
        bar();
        range.template operator()<have>(std::make_integer_sequence<int, pair_issued(M + 1, NST, NS) - have>{});
        extra.template operator()<M, 1>();
    };
    range.template operator()<0>(std::make_integer_sequence<int, 3>{});
    [&]<int... I>(std::integer_sequence<int, I...>) { (sync.template operator()<I - 1>(), ...); }(std::make_integer_sequence<int, NST / 2 + 1>{});
}
constexpr int G64_STAGE = 24576;
template <int PRE> struct G64Cnt { static constexpr int n(int s) { return s < PRE ? 4 : 6; } };
DI void g64_w(const char* sw, unsigned ldsb, int stage, int slot, int lw, int lane) {
    const unsigned vw = lw * 2048 + lane * 16, mw = ldsb + slot * G64_STAGE + 16384 + lw * 2048;
    const char* s = sw + stage * 8192;
    if (ABL != 2) asm volatile(SETM0(mw) GLD1(vw, s, 0) GLD1(vw, s, 1024)::[mw] "s"(mw), [vw] "v"(vw), [s] "s"(s) : "memory");
}
template <int NST, int NS, int PRE, class EX, class X>
DI void g64_loader(const char* sa, const char* sw, unsigned ldsb, int lw, int lane, X extra) {
    const unsigned va = lw * 4096 + lane * 16;
    ring_loader<NST, NS, G64Cnt<PRE>, EX>([&]<int S>() {
        const unsigned ma = ldsb + (S % NS) * G64_STAGE + lw * 4096;
        if (ABL != 2) asm volatile(SETM0(ma) GLD1(va, sa, 0) GLD1(va, sa, 1024) GLD1(va, sa, 2048) GLD1(va, sa, 3072)::[ma] "s"(ma), [va] "v"(va), [sa] "s"(sa) : "memory");
        if constexpr (S >= PRE) g64_w(sw, ldsb, S, S % NS, lw, lane);
        sa += 16384;
    }, extra);
}

struct G64Frag { bf16x8 a[4][2], w[4]; };
template <int NST, int NS, class H>
DI void g64_compute(lptr lds, int cw, int lane, f32x16 (&acc)[2], H hook) {
    const LP pa = mk_lp(lds + (lane >> 5) * 2048 + ((cw & 1) * 64 + (lane & 31)) * 16), pw = mk_lp(lds + (cw >> 1) * 1024 + lane * 16);
    G64Frag fr[2];
    auto rd = [&]<int S, int I>() {
        constexpr int so = (S % NS) * G64_STAGE;
        if constexpr (S < NST && I < 12) {
            if constexpr (I % 3 == 0) fr[S & 1].w[I / 3] = lds_at<so + 16384 + (I / 3) * 2048>(pw);
            else fr[S & 1].a[I / 3][I % 3 - 1] = lds_at<so + (I / 3) * 4096 + (I % 3 - 1) * 512>(pa);
        }
    };
    auto step = [&]<int S>() {
        if constexpr (S + 1 < NST) { lgkm_bar(); SBAR(); }
        if constexpr (ABL != 1)
            [&]<int... U>(std::integer_sequence<int, U...>) {
                ((acc[U % 2] = mfma32(fr[S & 1].w[U / 2], fr[S & 1].a[U / 2][U % 2], acc[U % 2]), SBAR(),
                  rd.template operator()<S + 1, 2 * U>(), rd.template operator()<S + 1, 2 * U + 1>(), SBAR()), ...);
            }(std::make_integer_sequence<int, 8>{});
        hook(S);
    };
    bar();
    [&]<int... I>(std::integer_sequence<int, I...>) { (rd.template operator()<0, I>(), ...); }(std::make_integer_sequence<int, 12>{});
    hook(-1);
    [&]<int... S>(std::integer_sequence<int, S...>) { (step.template operator()<S>(), ...); }(std::make_integer_sequence<int, NST>{});
}
DI void tile_map(int b, int& p, int& j) { const int x = b & 7, i = b >> 3; p = (x >> 1) * 4 + (i >> 3); j = (x & 1) * 8 + (i & 7); }

constexpr int QA_NS = 4;
constexpr int QA_KV0 = QA_NS * G64_STAGE;
constexpr int QA_LDS = QA_KV0 + 65536;
constexpr float QSCALE = 0.17677669529663687f * 1.4426950408889634f;

constexpr int OP_NS = 6;
#define OPRE 4
#define PREW 0
#define PROG 1
DI void ph_qattn(const u16* __restrict__ wo, const u16* __restrict__ w1, const u16* __restrict__ w2, char* __restrict__ ws, lptr lds, int lane, int wid, int p, int j, TS& TL) {
    const unsigned ldsb = (unsigned)(uintptr_t)lds;
    const char* KT = ws + WS_KT; const char* VT = ws + WS_VT;
    const unsigned vkv = lane * 16;
    auto kv_piece = [&](int chunk, unsigned buf, int w, int i) {
        const char* s = ((i & 4) ? VT : KT) + chunk * 32768 + w * 4096 + (i & 3) * 1024;
        const unsigned m = ldsb + buf + (i >> 2) * 32768 + w * 4096 + (i & 3) * 1024;
        asm volatile(SETM0(m) GLDS(v, s, 0)::[m] "s"(m), [v] "v"(vkv), [s] "s"(s) : "memory");
    };
    const int b = blockIdx.x;
    const bool big = b < 224;
    const rsrc_t rws = mk_rsrc(ws);
    const lptr stg = lds + 65536 + wid * 4096;
    const int jA = b & 15, gA = big ? (b >> 4) : ((b - 224) >> 4);
    const int gB = big ? (b / 56) : 2 + ((b - 224) >> 4), cB = big ? (b % 56) : jA;
    bf16x8 but = {};
    if (big && (lane & 15) < 2) but = *(const bf16x8*)(ws + WS_BUT + (((gB * 8 + wid) * 2 + (lane & 15)) * 4 + (lane >> 4)) * 16);
    if (wid == 0) STAMP(ws, 2, 0);
    if (wid >= 4) {
        g64_loader<16, QA_NS, 0, NoEx>(ws + WS_H + (size_t)p * 262144, ws + WS_WQ + (size_t)j * 131072, ldsb, wid - 4, lane, []<int I>() {});
        bar();
    } else {
        f32x16 r[2] = {};
        g64_compute<16, QA_NS>(lds, wid, lane, r, [&](int i) {
            if (i >= 0 && i < 8) { kv_piece(0, QA_KV0, wid, i); kv_piece(0, QA_KV0, wid + 4, i); }
        });
        if (wid == 0) STAMP(ws, 2, 1);
#pragma unroll
        for (int tb = 0; tb < 2; ++tb)
#pragma unroll
            for (int s = 0; s < 2; ++s) {
                u32x4 o;
#pragma unroll
                for (int i = 0; i < 4; ++i) o[i] = pk2(r[tb][8 * s + 2 * i] * QSCALE, r[tb][8 * s + 2 * i + 1] * QSCALE);
                *(LDSP u32x4*)(lds + 32768 + ((wid * 2 + tb) * 2 + s) * 1024 + lane * 16) = o;
            }
        wait_vm<0>();
        lgkm_bar();
    }
    const int qg = wid & 3, kh = wid >> 2;
    const int lo = kh * 16384 + lane * 16;
    bf16x8 qA0, qA1, qB0, qB1, k0, k1, v0, v1;
    {
        const lptr qp = lds + 32768 + qg * 4096 + lane * 16;
        qA0 = lds_b128(qp); qA1 = lds_b128(qp + 1024); qB0 = lds_b128(qp + 2048); qB1 = lds_b128(qp + 3072);
        k0 = lds_b128(lds + QA_KV0 + lo); k1 = lds_b128(lds + QA_KV0 + lo + 1024);
    }
    bf16x8 sel;
    {
        const unsigned on = ((lane & 15) == ((lane >> 4) & 1)) ? 0x3f803f80u : 0u;
        sel = __builtin_bit_cast(bf16x8, (u32x4){on, on, on, on});
    }
    f32x16 oA = {}, oB = {}, sA, sB;
    f32x4 lA = {}, lB = {};
    const f32x16 zero = {};
    u32x4 pA0, pA1, pB0 = {0, 0, 0, 0}, pB1 = {0, 0, 0, 0};
    PIN(but);
    lgkm_bar();
    if (wid == 0) STAMP(ws, 2, 2);
#define EXP2(E, d, S, i) do { E[d] = __builtin_amdgcn_exp2f(S[i]); E[d + 1] = __builtin_amdgcn_exp2f(S[i + 1]); PIN(E); } while (0)
#define EXP4(E, S, i) do { E[0] = __builtin_amdgcn_exp2f(S[i]); E[1] = __builtin_amdgcn_exp2f(S[i + 1]); E[2] = __builtin_amdgcn_exp2f(S[i + 2]); E[3] = __builtin_amdgcn_exp2f(S[i + 3]); } while (0)
#define PK2(P, d, E) do { P[d] = pk2(E[0], E[1]); P[d + 1] = pk2(E[2], E[3]); PIN(P); } while (0)
#define BF(x) __builtin_bit_cast(bf16x8, x)
#define HALF(SX, PX0, PX1, SY, QY0, QY1, OZ, LZ, PZ0, PZ1, R0, R1, R2, R3) do { \
        SY = mfma32(k0, QY0, zero); SBAR(); PK2(PZ1, 2, e); R0; SBAR(); EXP4(e, SX, 0); PIN(e); SBAR(); \
        SY = mfma32(k1, QY1, SY); SBAR(); PK2(PX0, 0, e); R1; SBAR(); EXP2(e, 0, SX, 4); SBAR(); LZ = mfma16(sel, BF(PZ0), LZ); SBAR(); EXP2(e, 2, SX, 6); SBAR(); \
        OZ = mfma32(v0, BF(PZ0), OZ); SBAR(); PK2(PX0, 2, e); R2; SBAR(); EXP4(e, SX, 8); PIN(e); SBAR(); \
        OZ = mfma32(v1, BF(PZ1), OZ); SBAR(); PK2(PX1, 0, e); R3; SBAR(); EXP2(e, 0, SX, 12); SBAR(); LZ = mfma16(sel, BF(PZ1), LZ); SBAR(); EXP2(e, 2, SX, 14); SBAR(); } while (0)
    sA = mfma32(k0, qA0, zero); sA = mfma32(k1, qA1, sA);
    v0 = k0; v1 = k1;
    f32x4 e = {0, 0, 0, 0};
    bf16x8 w0, w1_, sf[4];
    f32x4 ut[4];
    u32x4 sfl;
    unsigned junk = 0;
    const lptr tp32 = stg + (4 * (lane >> 5) + ((lane & 15) >> 2)) * 128 + ((lane >> 4) & 1) * 32 + (lane & 3) * 8;
    const lptr tp16 = stg + (16 * (lane >> 5) + 4 * ((lane >> 4) & 1) + ((lane & 15) >> 2)) * 128 + (lane & 3) * 8;
    unsigned dstA = big ? WS_W2 + (jA >> 1) * 917504 + (gA * 8 + wid) * 8192 + (jA & 1) * 4096 : WS_WO + jA * 131072 + (gA * 8 + wid) * 4096, dstB[4];
#pragma unroll
    for (int i = 0; i < 4; ++i) {
        const int nbk = cB * 4 + i, jt = nbk / 14;
        dstB[i] = big ? WS_W1 + jt * 458752 + (gB * 8 + wid) * 14336 + (nbk - jt * 14) * 1024 : WS_WO + cB * 131072 + (gB * 8 + wid) * 4096 + i * 1024;
        asm volatile("" : "+s"(dstB[i]));
    }
    const int ldB = big ? F_ : D_;
    const u16* srcA = (big ? w2 : wo) + jA * 64 + (size_t)(gA * 256 + wid * 32) * D_;
    const u16* srcB = (big ? w1 : wo) + cB * 64 + (size_t)(gB * 256 + wid * 32) * ldB;
    asm volatile("" : "+s"(dstA), "+s"(srcA), "+s"(srcB));
    auto side = [&]<int C, int IT, int I>() {
        if constexpr (C < 3 && IT < 2 && !(AA & 2)) kv_piece(C + 1, (C & 1) ? QA_KV0 : 0, wid, IT * 4 + I);
        if constexpr (AA & 1) return;
        if constexpr ((C == 0 && IT == 2) || (C == 1 && IT == 4)) {
            const int ld = C == 0 ? D_ : ldB;
            const u16* s = (C == 0 ? srcA : srcB) + I * 8 * ld;
            const unsigned vo = (unsigned)((lane >> 3) * ld * 2 + (lane & 7) * 16), m = ldsb + 65536 + wid * 4096 + I * 1024;
            asm volatile(SETM0(m) GLDS(v, s, 0)::[m] "s"(m), [v] "v"(vo), [s] "s"(s) : "memory");
        }
        if constexpr ((C == 1 || C == 2) && IT == 2) {
            if constexpr (I == 0) wait_vm<8>();
            const lptr q = !big ? tp32 + (I >> 1) * 2048 + (I & 1) * 64 : (C == 2 ? tp16 + I * 32 : tp16 + (lane & 3) * 8 + (I >> 1) * 64 + (I & 1) * 8);
            sf[I] = cat(lds_tr(q), lds_tr(q + 1024));
        }
        if constexpr ((C == 1 || C == 2) && IT == 3) {
            const unsigned dst = C == 1 ? dstA + I * 1024 : dstB[I];
            if constexpr (C == 2) ut[I] = mfma16(sf[I], but, (f32x4){0, 0, 0, 0});
            st16(rws, lane * 16, dst, __builtin_bit_cast(u32x4, sf[I]));
        }
        if constexpr (C == 2 && IT == 4) *(LDSP f32x4*)(stg + I * 1024 + lane * 16) = ut[I];
        if constexpr (PREW && C == 3 && IT == 2 && I < 2)
            touch(junk, (I * 64 + lane < 112 ? I * 64 + lane : 0) * 64, ws + WS_G + p * (F_ / 8 * 2048) + j * 57344 + wid * 7168);
        if constexpr (C == 3 && IT == 0 && I == 0) { if (wid == 0) grid_post(ws, FL_SIDE, 1u, lane); }
        if constexpr (C == 3 && IT == 0) {
            if (wid == 0) {
                const lptr q = lds + 65536 + I * 8192 + (lane >> 4) * 1024 + ((lane >> 2) & 3) * 256 + (lane & 3) * 4;
                ut[I] = (f32x4){*(const LDSP float*)q, *(const LDSP float*)(q + 16), *(const LDSP float*)(q + 4096), *(const LDSP float*)(q + 4096 + 16)};
            }
        }
        if constexpr (C == 3 && IT == 1 && I == 1) {
            if (wid == 0 && big) {
                const f32x4 a = (ut[0] + ut[1]) + (ut[2] + ut[3]);
#pragma unroll
                for (int i = 0; i < 2; ++i) __builtin_amdgcn_raw_buffer_store_b32(__float_as_uint(a[i] + a[2 + i]), rws, WS_UTP + ((gB * 2 + i) * F_ + cB * 64 + lane) * 4, 0, 16);
            }
        }
        if constexpr (C == 3 && IT == 6 && I == 0) {
            if (wid == 0) { wait_vm<0>(); grid_post(ws, FL_SIDE, 2u, lane); }
            async_ld(sfl, lane * 16, ws + WS_FLAG + FL_SIDE + (blockIdx.x & 7) * 1024);
        }
    };
#define SIDE(I) side.template operator()<C, IT, I>()
    auto step = [&]<int C, int IT>() {
        const lptr kp = lds + ((C & 1) ? 0 : QA_KV0) + lo, kn = lds + ((C & 1) ? QA_KV0 : 0) + lo;
        if constexpr (PRIO && IT == 5) __builtin_amdgcn_s_setprio(2);
        if constexpr (PRIO && IT == 6) __builtin_amdgcn_s_setprio(1);
        if constexpr (IT < 7) {
            HALF(sA, pA0, pA1, sB, qB0, qB1, oB, lB, pB0, pB1, SIDE(0), k0 = lds_b128(kp + (IT + 1) * 2048),
                 k1 = lds_b128(kp + (IT + 1) * 2048 + 1024), v0 = lds_b128(kp + 32768 + IT * 2048));
            if constexpr (PRIO && IT == 6) __builtin_amdgcn_s_setprio(0);
            HALF(sB, pB0, pB1, sA, qA0, qA1, oA, lA, pA0, pA1, v1 = lds_b128(kp + 32768 + IT * 2048 + 1024),
                 SIDE(1); if constexpr (IT == 6) w0 = lds_b128(kp + 32768 + 7 * 2048), SIDE(2); if constexpr (IT == 6) w1_ = lds_b128(kp + 32768 + 7 * 2048 + 1024), SIDE(3));
        } else {
            if constexpr (C < 3 && !(AA & 4)) {
                wait_vm<(AA & 1) || C == 2 ? 0 : (C == 1 ? 8 : 4)>();
                lgkm_bar();
                if constexpr (PRIO) __builtin_amdgcn_s_setprio(3);
            }
            HALF(sA, pA0, pA1, sB, qB0, qB1, oB, lB, pB0, pB1, (void)0, if constexpr (C < 3) k0 = lds_b128(kn), if constexpr (C < 3) k1 = lds_b128(kn + 1024), (void)0);
            v0 = w0; v1 = w1_;
            HALF(sB, pB0, pB1, sA, qA0, qA1, oA, lA, pA0, pA1, (void)0, (void)0, (void)0, (void)0);
        }
    };
    if constexpr (PRIO) __builtin_amdgcn_s_setprio(3);
    [&]<int... N>(std::integer_sequence<int, N...>) { (step.template operator()<N / 8, N % 8>(), ...); }(std::make_integer_sequence<int, 32>{});
    PK2(pB1, 2, e);
    oB = mfma32(v0, BF(pB0), oB); oB = mfma32(v1, BF(pB1), oB);
    lB = mfma16(sel, BF(pB0), lB); lB = mfma16(sel, BF(pB1), lB);
    PIN(lA); PIN(lB); PIN(oA); PIN(oB);
    if (wid == 0) STAMP(ws, 2, 3);
    const lptr ex = lds + QA_KV0;
    {
        const f32x16& so = kh ? oA : oB;
        const f32x4& sl = kh ? lA : lB;
#pragma unroll
        for (int i = 0; i < 4; ++i) *(LDSP f32x4*)(ex + wid * 4096 + i * 1024 + lane * 16) = (f32x4){so[4 * i], so[4 * i + 1], so[4 * i + 2], so[4 * i + 3]};
        const f32x4& ml = kh ? lB : lA;
        if (lane < 16) {
            *(LDSP float*)(ex + 32768 + wid * 256 + lane * 4) = sl[0]; *(LDSP float*)(ex + 32768 + wid * 256 + 64 + lane * 4) = sl[1];
            *(LDSP float*)(ex + 32768 + wid * 256 + 128 + lane * 4) = ml[0]; *(LDSP float*)(ex + 32768 + wid * 256 + 192 + lane * 4) = ml[1];
        }
    }
    lgkm_bar();
    tcap<0>(TL);
    asm volatile("s_waitcnt vmcnt(0)" : "+v"(sfl), "+v"(junk)::"memory");
    if (wid >= 4) {
        if (!grid_ok(sfl, 1u)) grid_wait(ws, FL_SIDE, 1u, lane);
#pragma unroll
        for (int i = 0; i < OPRE; ++i) g64_w(ws + WS_WO + (size_t)j * 131072, ldsb, i, i, wid - 4, lane);
    }
    f32x16 o = kh ? oB : oA;
    float l;
    {
        l = *(const LDSP float*)(ex + 32768 + wid * 256 + 128 + (lane & 31) * 4) + *(const LDSP float*)(ex + 32768 + (wid ^ 4) * 256 + (lane & 31) * 4);
#pragma unroll
        for (int i = 0; i < 4; ++i) {
            const f32x4 v = *(const LDSP f32x4*)(ex + (wid ^ 4) * 4096 + i * 1024 + lane * 16);
#pragma unroll
            for (int e = 0; e < 4; ++e) o[4 * i + e] += v[e];
        }
    }
    float chk = l * 0.f;
#pragma unroll
    for (int i = 0; i < 16; ++i) chk = __builtin_fmaf(o[i], 0.f, chk);
    const bool bad = AA ? false : (!(chk == 0.f) || !(l >= 0x1p-90f));
    if (__builtin_expect(__any(bad), 0)) {
        const bf16x8 q0 = kh ? qB0 : qA0, q1 = kh ? qB1 : qA1;
        float m = -INFINITY; l = 0.f; o = zero;
        for (int kt = 0; kt < 64; ++kt) {
            const bf16x8 a0 = *(const bf16x8*)(KT + kt * 2048 + lane * 16), a1 = *(const bf16x8*)(KT + kt * 2048 + 1024 + lane * 16);
            const bf16x8 b0 = *(const bf16x8*)(VT + kt * 2048 + lane * 16), b1 = *(const bf16x8*)(VT + kt * 2048 + 1024 + lane * 16);
            f32x16 s = mfma32(a0, q0, zero); s = mfma32(a1, q1, s);
            float mx = s[0];
#pragma unroll
            for (int i = 1; i < 16; ++i) mx = fmaxf(mx, s[i]);
            mx = fmaxf(mx, __shfl_xor(mx, 32));
            const float mn = fmaxf(m, mx), al = __builtin_amdgcn_exp2f(m - mn);
            m = mn;
            u32x4 p0, p1; float rs = 0.f;
#pragma unroll
            for (int i = 0; i < 4; ++i) {
                p0[i] = pk2(__builtin_amdgcn_exp2f(s[2 * i] - m), __builtin_amdgcn_exp2f(s[2 * i + 1] - m));
                p1[i] = pk2(__builtin_amdgcn_exp2f(s[8 + 2 * i] - m), __builtin_amdgcn_exp2f(s[9 + 2 * i] - m));
                rs += bflo(p0[i]) + bfhi(p0[i]) + bflo(p1[i]) + bfhi(p1[i]);
            }
            rs += __shfl_xor(rs, 32);
            l = l * al + rs;
#pragma unroll
            for (int i = 0; i < 16; ++i) o[i] *= al;
            o = mfma32(b0, BF(p0), o); o = mfma32(b1, BF(p1), o);
        }
    }
    {
        const float inv = __builtin_amdgcn_rcpf(l);
        const unsigned oAT = WS_O + p * 262144 + (2 * j + (qg >> 1)) * 8192 + ((qg & 1) * 2 + kh) * 512, vo = (lane >> 5) * 2048 + (lane & 31) * 16;
#pragma unroll
        for (int s = 0; s < 2; ++s) {
            u32x4 w;
#pragma unroll
            for (int i = 0; i < 4; ++i) w[i] = pk2(o[8 * s + 2 * i] * inv, o[8 * s + 2 * i + 1] * inv);
            st16(rws, vo, oAT + s * 4096, w);
        }
    }
    wait_vm<0>();
    lgkm_bar();
    if (wid == 0) panel_post(ws, FL_O, p, j, lane);
    if (wid == 0) STAMP(ws, 2, 4);
}

constexpr int F1_NS = 7, F1_STAGE = 22528, F1_NST = 32, F1_AUX = F1_NS * F1_STAGE;
template <int NW> DI void f1_w(const char* sw, unsigned ldsb, int stage, int pc0, int lane) {
    const unsigned vw = pc0 * 1024 + lane * 16, mw = ldsb + (stage % F1_NS) * F1_STAGE + 8192 + pc0 * 1024;
    const char* s = sw + stage * 14336;
    if constexpr (ABL == 2) {} else if constexpr (NW == 4)
        asm volatile(SETM0(mw) GLD1(vw, s, 0) GLD1(vw, s, 1024) GLD1(vw, s, 2048) GLD1(vw, s, 3072)::[mw] "s"(mw), [vw] "v"(vw), [s] "s"(s) : "memory");
    else
        asm volatile(SETM0(mw) GLD1(vw, s, 0) GLD1(vw, s, 1024) GLD1(vw, s, 2048)::[mw] "s"(mw), [vw] "v"(vw), [s] "s"(s) : "memory");
}
template <int NW> struct OpEx { static constexpr int n(int i) { return !PROG || i < 11 ? 0 : i == 12 ? 2 * NW : NW; } };
template <int NW>
DI void op_loader(char* ws, lptr lds, int lane, int lw, int p, int j, TS& TL) {
    const unsigned ldsb = (unsigned)(uintptr_t)lds;
    const char* sw = ws + WS_WO + (size_t)j * 131072;
    const char* s1 = ws + WS_W1 + (size_t)j * 458752;
    const int pc0 = lw < 2 ? lw * 4 : 8 + (lw - 2) * 3;
    tcap<1>(TL);
#pragma unroll
    for (int i = OPRE; i < OP_NS; ++i) g64_w(sw, ldsb, i, i, lw, lane);
    panel_wait(ws, FL_O, p, j, lane);
    tcap<2>(TL);
    g64_loader<16, OP_NS, OP_NS, OpEx<NW>>(ws + WS_O + (size_t)p * 262144, sw, ldsb, lw, lane, [&]<int I>() {
        if constexpr (I == 0) { tcap<3>(TL); }
        if constexpr (I == 15) { tcap<4>(TL); }
        if constexpr (PROG && I >= 11) f1_w<NW>(s1, ldsb, I < 13 ? I - 7 : I - 13, pc0, lane);
        if constexpr (PROG && I == 12) f1_w<NW>(s1, ldsb, 6, pc0, lane);
    });
}
DI void op_compute(const u16* __restrict__ x, char* __restrict__ ws, lptr lds, int lane, int wid, int p, int j) {
    if (wid == 0) STAMP(ws, 3, 0);
    const int th = wid & 1, nb = wid >> 1, hi = lane >> 5;
    const int n0 = j * 64 + nb * 32 + 4 * hi;
    const float* mod = (const float*)(ws + WS_MOD);
    u32x4 xq[2][2]; f32x4 ga[4], a2[4];
    f32x16 r[2] = {};
    if (wid == 0) STAMP(ws, 3, 1);
    g64_compute<16, OP_NS>(lds, wid, lane, r, [&](int i) {
        if (i == -1) {
#pragma unroll
            for (int tb = 0; tb < 2; ++tb)
#pragma unroll
                for (int h = 0; h < 2; ++h) xq[tb][h] = *(const u32x4*)(x + (size_t)(p * 128 + th * 64 + tb * 32 + (lane & 31)) * D_ + j * 64 + nb * 32 + h * 16 + hi * 8);
#pragma unroll
            for (int q = 0; q < 4; ++q) {
                ga[q] = *(const f32x4*)(mod + 2048 + n0 + 8 * q);
                a2[q] = *(const f32x4*)(mod + 4096 + n0 + 8 * q);
            }
            SBAR();
        }
    });
    if (wid == 0) STAMP(ws, 3, 2);
#pragma unroll
    for (int tb = 0; tb < 2; ++tb) {
        u32x4 xo[2], yo[2]; float v[16]; float s = 0.f;
        u32x2 xr[2][4];
#pragma unroll
        for (int h = 0; h < 2; ++h) {
            const auto s0 = __builtin_amdgcn_permlane32_swap(xq[tb][h][0], xq[tb][h][2], false, false);
            const auto s1 = __builtin_amdgcn_permlane32_swap(xq[tb][h][1], xq[tb][h][3], false, false);
            xr[tb][2 * h] = (u32x2){s0[0], s1[0]}; xr[tb][2 * h + 1] = (u32x2){s0[1], s1[1]};
        }
#pragma unroll
        for (int i = 0; i < 4; ++i) {
            const float x0 = bflo(xr[tb][i][0]), x1 = bfhi(xr[tb][i][0]), x2 = bflo(xr[tb][i][1]), x3 = bfhi(xr[tb][i][1]);
            const unsigned a = pk2(x0 + ga[i][0] * r[tb][4 * i], x1 + ga[i][1] * r[tb][4 * i + 1]);
            const unsigned b = pk2(x2 + ga[i][2] * r[tb][4 * i + 2], x3 + ga[i][3] * r[tb][4 * i + 3]);
            xo[i >> 1][(i & 1) * 2] = a; xo[i >> 1][(i & 1) * 2 + 1] = b;
            v[4 * i] = bflo(a); v[4 * i + 1] = bfhi(a); v[4 * i + 2] = bflo(b); v[4 * i + 3] = bfhi(b);
            yo[i >> 1][(i & 1) * 2] = pk2(v[4 * i] * a2[i][0], v[4 * i + 1] * a2[i][1]);
            yo[i >> 1][(i & 1) * 2 + 1] = pk2(v[4 * i + 2] * a2[i][2], v[4 * i + 3] * a2[i][3]);
            s += (v[4 * i] + v[4 * i + 1]) + (v[4 * i + 2] + v[4 * i + 3]);
        }
        const unsigned off = p * 262144 + (j * 2 + nb) * 8192 + (th * 64 + tb * 32) * 16, vo = hi * 2048 + (lane & 31) * 16;
#pragma unroll
        for (int h = 0; h < 2; ++h) {
            st16(mk_rsrc(ws), vo, WS_Y + off + h * 4096, yo[h]);
            const auto s0 = __builtin_amdgcn_permlane32_swap(xo[h][0], xo[h][2], false, false);
            const auto s1 = __builtin_amdgcn_permlane32_swap(xo[h][1], xo[h][3], false, false);
            st16(mk_rsrc(ws), vo, WS_X2 + off + h * 4096, (u32x4){s0[0], s1[0], s0[1], s1[1]});
        }
        s += __shfl_xor(s, 32);
        const float mean = s * (1.f / 32.f);
        float q = 0.f;
#pragma unroll
        for (int i = 0; i < 16; ++i) { const float d = v[i] - mean; q += d * d; }
        q += __shfl_xor(q, 32);
        if (hi == 0) __builtin_amdgcn_raw_buffer_store_b64((u32x2){__float_as_uint(mean), __float_as_uint(q)}, mk_rsrc(ws),
                                                           WS_ST + ((j * 2 + nb) * S_ + p * 128 + th * 64 + tb * 32 + lane) * 8, 0, 16);
    }
    wait_vm<0>();
    bar();
    if (wid == 0) panel_post(ws, FL_Y, p, j, lane);
    if (wid == 0) STAMP(ws, 3, 3);
}

#define F2_D 6
#define F2_MF 2
constexpr int F2_NS = 10, F2_STAGE = 16384, F2_MAIN = 56 - F2_D, F2_NST = 56 + F2_D;
DI void f2_w(char* ws, unsigned ldsb, int i, int lw, int lane) {
    const int b = blockIdx.x;
    const unsigned mw = ldsb + i * F2_STAGE + 8192 + lw * 2048, va = lw * 2048 + lane * 16;
    const char* s = ws + WS_W2 + (size_t)((b >> 3) & 7) * 917504 + ((b & 1) ? (56 + F2_D) * 8192 : 0) + i * 8192;
    if (ABL != 2) asm volatile(SETM0(mw) GLD1(va, s, 0) GLD1(va, s, 1024)::[mw] "s"(mw), [va] "v"(va), [s] "s"(s) : "memory");
}
struct F1Ex { static constexpr int n(int m) { return !PROG || m < 13 ? 0 : m == 14 ? 4 : 6; } };
template <int NW> struct F1Cnt { static constexpr int n(int s) { return (s < F1_NS ? 2 : 2 + NW) + (s == 2 ? 16 : 0); } };
template <int NW>
DI void f1_loader(char* ws, int p, int j, lptr lds, int lw, int lane, TS& TL) {
    const char* sa = ws + WS_Y + (size_t)p * 262144;
    const char* sw = ws + WS_W1 + (size_t)j * 458752;
    const unsigned ldsb = (unsigned)(uintptr_t)lds;
    const int pc0 = lw < 2 ? lw * 4 : 8 + (lw - 2) * 3;
    const unsigned va = lw * 2048 + lane * 16;
#pragma unroll
    for (int i = 0; i < F1_NS; ++i) if (i != 3 && !PROG) f1_w<NW>(sw, ldsb, i, pc0, lane);
    if (NW == 3) {
        grid_wait(ws, FL_SIDE, 2u, lane);
        if (lane < 56) {
            const unsigned o = WS_UTP + ((lw - 2) * F_ + j * 224 + lane * 4) * 4;
            const rsrc_t r = mk_rsrc(ws);
            const f32x4 u = (ld16f(r, o) + ld16f(r, o + 2 * F_ * 4)) + (ld16f(r, o + 4 * F_ * 4) + ld16f(r, o + 6 * F_ * 4));
            *(LDSP f32x4*)(lds + F1_AUX + (lw - 2) * 1024 + lane * 16) = u * (lw == 2 ? 1.f : -1.4426950408889634f);
        }
    }
    bar();
    tcap<5>(TL);
    f1_w<NW>(sw, ldsb, 3, pc0, lane);
    panel_wait(ws, FL_Y, p, j, lane);
    tcap<6>(TL);
    f32x2 sp[16];
    pair_loader<F1_NST, F1_NS, F1Cnt<NW>, F1Ex>([&]<int S>() {
        const unsigned ma = ldsb + (S % F1_NS) * F1_STAGE + lw * 2048;
        if constexpr (ABL != 2) asm volatile(SETM0(ma) GLD1(va, sa, 0) GLD1(va, sa, 1024)::[ma] "s"(ma), [va] "v"(va), [sa] "s"(sa) : "memory");
        if constexpr (S >= F1_NS) f1_w<NW>(sw, ldsb, S, pc0, lane);
        sa += 8192;
        if constexpr (S == 2) {
            const char* s = ws + WS_ST + (size_t)(p * 128 + lw * 32) * 8;
            const unsigned v0 = (lane >> 5) * 16 * (S_ * 8) + (lane & 31) * 8;
#pragma unroll
            for (int i = 0; i < 16; ++i) async_ld(sp[i], v0 + i * (S_ * 8), s);
        }
    }, [&]<int M, int PH>() {
        if constexpr (M == 0 && PH == 0) {
#pragma unroll
            for (int i = 0; i < 16; ++i) PIN(sp[i]);
        }
        if constexpr (PROG && M == 13 && PH == 1) { f2_w(ws, ldsb, 5, lw, lane); f2_w(ws, ldsb, 6, lw, lane); f2_w(ws, ldsb, 7, lw, lane); }
        if constexpr (PROG && M == 14 && PH == 1) { f2_w(ws, ldsb, 0, lw, lane); f2_w(ws, ldsb, 8, lw, lane); }
        if constexpr (PROG && M == 15 && PH == 1) { f2_w(ws, ldsb, 1, lw, lane); f2_w(ws, ldsb, 2, lw, lane); f2_w(ws, ldsb, 3, lw, lane); }
        if constexpr (M == -1 && PH == 1) { tcap<7>(TL); }
        if constexpr (M == 15 && PH == 1) { tcap<8>(TL); }
        if constexpr (M == 2 && PH == 1) {
            float s = 0.f;
#pragma unroll
            for (int i = 0; i < 16; ++i) s += sp[i][0];
            const float mu = swap32_add(s) * (1.f / 32.f);
            float q = 0.f;
#pragma unroll
            for (int i = 0; i < 16; ++i) { const float d = sp[i][0] - mu; q += sp[i][1] + 32.f * d * d; }
            const float rl = -1.4426950408889634f * __builtin_amdgcn_rsqf(swap32_add(q) * (1.f / 1024.f) + 1e-5f);
            if (lane < 32) *(LDSP f32x2*)(lds + F1_AUX + 2048 + (lw * 32 + lane) * 8) = (f32x2){mu, rl};
            asm volatile("s_waitcnt lgkmcnt(0)" ::: "memory");
        }
    });
}
struct F1Frag { bf16x8 a[4], w[7]; };
DI void f1_compute(char* __restrict__ ws, lptr lds, int lane, int wid, int p, int j) {
    if (wid == 0) STAMP(ws, 4, 0);
    const int th = wid & 1, nh = wid >> 1, g = lane >> 4;
    f32x4 acc[7][4] = {};
    const LP pa = mk_lp(lds + g * 2048 + (th * 64 + (lane & 15)) * 16), pw = mk_lp(lds + nh * 7168 + lane * 16);
    F1Frag fr[2];
    auto rd = [&]<int S, int I>() {
        constexpr int so = (S % F1_NS) * F1_STAGE;
        if constexpr (S < F1_NST && I < 11) {
            if constexpr (I >= 1 && I <= 4) fr[S & 1].a[I - 1] = lds_at<so + (I - 1) * 256>(pa);
            else fr[S & 1].w[I ? I - 4 : 0] = lds_at<so + 8192 + (I ? I - 4 : 0) * 1024>(pw);
        }
    };
    auto step = [&]<int S>() {
        if constexpr (S % 2 == 0) { lgkm_bar(); SBAR(); }
        if constexpr (ABL != 1)
            [&]<int... U>(std::integer_sequence<int, U...>) {
                ((acc[U / 4][U % 4] = mfma16(fr[S & 1].w[U / 4], fr[S & 1].a[U % 4], acc[U / 4][U % 4]), SBAR(), rd.template operator()<S + 1, U>(), SBAR()), ...);
            }(std::make_integer_sequence<int, 28>{});
    };
    if (wid == 0) STAMP(ws, 4, 1);
    bar();
    if (wid == 0) STAMP(ws, 4, 2);
    [&]<int... I>(std::integer_sequence<int, I...>) { (rd.template operator()<0, I>(), ...); }(std::make_integer_sequence<int, 11>{});
    [&]<int... S>(std::integer_sequence<int, S...>) { (step.template operator()<S>(), ...); }(std::make_integer_sequence<int, F1_NST>{});
    if (wid == 0) STAMP(ws, 4, 3);
    f32x2 st[4];
#pragma unroll
    for (int tb = 0; tb < 4; ++tb) st[tb] = *(const LDSP f32x2*)(lds + F1_AUX + 2048 + (th * 64 + tb * 16 + (lane & 15)) * 8);
    const lptr pu = lds + F1_AUX + (nh * 112 + 4 * g) * 4;
    const unsigned gp = WS_G + p * (F_ / 8 * 2048) + (j * 14 + nh * 7) * 4096 + th * 1024, vo = (g & 1) * 2048 + ((lane >> 5) * 16 + (lane & 15)) * 16;
    f32x4 u = *(const LDSP f32x4*)pu, tt = *(const LDSP f32x4*)(pu + 1024);
    const f32x2 cl = {-1.4426950408889634f, -1.4426950408889634f};
#pragma unroll
    for (int b = 0; b < 7; ++b) {
        f32x4 un, tn;
        if (b < 6) { un = *(const LDSP f32x4*)(pu + (b + 1) * 64); tn = *(const LDSP f32x4*)(pu + 1024 + (b + 1) * 64); }
        unsigned w[4][2];
#pragma unroll
        for (int tb = 0; tb < 4; ++tb)
#pragma unroll
            for (int e = 0; e < 2; ++e) {
                const f32x2 a = __builtin_elementwise_fma((f32x2){u[2 * e], u[2 * e + 1]}, (f32x2){-st[tb][0], -st[tb][0]}, (f32x2){acc[b][tb][2 * e], acc[b][tb][2 * e + 1]});
                const f32x2 z = __builtin_elementwise_fma(a, (f32x2){st[tb][1], st[tb][1]}, (f32x2){tt[2 * e], tt[2 * e + 1]});
                const f32x2 dn = __builtin_elementwise_fma((f32x2){__builtin_amdgcn_exp2f(z[0]), __builtin_amdgcn_exp2f(z[1])}, cl, cl);
                const f32x2 o = z * (f32x2){__builtin_amdgcn_rcpf(dn[0]), __builtin_amdgcn_rcpf(dn[1])};
                w[tb][e] = pk2(o[0], o[1]);
            }
#pragma unroll
        for (int tp = 0; tp < 2; ++tp) {
            const auto s0 = __builtin_amdgcn_permlane32_swap(w[2 * tp][0], w[2 * tp + 1][0], false, false);
            const auto s1 = __builtin_amdgcn_permlane32_swap(w[2 * tp][1], w[2 * tp + 1][1], false, false);
            st16(mk_rsrc(ws), vo + tp * 512, gp + b * 4096, (u32x4){s0[0], s1[0], s0[1], s1[1]});
        }
        u = un; tt = tn;
        SBAR();
    }
    wait_vm<0>();
    lgkm_bar();
    if (wid == 0) panel_post(ws, FL_G, p, j, lane);
    if (wid == 0) STAMP(ws, 4, 4);
}

struct F2Cnt { static constexpr int n(int s) { return s < F2_NS ? 2 : s < F2_MAIN ? 4 : 3; } };
struct F2Frag { bf16x8 a[4], w[4]; };
DI void f2_loader(char* ws, lptr lds, int lane, int lw, int p, int j, TS& TL) {
    const int b = blockIdx.x, kh = b & 1, jn = (b >> 3) & 7;
    const unsigned ldsb = (unsigned)(uintptr_t)lds;
    const char* sa = ws + WS_G + (size_t)p * (F_ / 8 * 2048) + (kh ? (56 + F2_D) * 8192 : 0);
    const char* sw = ws + WS_W2 + (size_t)jn * 917504 + (kh ? (56 + F2_D) * 8192 : 0);
    const unsigned va = lw * 2048 + lane * 16, vk = ((lw >> 1) * 4 + kh * 2 + (lw & 1)) * 1024 + lane * 16;
#pragma unroll
    for (int i = 0; i < F2_NS; ++i) if (i != 4 && i != 9 && !PROG) f2_w(ws, ldsb, i, lw, lane);
    bar();
    tcap<9>(TL);
    f2_w(ws, ldsb, 4, lw, lane); f2_w(ws, ldsb, 9, lw, lane);
    panel_wait(ws, FL_G, p, j, lane);
    tcap<10>(TL);
    pair_loader<F2_NST, F2_NS, F2Cnt, NoEx>([&]<int S>() {
        const unsigned ma = ldsb + (S % F2_NS) * F2_STAGE + lw * 2048;
        if constexpr (S == F2_MAIN) { if (kh) { sa -= 112 * 8192 - F2_MAIN * 8192 + 0; sw -= 112 * 8192 - F2_MAIN * 8192 + 0; } }
        if constexpr (ABL == 2) {} else if constexpr (S < F2_NS) {
            asm volatile(SETM0(ma) GLD1(va, sa, 0) GLD1(va, sa, 1024)::[ma] "s"(ma), [va] "v"(va), [sa] "s"(sa) : "memory");
        } else if constexpr (S < F2_MAIN) {
            const unsigned mw = ma + 8192;
            asm volatile(SETM0(ma) GLD1(va, sa, 0) GLD1(va, sa, 1024) SETM0(mw) GLD1(va, sw, 0) GLD1(va, sw, 1024)
                         ::[ma] "s"(ma), [mw] "s"(mw), [va] "v"(va), [sa] "s"(sa), [sw] "s"(sw) : "memory");
        } else {
            const unsigned mw = ldsb + (S % F2_NS) * F2_STAGE + 8192 + lw * 1024;
            asm volatile(SETM0(ma) GLD1(va, sa, 0) GLD1(va, sa, 1024) SETM0(mw) GLD1(vk, sw, 0)
                         ::[ma] "s"(ma), [mw] "s"(mw), [va] "v"(va), [vk] "v"(vk), [sa] "s"(sa), [sw] "s"(sw) : "memory");
        }
        sa += 8192; sw += 8192;
    }, [&]<int M, int PH>() { if constexpr (M == -1 && PH == 1) { tcap<11>(TL); } if constexpr (M == 4 && PH == 1) { tcap<12>(TL); } });
}
DI void f2_compute(char* __restrict__ ws, u16* __restrict__ out, lptr lds, int lane, int wid, int p) {
    const int b = blockIdx.x, kh = b & 1, jn = (b >> 3) & 7;
    if (wid == 0) STAMP(ws, 5, 0);
    const int th = wid & 1, nh = wid >> 1, g = lane >> 4;
    f32x4 acc[4][4] = {};
    const LP pa = mk_lp(lds + g * 2048 + (th * 64 + (lane & 15)) * 16);
    const LP pk = mk_lp(lds + (nh * 4 + kh * 2) * 1024 + lane * 16), ps = mk_lp(lds + (nh * 4 + (kh ^ 1) * 2) * 1024 + lane * 16);
    F2Frag fr[2];
    LP pt;
    auto rd = [&]<int S, int I>() {
        constexpr int so = (S % F2_NS) * F2_STAGE;
        if constexpr (S < F2_NST && I < (S < F2_MAIN ? 8 : 6)) {
            if constexpr (I >= 1 && I <= 4) fr[S & 1].a[I - 1] = lds_at<so + (I - 1) * 256>(pa);
            else if constexpr (I >= 6) fr[S & 1].w[I - 4] = lds_at<so + 8192 + (I - 6) * 1024>(ps);
            else fr[S & 1].w[I ? 1 : 0] = lds_at<so + 8192 + (I ? 1024 : 0)>(S < F2_MAIN ? pk : pt);
        }
    };
    auto step = [&]<int S>() {
        if constexpr (ABL != 1)
            [&]<int... U>(std::integer_sequence<int, U...>) {
                ((acc[U / 4][U % 4] = mfma16(fr[S & 1].w[U / 4], fr[S & 1].a[U % 4], acc[U / 4][U % 4]), SBAR(), rd.template operator()<S + 1, U>(), SBAR()), ...);
            }(std::make_integer_sequence<int, S < F2_MAIN ? 16 : 8>{});
    };
    if (wid == 0) STAMP(ws, 5, 1);
    bar();
    [&]<int... I>(std::integer_sequence<int, I...>) { (rd.template operator()<0, I>(), ...); }(std::make_integer_sequence<int, 8>{});
    static_assert(F2_MAIN % 2 == 0 && F2_D >= 4, "");
    [&]<int... M>(std::integer_sequence<int, M...>) { ((lgkm_bar(), SBAR(), step.template operator()<2 * M>(), step.template operator()<2 * M + 1>()), ...); }
    (std::make_integer_sequence<int, F2_MAIN / 2 - 1>{});
    pt = mk_lp(lds + nh * 2048 + lane * 16);
    lgkm_bar(); SBAR();
    step.template operator()<F2_MAIN - 2>(); step.template operator()<F2_MAIN - 1>();
    if (wid == 0) STAMP(ws, 5, 2);
    const rsrc_t rws = mk_rsrc(ws);
    const unsigned exo = WS_EX + wid * 8192 + lane * 16;
#pragma unroll
    for (int i = 0; i < 8; ++i) st16<16>(rws, exo + i * 1024, b * 32768, __builtin_bit_cast(u32x4, acc[2 + (i >> 2)][i & 3]));
    SBAR();
    __attribute__((address_space(1))) unsigned* flags = (__attribute__((address_space(1))) unsigned*)(ws + WS_FLAG + FL_EX);
    u32x4 xr[4], pr[8]; f32x4 gb[2];
    const int c8 = jn * 16 + nh * 8 + kh * 4 + g;
    TS TT; TCAP(TT, 0);
    auto tail = [&]<int M>() {
        if constexpr (M == F2_MF) wait_vm<0>();
        if constexpr (M == F2_D - 2) {
            if (wid == 0) {
                for (int spin = 0; spin < (1 << 22) && __hip_atomic_load(flags + (b ^ 1) * 32, __ATOMIC_RELAXED, __HIP_MEMORY_SCOPE_AGENT) == 0; ++spin) __builtin_amdgcn_s_sleep(1);
            }
        }
        lgkm_bar(); SBAR();
        if constexpr (M == F2_MF) {
            if (wid == 0 && lane == 0) __hip_atomic_store(flags + b * 32, 1u, __ATOMIC_RELAXED, __HIP_MEMORY_SCOPE_AGENT);
#pragma unroll
            for (int tb = 0; tb < 4; ++tb) xr[tb] = ld16(rws, WS_X2 + p * 262144 + c8 * 2048 + (th * 64 + tb * 16 + (lane & 15)) * 16);
            gb[0] = *(const f32x4*)(ws + WS_MOD + (5120 + c8 * 8) * 4); gb[1] = *(const f32x4*)(ws + WS_MOD + (5120 + c8 * 8 + 4) * 4);
            SBAR();
        }
        if constexpr (M == F2_D - 2) {
#pragma unroll
            for (int i = 0; i < 8; ++i) pr[i] = __builtin_amdgcn_raw_buffer_load_b128(rws, exo + i * 1024 + (b ^ 1) * 32768, 0, 16);
            SBAR();
        }
        step.template operator()<F2_MAIN + 2 * M>(); step.template operator()<F2_MAIN + 2 * M + 1>();
    };
    [&]<int... I>(std::integer_sequence<int, I...>) { (tail.template operator()<I>(), ...); }(std::make_integer_sequence<int, F2_D>{});
    if (wid == 0) STAMP(ws, 5, 3);
#pragma unroll
    for (int tb = 0; tb < 4; ++tb) {
        const f32x4 lo = acc[0][tb] + __builtin_bit_cast(f32x4, pr[tb]), hi = acc[1][tb] + __builtin_bit_cast(f32x4, pr[4 + tb]);
        const u32x4 xv = xr[tb];
        const u32x4 o = {pk2(bflo(xv[0]) + gb[0][0] * lo[0], bfhi(xv[0]) + gb[0][1] * lo[1]), pk2(bflo(xv[1]) + gb[0][2] * lo[2], bfhi(xv[1]) + gb[0][3] * lo[3]),
                         pk2(bflo(xv[2]) + gb[1][0] * hi[0], bfhi(xv[2]) + gb[1][1] * hi[1]), pk2(bflo(xv[3]) + gb[1][2] * hi[2], bfhi(xv[3]) + gb[1][3] * hi[3])};
        st16(mk_rsrc(out), (lane & 15) * (D_ * 2) + c8 * 16, (p * 128 + th * 64 + tb * 16) * (D_ * 2), o);
    }
    if (wid == 0) STAMP(ws, 5, 4);
    if (wid == 0) TDUMP(TT, ws, 7, F2_D + 1);
}

constexpr int MAIN_LDS = 163840;
__global__ void __launch_bounds__(512) k_main(const u16* __restrict__ x, const u16* __restrict__ wo, const u16* __restrict__ w1, const u16* __restrict__ w2,
                                              char* __restrict__ ws, u16* __restrict__ out) {
    extern __shared__ __attribute__((aligned(16))) char smem[];
    const lptr lds = (lptr)smem;
    const int lane = threadIdx.x & 63, wid = __builtin_amdgcn_readfirstlane(threadIdx.x >> 6);
    int p, j; tile_map(blockIdx.x, p, j);
    TS TL;
    ph_qattn(wo, w1, w2, ws, lds, lane, wid, p, j, TL);
    if (wid >= 4) {
        if (wid < 6) { op_loader<4>(ws, lds, lane, wid - 4, p, j, TL); f1_loader<4>(ws, p, j, lds, wid - 4, lane, TL); }
        else { op_loader<3>(ws, lds, lane, wid - 4, p, j, TL); f1_loader<3>(ws, p, j, lds, wid - 4, lane, TL); }
        f2_loader(ws, lds, lane, wid - 4, p, j, TL);
        if (wid == 4) TDUMP(TL, ws, 6, 13);
    } else {
        op_compute(x, ws, lds, lane, wid, p, j);
        f1_compute(ws, lds, lane, wid, p, j);
        f2_compute(ws, out, lds, lane, wid, p);
    }
}

extern "C" void kernel_launch(void* const* d_in, const int* in_sizes, int n_in, void* d_out, int out_size, void* d_ws, size_t ws_size,
                              hipStream_t stream) {
    const u16* x = (const u16*)d_in[0];
    const u16* wq = (const u16*)d_in[1];
    const u16* wk = (const u16*)d_in[2];
    const u16* wv = (const u16*)d_in[3];
    const u16* wo = (const u16*)d_in[4];
    const u16* w1 = (const u16*)d_in[6];
    const u16* w2 = (const u16*)d_in[7];
    const u16* c = (const u16*)d_in[8];
    const u16* wmod = (const u16*)d_in[9];
    char* ws = (char*)d_ws;
    (void)hipFuncSetAttribute((const void*)k_ln_kv, hipFuncAttributeMaxDynamicSharedMemorySize, K2_LDS);
    (void)hipFuncSetAttribute((const void*)k_main, hipFuncAttributeMaxDynamicSharedMemorySize, MAIN_LDS);
    k_prep<<<256, 512, 32768, stream>>>(c, wmod, wq, ws);
    k_ln_kv<<<256, 512, K2_LDS, stream>>>(x, wk, wv, ws);
    k_main<<<256, 512, MAIN_LDS, stream>>>(x, wo, w1, w2, ws, (u16*)d_out);
}
```

```cpp
#define TX 0
#pragma clang diagnostic ignored "-Wc++20-extensions"
#include <hip/hip_runtime.h>
#include <hip/hip_bf16.h>
#include <stdint.h>

typedef unsigned short u16;
typedef __bf16 bf16x8 __attribute__((ext_vector_type(8)));
typedef __bf16 bf16x2 __attribute__((ext_vector_type(2)));
typedef short s16x4 __attribute__((ext_vector_type(4)));
typedef short s16x8 __attribute__((ext_vector_type(8)));
typedef float f32x16 __attribute__((ext_vector_type(16)));
typedef float f32x4 __attribute__((ext_vector_type(4)));
typedef float f32x2 __attribute__((ext_vector_type(2)));
typedef unsigned u32x4 __attribute__((ext_vector_type(4)));
typedef unsigned u32x2 __attribute__((ext_vector_type(2)));
#define LDSP __attribute__((address_space(3)))
typedef LDSP char* lptr;
#define DI __device__ __forceinline__

constexpr int S_ = 2048, D_ = 1024, F_ = 3584;
constexpr size_t WS_MOD = 0;
constexpr size_t WS_UT = 32768;
constexpr size_t WS_KT = 65536;
constexpr size_t WS_VT = WS_KT + 131072;
constexpr size_t WS_MODP = WS_VT + 131072;
constexpr size_t WS_BUT = WS_MODP + 98304;
constexpr size_t WS_UTP = WS_BUT + 4096;
constexpr size_t WS_H = 1u << 20;
constexpr size_t WS_O = WS_H + (4u << 20);
constexpr size_t WS_X2 = WS_O + (4u << 20);
constexpr size_t WS_Y = WS_X2 + (4u << 20);
constexpr size_t WS_G = WS_Y + (4u << 20);
constexpr size_t WS_WQ = 32u << 20;
constexpr size_t WS_WO = WS_WQ + (2u << 20);
constexpr size_t WS_W1 = WS_WO + (2u << 20);
constexpr size_t WS_W2 = WS_W1 + (7u << 20);
constexpr size_t WS_FLAG = WS_UTP + 131072;
constexpr int FL_EX = 0, FL_O = 32768, FL_Y = 65536, FL_G = 98304, FL_H = 131072, FL_SIDE = 163840, FL_KV = FL_SIDE + 8192, FL_END = FL_KV + 8192;
constexpr size_t WS_ST = 60u << 20;
constexpr size_t WS_EX = 52u << 20;
static_assert(WS_FLAG + FL_END <= WS_H && FL_END % (256 * 4) == 0, "");
static_assert(WS_UTP + 4 * 2 * 3584 * 4 <= WS_H, "");

constexpr size_t WS_DBG = 64u << 20;
#define STAMP(ws, kid, i) do {} while (0)
struct TS {};
#define TCAP(T, i) do {} while (0)
#define TDUMP(T, ws, kid, n) do {} while (0)
#define TXP(ws, i) do { if (TX == (i) && blockIdx.x == 0 && threadIdx.x == 0) { unsigned long long b_; asm volatile("s_memrealtime %0\n\ts_waitcnt lgkmcnt(0)" : "=s"(b_)::"memory"); \
    *(unsigned long long*)((char*)(ws) + WS_DBG) = b_; } } while (0)
__global__ void k_spin(char* ws) {
    const unsigned long long t0 = *(volatile unsigned long long*)(ws + WS_DBG);
    for (int i = 0; i < 100000; ++i) { unsigned long long b_; asm volatile("s_memrealtime %0\n\ts_waitcnt lgkmcnt(0)" : "=s"(b_)::"memory"); if (b_ - t0 >= 12000ull) break; }
}
DI float bf2f(u16 v) { return __uint_as_float((unsigned)v << 16); }
DI float bflo(unsigned v) { return __uint_as_float(v << 16); }
DI float bfhi(unsigned v) { return __uint_as_float(v & 0xffff0000u); }
DI unsigned pk2(float a, float b) { f32x2 v = {a, b}; return __builtin_bit_cast(unsigned, __builtin_convertvector(v, bf16x2)); }
DI float rbf(float a) { return bflo(pk2(a, 0.f)); }
DI f32x16 mfma32(bf16x8 a, bf16x8 b, f32x16 c) { return __builtin_amdgcn_mfma_f32_32x32x16_bf16(a, b, c, 0, 0, 0); }
DI f32x4 mfma16(bf16x8 a, bf16x8 b, f32x4 c) { return __builtin_amdgcn_mfma_f32_16x16x32_bf16(a, b, c, 0, 0, 0); }
DI bf16x8 lds_b128(lptr p) { return *(const LDSP bf16x8*)p; }
DI s16x4 lds_tr(lptr p) { return __builtin_amdgcn_ds_read_tr16_b64_v4i16((LDSP s16x4*)p); }
DI bf16x8 cat(s16x4 a, s16x4 b) { return __builtin_bit_cast(bf16x8, __builtin_shufflevector(a, b, 0, 1, 2, 3, 4, 5, 6, 7)); }
#define WT 16
typedef __amdgpu_buffer_rsrc_t rsrc_t;
DI rsrc_t mk_rsrc(void* base) { return __builtin_amdgcn_make_buffer_rsrc(base, 0, 0x7fffffff, 0x00020000); }
template <int POL = WT> DI void st16(rsrc_t r, unsigned voff, unsigned soff, u32x4 v) { __builtin_amdgcn_raw_buffer_store_b128(v, r, voff + soff, 0, POL); }
struct LP { lptr b[3]; };
DI LP mk_lp(lptr p) { LP r = {{p, p + 65536, p + 131072}}; asm volatile("" : "+v"(r.b[0]), "+v"(r.b[1]), "+v"(r.b[2])); return r; }
template <int OFF> DI bf16x8 lds_at(const LP& p) { return *(const LDSP bf16x8*)(p.b[OFF >> 16] + (OFF & 65535)); }
#define SBAR() __builtin_amdgcn_sched_barrier(0)
#define PIN(x) asm volatile("" : "+v"(x))
template <int N> DI void wait_vm() { asm volatile("s_waitcnt vmcnt(%0)" ::"i"(N) : "memory"); }
DI void bar() { asm volatile("s_barrier" ::: "memory"); }
DI void lgkm_bar() { asm volatile("s_waitcnt lgkmcnt(0)\n\ts_barrier" ::: "memory"); }
template <int C> DI float dpp_add(float v) { return v + __builtin_bit_cast(float, __builtin_amdgcn_update_dpp(0, __builtin_bit_cast(int, v), C, 0xf, 0xf, true)); }
DI float swap16_add(float v) { const auto r = __builtin_amdgcn_permlane16_swap(__float_as_uint(v), __float_as_uint(v), false, false); return __uint_as_float(r[0]) + __uint_as_float(r[1]); }
DI float swap32_add(float v) { const auto r = __builtin_amdgcn_permlane32_swap(__float_as_uint(v), __float_as_uint(v), false, false); return __uint_as_float(r[0]) + __uint_as_float(r[1]); }
DI float wave_sum(float v) {
    v = dpp_add<0xB1>(v); v = dpp_add<0x4E>(v); v = dpp_add<0x141>(v); v = dpp_add<0x140>(v);
    return swap32_add(swap16_add(v));
}

#define ABL 0
#define AA 0
#define PRIO 1
#define GLDS(v, s, off) "global_load_lds_dwordx4 %[" #v "], %[" #s "] offset:" #off "\n\t"
#define GLD1(v, s, off) "global_load_lds_dwordx4 %[" #v "], %[" #s "] offset:" #off " sc1\n\t"
#define SETM0(m) "s_mov_b32 m0, %[" #m "]\n\ts_nop 0\n\t"

typedef __attribute__((address_space(1))) unsigned* gu32;
DI void flag_st(char* ws, unsigned off, unsigned v) { __hip_atomic_store((gu32)(ws + WS_FLAG + off), v, __ATOMIC_RELAXED, __HIP_MEMORY_SCOPE_AGENT); }
DI u32x4 ld16(rsrc_t r, unsigned off) { return __builtin_amdgcn_raw_buffer_load_b128(r, off, 0, 16); }
DI f32x4 ld16f(rsrc_t r, unsigned off) { return __builtin_bit_cast(f32x4, ld16(r, off)); }
DI void async_ld(u32x4& d, unsigned v, const char* s) { asm volatile("global_load_dwordx4 %0, %1, %2 sc1" : "=v"(d) : "v"(v), "s"(s) : "memory"); }
DI void async_ld(f32x2& d, unsigned v, const char* s) { asm volatile("global_load_dwordx2 %0, %1, %2 sc1" : "=v"(d) : "v"(v), "s"(s) : "memory"); }
DI void touch(unsigned& d, unsigned v, const char* s) { asm volatile("global_load_dword %0, %1, %2 sc1" : "+v"(d) : "v"(v), "s"(s) : "memory"); }
DI void panel_post(char* ws, int fl, int p, int j, int lane) { if (lane < 16) flag_st(ws, fl + (p * 16 + lane) * 128 + j * 4, 1u); }
DI void panel_wait(char* ws, int fl, int p, int j, int lane) {
    const gu32 f = (gu32)(ws + WS_FLAG + fl + (p * 16 + j) * 128) + (lane & 15);
    for (int spin = 0; spin < (1 << 22) && !__all(__hip_atomic_load(f, __ATOMIC_RELAXED, __HIP_MEMORY_SCOPE_AGENT) != 0); ++spin) __builtin_amdgcn_s_sleep(1);
    asm volatile("" ::: "memory");
}
DI void grid_post(char* ws, int fl, unsigned v, int lane) { if (lane < 8) flag_st(ws, fl + lane * 1024 + blockIdx.x * 4, v); }
DI bool grid_ok(u32x4 f, unsigned v) { return __all(f[0] >= v && f[1] >= v && f[2] >= v && f[3] >= v); }
DI void grid_wait(char* ws, int fl, unsigned v, int lane) {
    for (int spin = 0; spin < (1 << 22) && !grid_ok(ld16(mk_rsrc(ws), WS_FLAG + fl + (blockIdx.x & 7) * 1024 + lane * 16), v); ++spin) { __builtin_amdgcn_s_sleep(1); asm volatile("" ::: "memory"); }
    asm volatile("" ::: "memory");
}

DI void tile_issue(const u16* src, int ld, unsigned ldsb, int lane) {
    const unsigned vo = (unsigned)((lane >> 3) * ld * 2 + (lane & 7) * 16);
#pragma unroll
    for (int i = 0; i < 4; ++i) {
        const u16* s = src + (size_t)i * 8 * ld;
        const unsigned m = ldsb + i * 1024;
        asm volatile(SETM0(m) GLDS(v, s, 0)::[m] "s"(m), [v] "v"(vo), [s] "s"(s) : "memory");
    }
}
template <bool PERM> DI void tile_store32(lptr st, rsrc_t r, unsigned dst, int lane) {
    const lptr p = st + ((PERM ? 4 : 8) * (lane >> 5) + ((lane & 15) >> 2)) * 128 + ((lane >> 4) & 1) * 32 + (lane & 3) * 8;
    bf16x8 f[4];
#pragma unroll
    for (int i = 0; i < 4; ++i) { const lptr q = p + (i >> 1) * 2048 + (i & 1) * 64; f[i] = cat(lds_tr(q), lds_tr(q + (PERM ? 1024 : 512))); }
#pragma unroll
    for (int i = 0; i < 4; ++i) st16(r, i * 1024 + lane * 16, dst, __builtin_bit_cast(u32x4, f[i]));
}

__global__ void __launch_bounds__(512) k_prep(const u16* __restrict__ c, const u16* __restrict__ wmod, const u16* __restrict__ wq, char* __restrict__ ws) {
    extern __shared__ __attribute__((aligned(16))) char smem[];
    const lptr lds = (lptr)smem;
    const int t = threadIdx.x, lane = t & 63, wid = __builtin_amdgcn_readfirstlane(t >> 6), b = blockIdx.x;
    if (wid == 0) STAMP(ws, 0, 0);
    TXP(ws, 0);
    if (t < FL_END / 1024) *(unsigned*)(ws + WS_FLAG + (b * (FL_END / 1024) + t) * 4) = 0u;
    if (b >= 192) {
        const int j = (b - 192) & 15, kg = (b - 192) >> 4;
        tile_issue(wq + (size_t)(kg * 256 + wid * 32) * D_ + j * 64, D_, (unsigned)(uintptr_t)lds + wid * 4096, lane);
        wait_vm<0>();
        tile_store32<false>(lds + wid * 4096, mk_rsrc(ws), WS_WQ + j * 131072 + (kg * 8 + wid) * 4096, lane);
        if (wid == 0) STAMP(ws, 0, 1);
        return;
    }
    const int cg = b % 48, kg = b / 48, ch = t & 15, rg = t >> 4;
    u32x4 w[8]; u16 cv[8];
#pragma unroll
    for (int i = 0; i < 8; ++i) w[i] = __builtin_nontemporal_load((const u32x4*)(wmod + (size_t)(kg * 256 + i * 32 + rg) * 6144 + cg * 128 + ch * 8));
#pragma unroll
    for (int i = 0; i < 8; ++i) cv[i] = c[kg * 256 + i * 32 + rg];
    float acc[8] = {0, 0, 0, 0, 0, 0, 0, 0};
#pragma unroll
    for (int i = 0; i < 8; ++i) {
        const float f = bf2f(cv[i]);
        const float s = rbf(f / (1.f + __expf(-f)));
#pragma unroll
        for (int j = 0; j < 4; ++j) { acc[2 * j] += s * bflo(w[i][j]); acc[2 * j + 1] += s * bfhi(w[i][j]); }
    }
#pragma unroll
    for (int j = 0; j < 8; ++j) acc[j] = swap32_add(swap16_add(acc[j]));
    if (lane < 16) {
        *(LDSP f32x4*)(lds + wid * 512 + ch * 32) = (f32x4){acc[0], acc[1], acc[2], acc[3]};
        *(LDSP f32x4*)(lds + wid * 512 + ch * 32 + 16) = (f32x4){acc[4], acc[5], acc[6], acc[7]};
    }
    lgkm_bar();
    if (t < 128) {
        float v = 0.f;
#pragma unroll
        for (int i = 0; i < 8; ++i) v += *(const LDSP float*)(lds + i * 512 + t * 4);
        ((float*)(ws + WS_MODP))[kg * 6144 + cg * 128 + t] = v;
    }
    if (wid == 0) STAMP(ws, 0, 1);
}

constexpr int K2_W = 32768, K2_MOD = K2_W + 65536, K2_RED = K2_MOD + 8192, K2_LDS = K2_RED + 16384;
__global__ void __launch_bounds__(512) k_ln_kv(const u16* __restrict__ x, const u16* __restrict__ wk, const u16* __restrict__ wv, char* __restrict__ ws) {
    extern __shared__ __attribute__((aligned(16))) char smem[];
    const lptr lds = (lptr)smem;
    const int t = threadIdx.x, lane = t & 63, wid = __builtin_amdgcn_readfirstlane(t >> 6);
    const int b = blockIdx.x, tg = ((b >> 4) << 3) | (b & 7), which = (b >> 3) & 1;
    const float* modp = (const float*)(ws + WS_MODP);
    if (wid == 0) STAMP(ws, 1, 0);
    TXP(ws, 1);
    u32x4 xr[2][2]; f32x4 mp[4];
    {
        const u16* xp = x + (size_t)(tg * 16 + wid * 2) * D_;
        const unsigned vx = lane * 16, vm = t * 16;
        asm volatile("global_load_dwordx4 %0, %8, %10\n\tglobal_load_dwordx4 %1, %8, %10 offset:1024\n\t"
                     "global_load_dwordx4 %2, %8, %10 offset:2048\n\tglobal_load_dwordx4 %3, %8, %10 offset:3072\n\t"
                     "global_load_dwordx4 %4, %9, %11\n\tglobal_load_dwordx4 %5, %9, %12\n\tglobal_load_dwordx4 %6, %9, %13\n\tglobal_load_dwordx4 %7, %9, %14"
                     : "=&v"(xr[0][0]), "=&v"(xr[0][1]), "=&v"(xr[1][0]), "=&v"(xr[1][1]), "=&v"(mp[0]), "=&v"(mp[1]), "=&v"(mp[2]), "=&v"(mp[3])
                     : "v"(vx), "v"(vm), "s"(xp), "s"(modp), "s"(modp + 6144), "s"(modp + 2 * 6144), "s"(modp + 3 * 6144) : "memory");
    }
    float mf[4] = {0, 0, 0, 0};
    if (t < 24) {
#pragma unroll
        for (int i = 0; i < 4; ++i) mf[i] = modp[i * 6144 + b * 24 + t];
    }
    {
        const u16* W = (which ? wv : wk) + wid * 4096;
        const int row = lane >> 2, chn = (lane & 3) ^ (((row >> 3) & 1) << 1);
        const unsigned vo = (unsigned)(row * 64 + chn * 16);
#pragma unroll
        for (int q = 0; q < 2; ++q) {
            const unsigned m = (unsigned)(uintptr_t)lds + K2_W + wid * 8192 + q * 4096;
            const u16* sb_ = W + q * 2048;
            asm volatile(SETM0(m) GLDS(v, s, 0) GLDS(v, s, 1024) GLDS(v, s, 2048) GLDS(v, s, 3072)::[m] "s"(m), [v] "v"(vo), [s] "s"(sb_) : "memory");
        }
    }
    asm volatile("s_waitcnt vmcnt(8)" : "+v"(xr[0][0]), "+v"(xr[0][1]), "+v"(xr[1][0]), "+v"(xr[1][1]), "+v"(mp[0]), "+v"(mp[1]), "+v"(mp[2]), "+v"(mp[3])::"memory");
    {
        f32x4 m = (mp[0] + mp[1]) + (mp[2] + mp[3]);
#pragma unroll
        for (int e = 0; e < 4; ++e) { m[e] = rbf(m[e]); if (t >= 256) m[e] = rbf(1.f + m[e]); }
        *(LDSP f32x4*)(lds + K2_MOD + t * 16) = m;
    }
    float v[2][16], rstd[2];
#pragma unroll
    for (int r = 0; r < 2; ++r) {
#pragma unroll
        for (int h = 0; h < 2; ++h)
#pragma unroll
            for (int j = 0; j < 4; ++j) { v[r][h * 8 + 2 * j] = bflo(xr[r][h][j]); v[r][h * 8 + 2 * j + 1] = bfhi(xr[r][h][j]); }
        float s = 0;
#pragma unroll
        for (int j = 0; j < 16; ++j) s += v[r][j];
        const float mean = wave_sum(s) * (1.f / 1024.f);
        float q = 0;
#pragma unroll
        for (int j = 0; j < 16; ++j) { v[r][j] -= mean; q += v[r][j] * v[r][j]; }
        rstd[r] = __builtin_amdgcn_rsqf(wave_sum(q) * (1.f / 1024.f) + 1e-5f);
    }
    if (wid == 0) STAMP(ws, 1, 1);
    lgkm_bar();
#pragma unroll
    for (int h = 0; h < 2; ++h) {
        f32x4 sa[2], a1[2];
#pragma unroll
        for (int q = 0; q < 2; ++q) {
            sa[q] = *(const LDSP f32x4*)(lds + K2_MOD + (h * 512 + lane * 8 + q * 4) * 4);
            a1[q] = *(const LDSP f32x4*)(lds + K2_MOD + 4096 + (h * 512 + lane * 8 + q * 4) * 4);
        }
#pragma unroll
        for (int r = 0; r < 2; ++r) {
            u32x4 o;
#pragma unroll
            for (int j = 0; j < 4; ++j) {
                const int e = 2 * j;
                o[j] = pk2(v[r][h * 8 + e] * rstd[r] * a1[e >> 2][e & 3] + sa[e >> 2][e & 3], v[r][h * 8 + e + 1] * rstd[r] * a1[e >> 2][(e & 3) + 1] + sa[e >> 2][(e & 3) + 1]);
            }
            const int kc = h * 64 + lane;
            *(LDSP u32x4*)(lds + kc * 256 + (((wid * 2 + r) ^ (kc & 7)) * 16)) = o;
        }
    }
    wait_vm<0>();
    lgkm_bar();
    if (wid == 0) STAMP(ws, 1, 2);
    if (t < 24) {
        const int col = b * 24 + t, part = col >> 10, k = col & 1023;
        float v = rbf((mf[0] + mf[1]) + (mf[2] + mf[3]));
        if (part == 1 || part == 4) v = rbf(1.f + v);
        ((float*)(ws + WS_MOD))[col] = v;
        if (part == 3 || part == 4)
            ((u16*)(ws + WS_BUT))[(((k >> 5) * 2 + (part == 3)) * 4 + 2 * ((k >> 4) & 1) + ((k >> 2) & 1)) * 8 + (k & 3) + 4 * ((k >> 3) & 1)] = (u16)(__float_as_uint(v) >> 16);
    }
    {
        const unsigned hAT = WS_H + (tg >> 3) * 262144 + (tg & 7) * 256;
#pragma unroll
        for (int i = 0; i < 2; ++i) {
            const int kc = which * 64 + i * 32 + (t >> 4), tk = t & 15;
            st16(mk_rsrc(ws), kc * 2048 + tk * 16, hAT, *(const LDSP u32x4*)(lds + kc * 256 + ((tk ^ (kc & 7)) * 16)));
        }
    }
    f32x4 acc[2] = {{0, 0, 0, 0}, {0, 0, 0, 0}};
    {
        const int g = lane >> 4, tk = lane & 15, q = (lane & 15) >> 2, p = lane & 3;
#pragma unroll
        for (int s = 0; s < 4; ++s) {
            const int kc = wid * 16 + s * 4 + g;
            const bf16x8 hf = lds_b128(lds + kc * 256 + ((tk ^ (kc & 7)) * 16));
            const int row = kc * 8 + q;
#pragma unroll
            for (int nb = 0; nb < 2; ++nb) {
                const lptr wp = lds + K2_W + row * 64 + ((nb ^ (kc & 1)) * 32) + p * 8;
                const bf16x8 wf = cat(lds_tr(wp), lds_tr(wp + 256));
                acc[nb] = which ? mfma16(hf, wf, acc[nb]) : mfma16(wf, hf, acc[nb]);
            }
        }
    }
#pragma unroll
    for (int nb = 0; nb < 2; ++nb) *(LDSP f32x4*)(lds + K2_RED + ((wid * 2 + nb) * 64 + lane) * 16) = acc[nb];
    lgkm_bar();
    if (t < 128) {
        const int nb = t >> 6;
        f32x4 r = *(const LDSP f32x4*)(lds + K2_RED + (nb * 64 + lane) * 16);
#pragma unroll
        for (int w = 1; w < 8; ++w) r += *(const LDSP f32x4*)(lds + K2_RED + ((w * 2 + nb) * 64 + lane) * 16);
        u32x2 o = {pk2(r[0], r[1]), pk2(r[2], r[3])};
        const int g = lane >> 4, c = lane & 15, kt = tg >> 1;
        if (which) {
            *(u32x2*)(ws + WS_VT + kt * 2048 + ((tg & 1) * 2 + (g & 1)) * 512 + (nb * 16 + c) * 16 + (g >> 1) * 8) = o;
        } else {
            *(u32x2*)(ws + WS_KT + kt * 2048 + (nb * 2 + (g & 1)) * 512 + ((tg & 1) * 16 + c) * 16 + (g >> 1) * 8) = o;
        }
    }
    if (wid == 0) STAMP(ws, 1, 3);
}

#define R0 3
constexpr int ring_issued(int i, int NST, int NS) { int n = R0 < NS - 1 ? R0 : NS - 1; for (int k = 0; k < i; ++k) { n += 2; if (n > k + NS) n = k + NS; if (n > NST) n = NST; } return n; }
template <int I> DI void tcap(TS& T) { TCAP(T, I); }
struct NoEx { static constexpr int n(int) { return 0; } };
template <int NST, int NS, class CNT, class EX, class F, class X>
DI void ring_loader(F issue, X extra) {
    auto range = [&]<int S0, int... I>(std::integer_sequence<int, I...>) { (issue.template operator()<S0 + I>(), ...); };
    auto sync = [&]<int I>() {
        constexpr int have = ring_issued(I, NST, NS);
        constexpr int out = [] { int c = 0; for (int s = I + 1; s < have; ++s) c += CNT::n(s); for (int i = 0; i < I; ++i) c += EX::n(i); return c; }();
        static_assert(out < 64 && I < have && (EX::n(I) == 0 || ring_issued(I + 1, NST, NS) == NST), "");
        wait_vm<out>();
        bar();
        range.template operator()<have>(std::make_integer_sequence<int, ring_issued(I + 1, NST, NS) - have>{});
        extra.template operator()<I>();
    };
    range.template operator()<0>(std::make_integer_sequence<int, ring_issued(0, NST, NS)>{});
    [&]<int... I>(std::integer_sequence<int, I...>) { (sync.template operator()<I>(), ...); }(std::make_integer_sequence<int, NST>{});
}
constexpr int cmin(int a, int b) { return a < b ? a : b; }
constexpr int pair_issued(int m, int NST, int NS) {
    int n = 3;
    for (int k = -1; k < m; ++k) n = cmin(cmin(n + 4, k < 0 ? NS : 2 * k + 1 + NS), NST);
    return n;
}
template <int NST, int NS, class CNT, class EX, class F, class X>
DI void pair_loader(F issue, X extra) {
    auto range = [&]<int S0, int... I>(std::integer_sequence<int, I...>) { (issue.template operator()<S0 + I>(), ...); };
    auto sync = [&]<int M>() {
        constexpr int need = M < 0 ? 0 : cmin(2 * M + 2, NST - 1), have = pair_issued(M, NST, NS);
        constexpr int out = [] { int c = 0; for (int s = need + 1; s < have; ++s) c += CNT::n(s); for (int i = 0; i < M; ++i) c += EX::n(i); return c; }();
        static_assert(out < 64 && need < have && (EX::n(M) == 0 || pair_issued(M + 1, NST, NS) == NST), "");
        wait_vm<out>();
        extra.template operator()<M, 0>();
        bar();
        range.template operator()<have>(std::make_integer_sequence<int, pair_issued(M + 1, NST, NS) - have>{});
        extra.template operator()<M, 1>();
    };
    range.template operator()<0>(std::make_integer_sequence<int, 3>{});
    [&]<int... I>(std::integer_sequence<int, I...>) { (sync.template operator()<I - 1>(), ...); }(std::make_integer_sequence<int, NST / 2 + 1>{});
}
constexpr int G64_STAGE = 24576;
template <int PRE> struct G64Cnt { static constexpr int n(int s) { return s < PRE ? 4 : 6; } };
DI void g64_w(const char* sw, unsigned ldsb, int stage, int slot, int lw, int lane) {
    const unsigned vw = lw * 2048 + lane * 16, mw = ldsb + slot * G64_STAGE + 16384 + lw * 2048;
    const char* s = sw + stage * 8192;
    if (ABL != 2) asm volatile(SETM0(mw) GLD1(vw, s, 0) GLD1(vw, s, 1024)::[mw] "s"(mw), [vw] "v"(vw), [s] "s"(s) : "memory");
}
template <int NST, int NS, int PRE, class EX, class X>
DI void g64_loader(const char* sa, const char* sw, unsigned ldsb, int lw, int lane, X extra) {
    const unsigned va = lw * 4096 + lane * 16;
    ring_loader<NST, NS, G64Cnt<PRE>, EX>([&]<int S>() {
        const unsigned ma = ldsb + (S % NS) * G64_STAGE + lw * 4096;
        if (ABL != 2) asm volatile(SETM0(ma) GLD1(va, sa, 0) GLD1(va, sa, 1024) GLD1(va, sa, 2048) GLD1(va, sa, 3072)::[ma] "s"(ma), [va] "v"(va), [sa] "s"(sa) : "memory");
        if constexpr (S >= PRE) g64_w(sw, ldsb, S, S % NS, lw, lane);
        sa += 16384;
    }, extra);
}

struct G64Frag { bf16x8 a[4][2], w[4]; };
template <int NST, int NS, class H>
DI void g64_compute(lptr lds, int cw, int lane, f32x16 (&acc)[2], H hook) {
    const LP pa = mk_lp(lds + (lane >> 5) * 2048 + ((cw & 1) * 64 + (lane & 31)) * 16), pw = mk_lp(lds + (cw >> 1) * 1024 + lane * 16);
    G64Frag fr[2];
    auto rd = [&]<int S, int I>() {
        constexpr int so = (S % NS) * G64_STAGE;
        if constexpr (S < NST && I < 12) {
            if constexpr (I % 3 == 0) fr[S & 1].w[I / 3] = lds_at<so + 16384 + (I / 3) * 2048>(pw);
            else fr[S & 1].a[I / 3][I % 3 - 1] = lds_at<so + (I / 3) * 4096 + (I % 3 - 1) * 512>(pa);
        }
    };
    auto step = [&]<int S>() {
        if constexpr (S + 1 < NST) { lgkm_bar(); SBAR(); }
        if constexpr (ABL != 1)
            [&]<int... U>(std::integer_sequence<int, U...>) {
                ((acc[U % 2] = mfma32(fr[S & 1].w[U / 2], fr[S & 1].a[U / 2][U % 2], acc[U % 2]), SBAR(),
                  rd.template operator()<S + 1, 2 * U>(), rd.template operator()<S + 1, 2 * U + 1>(), SBAR()), ...);
            }(std::make_integer_sequence<int, 8>{});
        hook(S);
    };
    bar();
    [&]<int... I>(std::integer_sequence<int, I...>) { (rd.template operator()<0, I>(), ...); }(std::make_integer_sequence<int, 12>{});
    hook(-1);
    [&]<int... S>(std::integer_sequence<int, S...>) { (step.template operator()<S>(), ...); }(std::make_integer_sequence<int, NST>{});
}
DI void tile_map(int b, int& p, int& j) { const int x = b & 7, i = b >> 3; p = (x >> 1) * 4 + (i >> 3); j = (x & 1) * 8 + (i & 7); }

constexpr int QA_NS = 4;
constexpr int QA_KV0 = QA_NS * G64_STAGE;
constexpr int QA_LDS = QA_KV0 + 65536;
constexpr float QSCALE = 0.17677669529663687f * 1.4426950408889634f;

constexpr int OP_NS = 6;
#define OPRE 4
#define PREW 0
#define PROG 1
DI void ph_qattn(const u16* __restrict__ wo, const u16* __restrict__ w1, const u16* __restrict__ w2, char* __restrict__ ws, lptr lds, int lane, int wid, int p, int j, TS& TL) {
    const unsigned ldsb = (unsigned)(uintptr_t)lds;
    const char* KT = ws + WS_KT; const char* VT = ws + WS_VT;
    const unsigned vkv = lane * 16;
    auto kv_piece = [&](int chunk, unsigned buf, int w, int i) {
        const char* s = ((i & 4) ? VT : KT) + chunk * 32768 + w * 4096 + (i & 3) * 1024;
        const unsigned m = ldsb + buf + (i >> 2) * 32768 + w * 4096 + (i & 3) * 1024;
        asm volatile(SETM0(m) GLDS(v, s, 0)::[m] "s"(m), [v] "v"(vkv), [s] "s"(s) : "memory");
    };
    const int b = blockIdx.x;
    const bool big = b < 224;
    const rsrc_t rws = mk_rsrc(ws);
    const lptr stg = lds + 65536 + wid * 4096;
    const int jA = b & 15, gA = big ? (b >> 4) : ((b - 224) >> 4);
    const int gB = big ? (b / 56) : 2 + ((b - 224) >> 4), cB = big ? (b % 56) : jA;
    bf16x8 but = {};
    if (big && (lane & 15) < 2) but = *(const bf16x8*)(ws + WS_BUT + (((gB * 8 + wid) * 2 + (lane & 15)) * 4 + (lane >> 4)) * 16);
    if (wid == 0) STAMP(ws, 2, 0);
    TXP(ws, 2);
    if (wid >= 4) {
        g64_loader<16, QA_NS, 0, NoEx>(ws + WS_H + (size_t)p * 262144, ws + WS_WQ + (size_t)j * 131072, ldsb, wid - 4, lane, []<int I>() {});
        bar();
    } else {
        f32x16 r[2] = {};
        g64_compute<16, QA_NS>(lds, wid, lane, r, [&](int i) {
            if (i >= 0 && i < 8) { kv_piece(0, QA_KV0, wid, i); kv_piece(0, QA_KV0, wid + 4, i); }
        });
        if (wid == 0) STAMP(ws, 2, 1);
#pragma unroll
        for (int tb = 0; tb < 2; ++tb)
#pragma unroll
            for (int s = 0; s < 2; ++s) {
                u32x4 o;
#pragma unroll
                for (int i = 0; i < 4; ++i) o[i] = pk2(r[tb][8 * s + 2 * i] * QSCALE, r[tb][8 * s + 2 * i + 1] * QSCALE);
                *(LDSP u32x4*)(lds + 32768 + ((wid * 2 + tb) * 2 + s) * 1024 + lane * 16) = o;
            }
        wait_vm<0>();
        lgkm_bar();
    }
    const int qg = wid & 3, kh = wid >> 2;
    const int lo = kh * 16384 + lane * 16;
    bf16x8 qA0, qA1, qB0, qB1, k0, k1, v0, v1;
    {
        const lptr qp = lds + 32768 + qg * 4096 + lane * 16;
        qA0 = lds_b128(qp); qA1 = lds_b128(qp + 1024); qB0 = lds_b128(qp + 2048); qB1 = lds_b128(qp + 3072);
        k0 = lds_b128(lds + QA_KV0 + lo); k1 = lds_b128(lds + QA_KV0 + lo + 1024);
    }
    bf16x8 sel;
    {
        const unsigned on = ((lane & 15) == ((lane >> 4) & 1)) ? 0x3f803f80u : 0u;
        sel = __builtin_bit_cast(bf16x8, (u32x4){on, on, on, on});
    }
    f32x16 oA = {}, oB = {}, sA, sB;
    f32x4 lA = {}, lB = {};
    const f32x16 zero = {};
    u32x4 pA0, pA1, pB0 = {0, 0, 0, 0}, pB1 = {0, 0, 0, 0};
    PIN(but);
    lgkm_bar();
    if (wid == 0) STAMP(ws, 2, 2);
    TXP(ws, 3);
#define EXP2(E, d, S, i) do { E[d] = __builtin_amdgcn_exp2f(S[i]); E[d + 1] = __builtin_amdgcn_exp2f(S[i + 1]); PIN(E); } while (0)
#define EXP4(E, S, i) do { E[0] = __builtin_amdgcn_exp2f(S[i]); E[1] = __builtin_amdgcn_exp2f(S[i + 1]); E[2] = __builtin_amdgcn_exp2f(S[i + 2]); E[3] = __builtin_amdgcn_exp2f(S[i + 3]); } while (0)
#define PK2(P, d, E) do { P[d] = pk2(E[0], E[1]); P[d + 1] = pk2(E[2], E[3]); PIN(P); } while (0)
#define BF(x) __builtin_bit_cast(bf16x8, x)
#define HALF(SX, PX0, PX1, SY, QY0, QY1, OZ, LZ, PZ0, PZ1, R0, R1, R2, R3) do { \
        SY = mfma32(k0, QY0, zero); SBAR(); PK2(PZ1, 2, e); R0; SBAR(); EXP4(e, SX, 0); PIN(e); SBAR(); \
        SY = mfma32(k1, QY1, SY); SBAR(); PK2(PX0, 0, e); R1; SBAR(); EXP2(e, 0, SX, 4); SBAR(); LZ = mfma16(sel, BF(PZ0), LZ); SBAR(); EXP2(e, 2, SX, 6); SBAR(); \
        OZ = mfma32(v0, BF(PZ0), OZ); SBAR(); PK2(PX0, 2, e); R2; SBAR(); EXP4(e, SX, 8); PIN(e); SBAR(); \
        OZ = mfma32(v1, BF(PZ1), OZ); SBAR(); PK2(PX1, 0, e); R3; SBAR(); EXP2(e, 0, SX, 12); SBAR(); LZ = mfma16(sel, BF(PZ1), LZ); SBAR(); EXP2(e, 2, SX, 14); SBAR(); } while (0)
    sA = mfma32(k0, qA0, zero); sA = mfma32(k1, qA1, sA);
    v0 = k0; v1 = k1;
    f32x4 e = {0, 0, 0, 0};
    bf16x8 w0, w1_, sf[4];
    f32x4 ut[4];
    u32x4 sfl;
    unsigned junk = 0;
    const lptr tp32 = stg + (4 * (lane >> 5) + ((lane & 15) >> 2)) * 128 + ((lane >> 4) & 1) * 32 + (lane & 3) * 8;
    const lptr tp16 = stg + (16 * (lane >> 5) + 4 * ((lane >> 4) & 1) + ((lane & 15) >> 2)) * 128 + (lane & 3) * 8;
    unsigned dstA = big ? WS_W2 + (jA >> 1) * 917504 + (gA * 8 + wid) * 8192 + (jA & 1) * 4096 : WS_WO + jA * 131072 + (gA * 8 + wid) * 4096, dstB[4];
#pragma unroll
    for (int i = 0; i < 4; ++i) {
        const int nbk = cB * 4 + i, jt = nbk / 14;
        dstB[i] = big ? WS_W1 + jt * 458752 + (gB * 8 + wid) * 14336 + (nbk - jt * 14) * 1024 : WS_WO + cB * 131072 + (gB * 8 + wid) * 4096 + i * 1024;
        asm volatile("" : "+s"(dstB[i]));
    }
    const int ldB = big ? F_ : D_;
    const u16* srcA = (big ? w2 : wo) + jA * 64 + (size_t)(gA * 256 + wid * 32) * D_;
    const u16* srcB = (big ? w1 : wo) + cB * 64 + (size_t)(gB * 256 + wid * 32) * ldB;
    asm volatile("" : "+s"(dstA), "+s"(srcA), "+s"(srcB));
    auto side = [&]<int C, int IT, int I>() {
        if constexpr (C < 3 && IT < 2 && !(AA & 2)) kv_piece(C + 1, (C & 1) ? QA_KV0 : 0, wid, IT * 4 + I);
        if constexpr (AA & 1) return;
        if constexpr ((C == 0 && IT == 2) || (C == 1 && IT == 4)) {
            const int ld = C == 0 ? D_ : ldB;
            const u16* s = (C == 0 ? srcA : srcB) + I * 8 * ld;
            const unsigned vo = (unsigned)((lane >> 3) * ld * 2 + (lane & 7) * 16), m = ldsb + 65536 + wid * 4096 + I * 1024;
            asm volatile(SETM0(m) GLDS(v, s, 0)::[m] "s"(m), [v] "v"(vo), [s] "s"(s) : "memory");
        }
        if constexpr ((C == 1 || C == 2) && IT == 2) {
            if constexpr (I == 0) wait_vm<8>();
            const lptr q = !big ? tp32 + (I >> 1) * 2048 + (I & 1) * 64 : (C == 2 ? tp16 + I * 32 : tp16 + (lane & 3) * 8 + (I >> 1) * 64 + (I & 1) * 8);
            sf[I] = cat(lds_tr(q), lds_tr(q + 1024));
        }
        if constexpr ((C == 1 || C == 2) && IT == 3) {
            const unsigned dst = C == 1 ? dstA + I * 1024 : dstB[I];
            if constexpr (C == 2) ut[I] = mfma16(sf[I], but, (f32x4){0, 0, 0, 0});
            st16(rws, lane * 16, dst, __builtin_bit_cast(u32x4, sf[I]));
        }
        if constexpr (C == 2 && IT == 4) *(LDSP f32x4*)(stg + I * 1024 + lane * 16) = ut[I];
        if constexpr (PREW && C == 3 && IT == 2 && I < 2)
            touch(junk, (I * 64 + lane < 112 ? I * 64 + lane : 0) * 64, ws + WS_G + p * (F_ / 8 * 2048) + j * 57344 + wid * 7168);
        if constexpr (C == 3 && IT == 0 && I == 0) { if (wid == 0) grid_post(ws, FL_SIDE, 1u, lane); }
        if constexpr (C == 3 && IT == 0) {
            if (wid == 0) {
                const lptr q = lds + 65536 + I * 8192 + (lane >> 4) * 1024 + ((lane >> 2) & 3) * 256 + (lane & 3) * 4;
                ut[I] = (f32x4){*(const LDSP float*)q, *(const LDSP float*)(q + 16), *(const LDSP float*)(q + 4096), *(const LDSP float*)(q + 4096 + 16)};
            }
        }
        if constexpr (C == 3 && IT == 1 && I == 1) {
            if (wid == 0 && big) {
                const f32x4 a = (ut[0] + ut[1]) + (ut[2] + ut[3]);
#pragma unroll
                for (int i = 0; i < 2; ++i) __builtin_amdgcn_raw_buffer_store_b32(__float_as_uint(a[i] + a[2 + i]), rws, WS_UTP + ((gB * 2 + i) * F_ + cB * 64 + lane) * 4, 0, 16);
            }
        }
        if constexpr (C == 3 && IT == 6 && I == 0) {
            if (wid == 0) { wait_vm<0>(); grid_post(ws, FL_SIDE, 2u, lane); }
            async_ld(sfl, lane * 16, ws + WS_FLAG + FL_SIDE + (blockIdx.x & 7) * 1024);
        }
    };
#define SIDE(I) side.template operator()<C, IT, I>()
    auto step = [&]<int C, int IT>() {
        const lptr kp = lds + ((C & 1) ? 0 : QA_KV0) + lo, kn = lds + ((C & 1) ? QA_KV0 : 0) + lo;
        if constexpr (PRIO && IT == 5) __builtin_amdgcn_s_setprio(2);
        if constexpr (PRIO && IT == 6) __builtin_amdgcn_s_setprio(1);
        if constexpr (IT < 7) {
            HALF(sA, pA0, pA1, sB, qB0, qB1, oB, lB, pB0, pB1, SIDE(0), k0 = lds_b128(kp + (IT + 1) * 2048),
                 k1 = lds_b128(kp + (IT + 1) * 2048 + 1024), v0 = lds_b128(kp + 32768 + IT * 2048));
            if constexpr (PRIO && IT == 6) __builtin_amdgcn_s_setprio(0);
            HALF(sB, pB0, pB1, sA, qA0, qA1, oA, lA, pA0, pA1, v1 = lds_b128(kp + 32768 + IT * 2048 + 1024),
                 SIDE(1); if constexpr (IT == 6) w0 = lds_b128(kp + 32768 + 7 * 2048), SIDE(2); if constexpr (IT == 6) w1_ = lds_b128(kp + 32768 + 7 * 2048 + 1024), SIDE(3));
        } else {
            if constexpr (C < 3 && !(AA & 4)) {
                wait_vm<(AA & 1) || C == 2 ? 0 : (C == 1 ? 8 : 4)>();
                lgkm_bar();
                if constexpr (PRIO) __builtin_amdgcn_s_setprio(3);
            }
            HALF(sA, pA0, pA1, sB, qB0, qB1, oB, lB, pB0, pB1, (void)0, if constexpr (C < 3) k0 = lds_b128(kn), if constexpr (C < 3) k1 = lds_b128(kn + 1024), (void)0);
            v0 = w0; v1 = w1_;
            HALF(sB, pB0, pB1, sA, qA0, qA1, oA, lA, pA0, pA1, (void)0, (void)0, (void)0, (void)0);
        }
    };
    if constexpr (PRIO) __builtin_amdgcn_s_setprio(3);
    [&]<int... N>(std::integer_sequence<int, N...>) { (step.template operator()<N / 8, N % 8>(), ...); }(std::make_integer_sequence<int, 32>{});
    PK2(pB1, 2, e);
    oB = mfma32(v0, BF(pB0), oB); oB = mfma32(v1, BF(pB1), oB);
    lB = mfma16(sel, BF(pB0), lB); lB = mfma16(sel, BF(pB1), lB);
    PIN(lA); PIN(lB); PIN(oA); PIN(oB);
    if (wid == 0) STAMP(ws, 2, 3);
    const lptr ex = lds + QA_KV0;
    {
        const f32x16& so = kh ? oA : oB;
        const f32x4& sl = kh ? lA : lB;
#pragma unroll
        for (int i = 0; i < 4; ++i) *(LDSP f32x4*)(ex + wid * 4096 + i * 1024 + lane * 16) = (f32x4){so[4 * i], so[4 * i + 1], so[4 * i + 2], so[4 * i + 3]};
        const f32x4& ml = kh ? lB : lA;
        if (lane < 16) {
            *(LDSP float*)(ex + 32768 + wid * 256 + lane * 4) = sl[0]; *(LDSP float*)(ex + 32768 + wid * 256 + 64 + lane * 4) = sl[1];
            *(LDSP float*)(ex + 32768 + wid * 256 + 128 + lane * 4) = ml[0]; *(LDSP float*)(ex + 32768 + wid * 256 + 192 + lane * 4) = ml[1];
        }
    }
    lgkm_bar();
    tcap<0>(TL);
    asm volatile("s_waitcnt vmcnt(0)" : "+v"(sfl), "+v"(junk)::"memory");
    if (wid >= 4) {
        if (!grid_ok(sfl, 1u)) grid_wait(ws, FL_SIDE, 1u, lane);
#pragma unroll
        for (int i = 0; i < OPRE; ++i) g64_w(ws + WS_WO + (size_t)j * 131072, ldsb, i, i, wid - 4, lane);
    }
    f32x16 o = kh ? oB : oA;
    float l;
    {
        l = *(const LDSP float*)(ex + 32768 + wid * 256 + 128 + (lane & 31) * 4) + *(const LDSP float*)(ex + 32768 + (wid ^ 4) * 256 + (lane & 31) * 4);
#pragma unroll
        for (int i = 0; i < 4; ++i) {
            const f32x4 v = *(const LDSP f32x4*)(ex + (wid ^ 4) * 4096 + i * 1024 + lane * 16);
#pragma unroll
            for (int e = 0; e < 4; ++e) o[4 * i + e] += v[e];
        }
    }
    float chk = l * 0.f;
#pragma unroll
    for (int i = 0; i < 16; ++i) chk = __builtin_fmaf(o[i], 0.f, chk);
    const bool bad = AA ? false : (!(chk == 0.f) || !(l >= 0x1p-90f));
    if (__builtin_expect(__any(bad), 0)) {
        const bf16x8 q0 = kh ? qB0 : qA0, q1 = kh ? qB1 : qA1;
        float m = -INFINITY; l = 0.f; o = zero;
        for (int kt = 0; kt < 64; ++kt) {
            const bf16x8 a0 = *(const bf16x8*)(KT + kt * 2048 + lane * 16), a1 = *(const bf16x8*)(KT + kt * 2048 + 1024 + lane * 16);
            const bf16x8 b0 = *(const bf16x8*)(VT + kt * 2048 + lane * 16), b1 = *(const bf16x8*)(VT + kt * 2048 + 1024 + lane * 16);
            f32x16 s = mfma32(a0, q0, zero); s = mfma32(a1, q1, s);
            float mx = s[0];
#pragma unroll
            for (int i = 1; i < 16; ++i) mx = fmaxf(mx, s[i]);
            mx = fmaxf(mx, __shfl_xor(mx, 32));
            const float mn = fmaxf(m, mx), al = __builtin_amdgcn_exp2f(m - mn);
            m = mn;
            u32x4 p0, p1; float rs = 0.f;
#pragma unroll
            for (int i = 0; i < 4; ++i) {
                p0[i] = pk2(__builtin_amdgcn_exp2f(s[2 * i] - m), __builtin_amdgcn_exp2f(s[2 * i + 1] - m));
                p1[i] = pk2(__builtin_amdgcn_exp2f(s[8 + 2 * i] - m), __builtin_amdgcn_exp2f(s[9 + 2 * i] - m));
                rs += bflo(p0[i]) + bfhi(p0[i]) + bflo(p1[i]) + bfhi(p1[i]);
            }
            rs += __shfl_xor(rs, 32);
            l = l * al + rs;
#pragma unroll
            for (int i = 0; i < 16; ++i) o[i] *= al;
            o = mfma32(b0, BF(p0), o); o = mfma32(b1, BF(p1), o);
        }
    }
    {
        const float inv = __builtin_amdgcn_rcpf(l);
        const unsigned oAT = WS_O + p * 262144 + (2 * j + (qg >> 1)) * 8192 + ((qg & 1) * 2 + kh) * 512, vo = (lane >> 5) * 2048 + (lane & 31) * 16;
#pragma unroll
        for (int s = 0; s < 2; ++s) {
            u32x4 w;
#pragma unroll
            for (int i = 0; i < 4; ++i) w[i] = pk2(o[8 * s + 2 * i] * inv, o[8 * s + 2 * i + 1] * inv);
            st16(rws, vo, oAT + s * 4096, w);
        }
    }
    wait_vm<0>();
    lgkm_bar();
    if (wid == 0) panel_post(ws, FL_O, p, j, lane);
    if (wid == 0) STAMP(ws, 2, 4);
}

constexpr int F1_NS = 7, F1_STAGE = 22528, F1_NST = 32, F1_AUX = F1_NS * F1_STAGE;
template <int NW> DI void f1_w(const char* sw, unsigned ldsb, int stage, int pc0, int lane) {
    const unsigned vw = pc0 * 1024 + lane * 16, mw = ldsb + (stage % F1_NS) * F1_STAGE + 8192 + pc0 * 1024;
    const char* s = sw + stage * 14336;
    if constexpr (ABL == 2) {} else if constexpr (NW == 4)
        asm volatile(SETM0(mw) GLD1(vw, s, 0) GLD1(vw, s, 1024) GLD1(vw, s, 2048) GLD1(vw, s, 3072)::[mw] "s"(mw), [vw] "v"(vw), [s] "s"(s) : "memory");
    else
        asm volatile(SETM0(mw) GLD1(vw, s, 0) GLD1(vw, s, 1024) GLD1(vw, s, 2048)::[mw] "s"(mw), [vw] "v"(vw), [s] "s"(s) : "memory");
}
template <int NW> struct OpEx { static constexpr int n(int i) { return !PROG || i < 11 ? 0 : i == 12 ? 2 * NW : NW; } };
template <int NW>
DI void op_loader(char* ws, lptr lds, int lane, int lw, int p, int j, TS& TL) {
    const unsigned ldsb = (unsigned)(uintptr_t)lds;
    const char* sw = ws + WS_WO + (size_t)j * 131072;
    const char* s1 = ws + WS_W1 + (size_t)j * 458752;
    const int pc0 = lw < 2 ? lw * 4 : 8 + (lw - 2) * 3;
    tcap<1>(TL);
#pragma unroll
    for (int i = OPRE; i < OP_NS; ++i) g64_w(sw, ldsb, i, i, lw, lane);
    panel_wait(ws, FL_O, p, j, lane);
    tcap<2>(TL);
    g64_loader<16, OP_NS, OP_NS, OpEx<NW>>(ws + WS_O + (size_t)p * 262144, sw, ldsb, lw, lane, [&]<int I>() {
        if constexpr (I == 0) { tcap<3>(TL); }
        if constexpr (I == 15) { tcap<4>(TL); }
        if constexpr (PROG && I >= 11) f1_w<NW>(s1, ldsb, I < 13 ? I - 7 : I - 13, pc0, lane);
        if constexpr (PROG && I == 12) f1_w<NW>(s1, ldsb, 6, pc0, lane);
    });
}
DI void op_compute(const u16* __restrict__ x, char* __restrict__ ws, lptr lds, int lane, int wid, int p, int j) {
    if (wid == 0) STAMP(ws, 3, 0);
    TXP(ws, 4);
    const int th = wid & 1, nb = wid >> 1, hi = lane >> 5;
    const int n0 = j * 64 + nb * 32 + 4 * hi;
    const float* mod = (const float*)(ws + WS_MOD);
    u32x4 xq[2][2]; f32x4 ga[4], a2[4];
    f32x16 r[2] = {};
    if (wid == 0) STAMP(ws, 3, 1);
    g64_compute<16, OP_NS>(lds, wid, lane, r, [&](int i) {
        if (i == -1) {
#pragma unroll
            for (int tb = 0; tb < 2; ++tb)
#pragma unroll
                for (int h = 0; h < 2; ++h) xq[tb][h] = *(const u32x4*)(x + (size_t)(p * 128 + th * 64 + tb * 32 + (lane & 31)) * D_ + j * 64 + nb * 32 + h * 16 + hi * 8);
#pragma unroll
            for (int q = 0; q < 4; ++q) {
                ga[q] = *(const f32x4*)(mod + 2048 + n0 + 8 * q);
                a2[q] = *(const f32x4*)(mod + 4096 + n0 + 8 * q);
            }
            SBAR();
        }
    });
    if (wid == 0) STAMP(ws, 3, 2);
#pragma unroll
    for (int tb = 0; tb < 2; ++tb) {
        u32x4 xo[2], yo[2]; float v[16]; float s = 0.f;
        u32x2 xr[2][4];
#pragma unroll
        for (int h = 0; h < 2; ++h) {
            const auto s0 = __builtin_amdgcn_permlane32_swap(xq[tb][h][0], xq[tb][h][2], false, false);
            const auto s1 = __builtin_amdgcn_permlane32_swap(xq[tb][h][1], xq[tb][h][3], false, false);
            xr[tb][2 * h] = (u32x2){s0[0], s1[0]}; xr[tb][2 * h + 1] = (u32x2){s0[1], s1[1]};
        }
#pragma unroll
        for (int i = 0; i < 4; ++i) {
            const float x0 = bflo(xr[tb][i][0]), x1 = bfhi(xr[tb][i][0]), x2 = bflo(xr[tb][i][1]), x3 = bfhi(xr[tb][i][1]);
            const unsigned a = pk2(x0 + ga[i][0] * r[tb][4 * i], x1 + ga[i][1] * r[tb][4 * i + 1]);
            const unsigned b = pk2(x2 + ga[i][2] * r[tb][4 * i + 2], x3 + ga[i][3] * r[tb][4 * i + 3]);
            xo[i >> 1][(i & 1) * 2] = a; xo[i >> 1][(i & 1) * 2 + 1] = b;
            v[4 * i] = bflo(a); v[4 * i + 1] = bfhi(a); v[4 * i + 2] = bflo(b); v[4 * i + 3] = bfhi(b);
            yo[i >> 1][(i & 1) * 2] = pk2(v[4 * i] * a2[i][0], v[4 * i + 1] * a2[i][1]);
            yo[i >> 1][(i & 1) * 2 + 1] = pk2(v[4 * i + 2] * a2[i][2], v[4 * i + 3] * a2[i][3]);
            s += (v[4 * i] + v[4 * i + 1]) + (v[4 * i + 2] + v[4 * i + 3]);
        }
        const unsigned off = p * 262144 + (j * 2 + nb) * 8192 + (th * 64 + tb * 32) * 16, vo = hi * 2048 + (lane & 31) * 16;
#pragma unroll
        for (int h = 0; h < 2; ++h) {
            st16(mk_rsrc(ws), vo, WS_Y + off + h * 4096, yo[h]);
            const auto s0 = __builtin_amdgcn_permlane32_swap(xo[h][0], xo[h][2], false, false);
            const auto s1 = __builtin_amdgcn_permlane32_swap(xo[h][1], xo[h][3], false, false);
            st16(mk_rsrc(ws), vo, WS_X2 + off + h * 4096, (u32x4){s0[0], s1[0], s0[1], s1[1]});
        }
        s += __shfl_xor(s, 32);
        const float mean = s * (1.f / 32.f);
        float q = 0.f;
#pragma unroll
        for (int i = 0; i < 16; ++i) { const float d = v[i] - mean; q += d * d; }
        q += __shfl_xor(q, 32);
        if (hi == 0) __builtin_amdgcn_raw_buffer_store_b64((u32x2){__float_as_uint(mean), __float_as_uint(q)}, mk_rsrc(ws),
                                                           WS_ST + ((j * 2 + nb) * S_ + p * 128 + th * 64 + tb * 32 + lane) * 8, 0, 16);
    }
    wait_vm<0>();
    bar();
    if (wid == 0) panel_post(ws, FL_Y, p, j, lane);
    if (wid == 0) STAMP(ws, 3, 3);
}

#define F2_D 6
#define F2_MF 2
constexpr int F2_NS = 10, F2_STAGE = 16384, F2_MAIN = 56 - F2_D, F2_NST = 56 + F2_D;
DI void f2_w(char* ws, unsigned ldsb, int i, int lw, int lane) {
    const int b = blockIdx.x;
    const unsigned mw = ldsb + i * F2_STAGE + 8192 + lw * 2048, va = lw * 2048 + lane * 16;
    const char* s = ws + WS_W2 + (size_t)((b >> 3) & 7) * 917504 + ((b & 1) ? (56 + F2_D) * 8192 : 0) + i * 8192;
    if (ABL != 2) asm volatile(SETM0(mw) GLD1(va, s, 0) GLD1(va, s, 1024)::[mw] "s"(mw), [va] "v"(va), [s] "s"(s) : "memory");
}
struct F1Ex { static constexpr int n(int m) { return !PROG || m < 13 ? 0 : m == 14 ? 4 : 6; } };
template <int NW> struct F1Cnt { static constexpr int n(int s) { return (s < F1_NS ? 2 : 2 + NW) + (s == 2 ? 16 : 0); } };
template <int NW>
DI void f1_loader(char* ws, int p, int j, lptr lds, int lw, int lane, TS& TL) {
    const char* sa = ws + WS_Y + (size_t)p * 262144;
    const char* sw = ws + WS_W1 + (size_t)j * 458752;
    const unsigned ldsb = (unsigned)(uintptr_t)lds;
    const int pc0 = lw < 2 ? lw * 4 : 8 + (lw - 2) * 3;
    const unsigned va = lw * 2048 + lane * 16;
#pragma unroll
    for (int i = 0; i < F1_NS; ++i) if (i != 3 && !PROG) f1_w<NW>(sw, ldsb, i, pc0, lane);
    if (NW == 3) {
        grid_wait(ws, FL_SIDE, 2u, lane);
        if (lane < 56) {
            const unsigned o = WS_UTP + ((lw - 2) * F_ + j * 224 + lane * 4) * 4;
            const rsrc_t r = mk_rsrc(ws);
            const f32x4 u = (ld16f(r, o) + ld16f(r, o + 2 * F_ * 4)) + (ld16f(r, o + 4 * F_ * 4) + ld16f(r, o + 6 * F_ * 4));
            *(LDSP f32x4*)(lds + F1_AUX + (lw - 2) * 1024 + lane * 16) = u * (lw == 2 ? 1.f : -1.4426950408889634f);
        }
    }
    bar();
    tcap<5>(TL);
    f1_w<NW>(sw, ldsb, 3, pc0, lane);
    panel_wait(ws, FL_Y, p, j, lane);
    tcap<6>(TL);
    f32x2 sp[16];
    pair_loader<F1_NST, F1_NS, F1Cnt<NW>, F1Ex>([&]<int S>() {
        const unsigned ma = ldsb + (S % F1_NS) * F1_STAGE + lw * 2048;
        if constexpr (ABL != 2) asm volatile(SETM0(ma) GLD1(va, sa, 0) GLD1(va, sa, 1024)::[ma] "s"(ma), [va] "v"(va), [sa] "s"(sa) : "memory");
        if constexpr (S >= F1_NS) f1_w<NW>(sw, ldsb, S, pc0, lane);
        sa += 8192;
        if constexpr (S == 2) {
            const char* s = ws + WS_ST + (size_t)(p * 128 + lw * 32) * 8;
            const unsigned v0 = (lane >> 5) * 16 * (S_ * 8) + (lane & 31) * 8;
#pragma unroll
            for (int i = 0; i < 16; ++i) async_ld(sp[i], v0 + i * (S_ * 8), s);
        }
    }, [&]<int M, int PH>() {
        if constexpr (M == 0 && PH == 0) {
#pragma unroll
            for (int i = 0; i < 16; ++i) PIN(sp[i]);
        }
        if constexpr (PROG && M == 13 && PH == 1) { f2_w(ws, ldsb, 5, lw, lane); f2_w(ws, ldsb, 6, lw, lane); f2_w(ws, ldsb, 7, lw, lane); }
        if constexpr (PROG && M == 14 && PH == 1) { f2_w(ws, ldsb, 0, lw, lane); f2_w(ws, ldsb, 8, lw, lane); }
        if constexpr (PROG && M == 15 && PH == 1) { f2_w(ws, ldsb, 1, lw, lane); f2_w(ws, ldsb, 2, lw, lane); f2_w(ws, ldsb, 3, lw, lane); }
        if constexpr (M == -1 && PH == 1) { tcap<7>(TL); }
        if constexpr (M == 15 && PH == 1) { tcap<8>(TL); }
        if constexpr (M == 2 && PH == 1) {
            float s = 0.f;
#pragma unroll
            for (int i = 0; i < 16; ++i) s += sp[i][0];
            const float mu = swap32_add(s) * (1.f / 32.f);
            float q = 0.f;
#pragma unroll
            for (int i = 0; i < 16; ++i) { const float d = sp[i][0] - mu; q += sp[i][1] + 32.f * d * d; }
            const float rl = -1.4426950408889634f * __builtin_amdgcn_rsqf(swap32_add(q) * (1.f / 1024.f) + 1e-5f);
            if (lane < 32) *(LDSP f32x2*)(lds + F1_AUX + 2048 + (lw * 32 + lane) * 8) = (f32x2){mu, rl};
            asm volatile("s_waitcnt lgkmcnt(0)" ::: "memory");
        }
    });
}
struct F1Frag { bf16x8 a[4], w[7]; };
DI void f1_compute(char* __restrict__ ws, lptr lds, int lane, int wid, int p, int j) {
    if (wid == 0) STAMP(ws, 4, 0);
    TXP(ws, 5);
    const int th = wid & 1, nh = wid >> 1, g = lane >> 4;
    f32x4 acc[7][4] = {};
    const LP pa = mk_lp(lds + g * 2048 + (th * 64 + (lane & 15)) * 16), pw = mk_lp(lds + nh * 7168 + lane * 16);
    F1Frag fr[2];
    auto rd = [&]<int S, int I>() {
        constexpr int so = (S % F1_NS) * F1_STAGE;
        if constexpr (S < F1_NST && I < 11) {
            if constexpr (I >= 1 && I <= 4) fr[S & 1].a[I - 1] = lds_at<so + (I - 1) * 256>(pa);
            else fr[S & 1].w[I ? I - 4 : 0] = lds_at<so + 8192 + (I ? I - 4 : 0) * 1024>(pw);
        }
    };
    auto step = [&]<int S>() {
        if constexpr (S % 2 == 0) { lgkm_bar(); SBAR(); }
        if constexpr (ABL != 1)
            [&]<int... U>(std::integer_sequence<int, U...>) {
                ((acc[U / 4][U % 4] = mfma16(fr[S & 1].w[U / 4], fr[S & 1].a[U % 4], acc[U / 4][U % 4]), SBAR(), rd.template operator()<S + 1, U>(), SBAR()), ...);
            }(std::make_integer_sequence<int, 28>{});
    };
    if (wid == 0) STAMP(ws, 4, 1);
    bar();
    if (wid == 0) STAMP(ws, 4, 2);
    [&]<int... I>(std::integer_sequence<int, I...>) { (rd.template operator()<0, I>(), ...); }(std::make_integer_sequence<int, 11>{});
    [&]<int... S>(std::integer_sequence<int, S...>) { (step.template operator()<S>(), ...); }(std::make_integer_sequence<int, F1_NST>{});
    if (wid == 0) STAMP(ws, 4, 3);
    f32x2 st[4];
#pragma unroll
    for (int tb = 0; tb < 4; ++tb) st[tb] = *(const LDSP f32x2*)(lds + F1_AUX + 2048 + (th * 64 + tb * 16 + (lane & 15)) * 8);
    const lptr pu = lds + F1_AUX + (nh * 112 + 4 * g) * 4;
    const unsigned gp = WS_G + p * (F_ / 8 * 2048) + (j * 14 + nh * 7) * 4096 + th * 1024, vo = (g & 1) * 2048 + ((lane >> 5) * 16 + (lane & 15)) * 16;
    f32x4 u = *(const LDSP f32x4*)pu, tt = *(const LDSP f32x4*)(pu + 1024);
    const f32x2 cl = {-1.4426950408889634f, -1.4426950408889634f};
#pragma unroll
    for (int b = 0; b < 7; ++b) {
        f32x4 un, tn;
        if (b < 6) { un = *(const LDSP f32x4*)(pu + (b + 1) * 64); tn = *(const LDSP f32x4*)(pu + 1024 + (b + 1) * 64); }
        unsigned w[4][2];
#pragma unroll
        for (int tb = 0; tb < 4; ++tb)
#pragma unroll
            for (int e = 0; e < 2; ++e) {
                const f32x2 a = __builtin_elementwise_fma((f32x2){u[2 * e], u[2 * e + 1]}, (f32x2){-st[tb][0], -st[tb][0]}, (f32x2){acc[b][tb][2 * e], acc[b][tb][2 * e + 1]});
                const f32x2 z = __builtin_elementwise_fma(a, (f32x2){st[tb][1], st[tb][1]}, (f32x2){tt[2 * e], tt[2 * e + 1]});
                const f32x2 dn = __builtin_elementwise_fma((f32x2){__builtin_amdgcn_exp2f(z[0]), __builtin_amdgcn_exp2f(z[1])}, cl, cl);
                const f32x2 o = z * (f32x2){__builtin_amdgcn_rcpf(dn[0]), __builtin_amdgcn_rcpf(dn[1])};
                w[tb][e] = pk2(o[0], o[1]);
            }
#pragma unroll
        for (int tp = 0; tp < 2; ++tp) {
            const auto s0 = __builtin_amdgcn_permlane32_swap(w[2 * tp][0], w[2 * tp + 1][0], false, false);
            const auto s1 = __builtin_amdgcn_permlane32_swap(w[2 * tp][1], w[2 * tp + 1][1], false, false);
            st16(mk_rsrc(ws), vo + tp * 512, gp + b * 4096, (u32x4){s0[0], s1[0], s0[1], s1[1]});
        }
        u = un; tt = tn;
        SBAR();
    }
    wait_vm<0>();
    lgkm_bar();
    if (wid == 0) panel_post(ws, FL_G, p, j, lane);
    if (wid == 0) STAMP(ws, 4, 4);
}

struct F2Cnt { static constexpr int n(int s) { return s < F2_NS ? 2 : s < F2_MAIN ? 4 : 3; } };
struct F2Frag { bf16x8 a[4], w[4]; };
DI void f2_loader(char* ws, lptr lds, int lane, int lw, int p, int j, TS& TL) {
    const int b = blockIdx.x, kh = b & 1, jn = (b >> 3) & 7;
    const unsigned ldsb = (unsigned)(uintptr_t)lds;
    const char* sa = ws + WS_G + (size_t)p * (F_ / 8 * 2048) + (kh ? (56 + F2_D) * 8192 : 0);
    const char* sw = ws + WS_W2 + (size_t)jn * 917504 + (kh ? (56 + F2_D) * 8192 : 0);
    const unsigned va = lw * 2048 + lane * 16, vk = ((lw >> 1) * 4 + kh * 2 + (lw & 1)) * 1024 + lane * 16;
#pragma unroll
    for (int i = 0; i < F2_NS; ++i) if (i != 4 && i != 9 && !PROG) f2_w(ws, ldsb, i, lw, lane);
    bar();
    tcap<9>(TL);
    f2_w(ws, ldsb, 4, lw, lane); f2_w(ws, ldsb, 9, lw, lane);
    panel_wait(ws, FL_G, p, j, lane);
    tcap<10>(TL);
    pair_loader<F2_NST, F2_NS, F2Cnt, NoEx>([&]<int S>() {
        const unsigned ma = ldsb + (S % F2_NS) * F2_STAGE + lw * 2048;
        if constexpr (S == F2_MAIN) { if (kh) { sa -= 112 * 8192 - F2_MAIN * 8192 + 0; sw -= 112 * 8192 - F2_MAIN * 8192 + 0; } }
        if constexpr (ABL == 2) {} else if constexpr (S < F2_NS) {
            asm volatile(SETM0(ma) GLD1(va, sa, 0) GLD1(va, sa, 1024)::[ma] "s"(ma), [va] "v"(va), [sa] "s"(sa) : "memory");
        } else if constexpr (S < F2_MAIN) {
            const unsigned mw = ma + 8192;
            asm volatile(SETM0(ma) GLD1(va, sa, 0) GLD1(va, sa, 1024) SETM0(mw) GLD1(va, sw, 0) GLD1(va, sw, 1024)
                         ::[ma] "s"(ma), [mw] "s"(mw), [va] "v"(va), [sa] "s"(sa), [sw] "s"(sw) : "memory");
        } else {
            const unsigned mw = ldsb + (S % F2_NS) * F2_STAGE + 8192 + lw * 1024;
            asm volatile(SETM0(ma) GLD1(va, sa, 0) GLD1(va, sa, 1024) SETM0(mw) GLD1(vk, sw, 0)
                         ::[ma] "s"(ma), [mw] "s"(mw), [va] "v"(va), [vk] "v"(vk), [sa] "s"(sa), [sw] "s"(sw) : "memory");
        }
        sa += 8192; sw += 8192;
    }, [&]<int M, int PH>() { if constexpr (M == -1 && PH == 1) { tcap<11>(TL); } if constexpr (M == 4 && PH == 1) { tcap<12>(TL); } });
}
DI void f2_compute(char* __restrict__ ws, u16* __restrict__ out, lptr lds, int lane, int wid, int p) {
    const int b = blockIdx.x, kh = b & 1, jn = (b >> 3) & 7;
    if (wid == 0) STAMP(ws, 5, 0);
    TXP(ws, 6);
    const int th = wid & 1, nh = wid >> 1, g = lane >> 4;
    f32x4 acc[4][4] = {};
    const LP pa = mk_lp(lds + g * 2048 + (th * 64 + (lane & 15)) * 16);
    const LP pk = mk_lp(lds + (nh * 4 + kh * 2) * 1024 + lane * 16), ps = mk_lp(lds + (nh * 4 + (kh ^ 1) * 2) * 1024 + lane * 16);
    F2Frag fr[2];
    LP pt;
    auto rd = [&]<int S, int I>() {
        constexpr int so = (S % F2_NS) * F2_STAGE;
        if constexpr (S < F2_NST && I < (S < F2_MAIN ? 8 : 6)) {
            if constexpr (I >= 1 && I <= 4) fr[S & 1].a[I - 1] = lds_at<so + (I - 1) * 256>(pa);
            else if constexpr (I >= 6) fr[S & 1].w[I - 4] = lds_at<so + 8192 + (I - 6) * 1024>(ps);
            else fr[S & 1].w[I ? 1 : 0] = lds_at<so + 8192 + (I ? 1024 : 0)>(S < F2_MAIN ? pk : pt);
        }
    };
    auto step = [&]<int S>() {
        if constexpr (ABL != 1)
            [&]<int... U>(std::integer_sequence<int, U...>) {
                ((acc[U / 4][U % 4] = mfma16(fr[S & 1].w[U / 4], fr[S & 1].a[U % 4], acc[U / 4][U % 4]), SBAR(), rd.template operator()<S + 1, U>(), SBAR()), ...);
            }(std::make_integer_sequence<int, S < F2_MAIN ? 16 : 8>{});
    };
    if (wid == 0) STAMP(ws, 5, 1);
    bar();
    [&]<int... I>(std::integer_sequence<int, I...>) { (rd.template operator()<0, I>(), ...); }(std::make_integer_sequence<int, 8>{});
    static_assert(F2_MAIN % 2 == 0 && F2_D >= 4, "");
    [&]<int... M>(std::integer_sequence<int, M...>) { ((lgkm_bar(), SBAR(), step.template operator()<2 * M>(), step.template operator()<2 * M + 1>()), ...); }
    (std::make_integer_sequence<int, F2_MAIN / 2 - 1>{});
    pt = mk_lp(lds + nh * 2048 + lane * 16);
    lgkm_bar(); SBAR();
    step.template operator()<F2_MAIN - 2>(); step.template operator()<F2_MAIN - 1>();
    if (wid == 0) STAMP(ws, 5, 2);
    const rsrc_t rws = mk_rsrc(ws);
    const unsigned exo = WS_EX + wid * 8192 + lane * 16;
#pragma unroll
    for (int i = 0; i < 8; ++i) st16<16>(rws, exo + i * 1024, b * 32768, __builtin_bit_cast(u32x4, acc[2 + (i >> 2)][i & 3]));
    SBAR();
    __attribute__((address_space(1))) unsigned* flags = (__attribute__((address_space(1))) unsigned*)(ws + WS_FLAG + FL_EX);
    u32x4 xr[4], pr[8]; f32x4 gb[2];
    const int c8 = jn * 16 + nh * 8 + kh * 4 + g;
    TS TT; TCAP(TT, 0);
    auto tail = [&]<int M>() {
        if constexpr (M == F2_MF) wait_vm<0>();
        if constexpr (M == F2_D - 2) {
            if (wid == 0) {
                for (int spin = 0; spin < (1 << 22) && __hip_atomic_load(flags + (b ^ 1) * 32, __ATOMIC_RELAXED, __HIP_MEMORY_SCOPE_AGENT) == 0; ++spin) __builtin_amdgcn_s_sleep(1);
            }
        }
        lgkm_bar(); SBAR();
        if constexpr (M == F2_MF) {
            if (wid == 0 && lane == 0) __hip_atomic_store(flags + b * 32, 1u, __ATOMIC_RELAXED, __HIP_MEMORY_SCOPE_AGENT);
#pragma unroll
            for (int tb = 0; tb < 4; ++tb) xr[tb] = ld16(rws, WS_X2 + p * 262144 + c8 * 2048 + (th * 64 + tb * 16 + (lane & 15)) * 16);
            gb[0] = *(const f32x4*)(ws + WS_MOD + (5120 + c8 * 8) * 4); gb[1] = *(const f32x4*)(ws + WS_MOD + (5120 + c8 * 8 + 4) * 4);
            SBAR();
        }
        if constexpr (M == F2_D - 2) {
#pragma unroll
            for (int i = 0; i < 8; ++i) pr[i] = __builtin_amdgcn_raw_buffer_load_b128(rws, exo + i * 1024 + (b ^ 1) * 32768, 0, 16);
            SBAR();
        }
        step.template operator()<F2_MAIN + 2 * M>(); step.template operator()<F2_MAIN + 2 * M + 1>();
    };
    [&]<int... I>(std::integer_sequence<int, I...>) { (tail.template operator()<I>(), ...); }(std::make_integer_sequence<int, F2_D>{});
    if (wid == 0) STAMP(ws, 5, 3);
#pragma unroll
    for (int tb = 0; tb < 4; ++tb) {
        const f32x4 lo = acc[0][tb] + __builtin_bit_cast(f32x4, pr[tb]), hi = acc[1][tb] + __builtin_bit_cast(f32x4, pr[4 + tb]);
        const u32x4 xv = xr[tb];
        const u32x4 o = {pk2(bflo(xv[0]) + gb[0][0] * lo[0], bfhi(xv[0]) + gb[0][1] * lo[1]), pk2(bflo(xv[1]) + gb[0][2] * lo[2], bfhi(xv[1]) + gb[0][3] * lo[3]),
                         pk2(bflo(xv[2]) + gb[1][0] * hi[0], bfhi(xv[2]) + gb[1][1] * hi[1]), pk2(bflo(xv[3]) + gb[1][2] * hi[2], bfhi(xv[3]) + gb[1][3] * hi[3])};
        st16(mk_rsrc(out), (lane & 15) * (D_ * 2) + c8 * 16, (p * 128 + th * 64 + tb * 16) * (D_ * 2), o);
    }
    if (wid == 0) STAMP(ws, 5, 4);
    TXP(ws, 7);
    if (wid == 0) TDUMP(TT, ws, 7, F2_D + 1);
}

constexpr int MAIN_LDS = 163840;
__global__ void __launch_bounds__(512) k_main(const u16* __restrict__ x, const u16* __restrict__ wo, const u16* __restrict__ w1, const u16* __restrict__ w2,
                                              char* __restrict__ ws, u16* __restrict__ out) {
    extern __shared__ __attribute__((aligned(16))) char smem[];
    const lptr lds = (lptr)smem;
    const int lane = threadIdx.x & 63, wid = __builtin_amdgcn_readfirstlane(threadIdx.x >> 6);
    int p, j; tile_map(blockIdx.x, p, j);
    TS TL;
    ph_qattn(wo, w1, w2, ws, lds, lane, wid, p, j, TL);
    if (wid >= 4) {
        if (wid < 6) { op_loader<4>(ws, lds, lane, wid - 4, p, j, TL); f1_loader<4>(ws, p, j, lds, wid - 4, lane, TL); }
        else { op_loader<3>(ws, lds, lane, wid - 4, p, j, TL); f1_loader<3>(ws, p, j, lds, wid - 4, lane, TL); }
        f2_loader(ws, lds, lane, wid - 4, p, j, TL);
        if (wid == 4) TDUMP(TL, ws, 6, 13);
    } else {
        op_compute(x, ws, lds, lane, wid, p, j);
        f1_compute(ws, lds, lane, wid, p, j);
        f2_compute(ws, out, lds, lane, wid, p);
    }
}

extern "C" void kernel_launch(void* const* d_in, const int* in_sizes, int n_in, void* d_out, int out_size, void* d_ws, size_t ws_size,
                              hipStream_t stream) {
    const u16* x = (const u16*)d_in[0];
    const u16* wq = (const u16*)d_in[1];
    const u16* wk = (const u16*)d_in[2];
    const u16* wv = (const u16*)d_in[3];
    const u16* wo = (const u16*)d_in[4];
    const u16* w1 = (const u16*)d_in[6];
    const u16* w2 = (const u16*)d_in[7];
    const u16* c = (const u16*)d_in[8];
    const u16* wmod = (const u16*)d_in[9];
    char* ws = (char*)d_ws;
    (void)hipFuncSetAttribute((const void*)k_ln_kv, hipFuncAttributeMaxDynamicSharedMemorySize, K2_LDS);
    (void)hipFuncSetAttribute((const void*)k_main, hipFuncAttributeMaxDynamicSharedMemorySize, MAIN_LDS);
    k_prep<<<256, 512, 32768, stream>>>(c, wmod, wq, ws);
    k_ln_kv<<<256, 512, K2_LDS, stream>>>(x, wk, wv, ws);
    k_main<<<256, 512, MAIN_LDS, stream>>>(x, wo, w1, w2, ws, (u16*)d_out);
    k_spin<<<1, 64, 0, stream>>>(ws);
}
```

```cpp
#pragma clang diagnostic ignored "-Wc++20-extensions"
#include <hip/hip_runtime.h>
#include <hip/hip_bf16.h>
#include <stdint.h>

typedef unsigned short u16;
typedef __bf16 bf16x8 __attribute__((ext_vector_type(8)));
typedef __bf16 bf16x2 __attribute__((ext_vector_type(2)));
typedef short s16x4 __attribute__((ext_vector_type(4)));
typedef short s16x8 __attribute__((ext_vector_type(8)));
typedef float f32x16 __attribute__((ext_vector_type(16)));
typedef float f32x4 __attribute__((ext_vector_type(4)));
typedef float f32x2 __attribute__((ext_vector_type(2)));
typedef unsigned u32x4 __attribute__((ext_vector_type(4)));
typedef unsigned u32x2 __attribute__((ext_vector_type(2)));
#define LDSP __attribute__((address_space(3)))
typedef LDSP char* lptr;
#define DI __device__ __forceinline__

constexpr int S_ = 2048, D_ = 1024, F_ = 3584;
constexpr size_t WS_MOD = 0;
constexpr size_t WS_UT = 32768;
constexpr size_t WS_KT = 65536;
constexpr size_t WS_VT = WS_KT + 131072;
constexpr size_t WS_MODP = WS_VT + 131072;
constexpr size_t WS_BUT = WS_MODP + 98304;
constexpr size_t WS_UTP = WS_BUT + 4096;
constexpr size_t WS_H = 1u << 20;
constexpr size_t WS_O = WS_H + (4u << 20);
constexpr size_t WS_X2 = WS_O + (4u << 20);
constexpr size_t WS_Y = WS_X2 + (4u << 20);
constexpr size_t WS_G = WS_Y + (4u << 20);
constexpr size_t WS_WQ = 32u << 20;
constexpr size_t WS_WO = WS_WQ + (2u << 20);
constexpr size_t WS_W1 = WS_WO + (2u << 20);
constexpr size_t WS_W2 = WS_W1 + (7u << 20);
constexpr size_t WS_FLAG = WS_UTP + 131072;
constexpr int FL_EX = 0, FL_O = 32768, FL_Y = 65536, FL_G = 98304, FL_H = 131072, FL_SIDE = 163840, FL_KV = FL_SIDE + 8192, FL_END = FL_KV + 8192;
constexpr size_t WS_ST = 60u << 20;
constexpr size_t WS_EX = 52u << 20;
static_assert(WS_FLAG + FL_END <= WS_H && FL_END % (256 * 4) == 0, "");
static_assert(WS_UTP + 4 * 2 * 3584 * 4 <= WS_H, "");

constexpr size_t WS_DBG = 64u << 20;
#define STAMP(ws, kid, i) do {} while (0)
struct TS {};
#define TCAP(T, i) do {} while (0)
#define TDUMP(T, ws, kid, n) do {} while (0)
#define TXP(ws, i) do {} while (0)
DI float bf2f(u16 v) { return __uint_as_float((unsigned)v << 16); }
DI float bflo(unsigned v) { return __uint_as_float(v << 16); }
DI float bfhi(unsigned v) { return __uint_as_float(v & 0xffff0000u); }
DI unsigned pk2(float a, float b) { f32x2 v = {a, b}; return __builtin_bit_cast(unsigned, __builtin_convertvector(v, bf16x2)); }
DI float rbf(float a) { return bflo(pk2(a, 0.f)); }
DI f32x16 mfma32(bf16x8 a, bf16x8 b, f32x16 c) { return __builtin_amdgcn_mfma_f32_32x32x16_bf16(a, b, c, 0, 0, 0); }
DI f32x4 mfma16(bf16x8 a, bf16x8 b, f32x4 c) { return __builtin_amdgcn_mfma_f32_16x16x32_bf16(a, b, c, 0, 0, 0); }
DI bf16x8 lds_b128(lptr p) { return *(const LDSP bf16x8*)p; }
DI s16x4 lds_tr(lptr p) { return __builtin_amdgcn_ds_read_tr16_b64_v4i16((LDSP s16x4*)p); }
DI bf16x8 cat(s16x4 a, s16x4 b) { return __builtin_bit_cast(bf16x8, __builtin_shufflevector(a, b, 0, 1, 2, 3, 4, 5, 6, 7)); }
#define WT 16
typedef __amdgpu_buffer_rsrc_t rsrc_t;
DI rsrc_t mk_rsrc(void* base) { return __builtin_amdgcn_make_buffer_rsrc(base, 0, 0x7fffffff, 0x00020000); }
template <int POL = WT> DI void st16(rsrc_t r, unsigned voff, unsigned soff, u32x4 v) { __builtin_amdgcn_raw_buffer_store_b128(v, r, voff + soff, 0, POL); }
struct LP { lptr b[3]; };
DI LP mk_lp(lptr p) { LP r = {{p, p + 65536, p + 131072}}; asm volatile("" : "+v"(r.b[0]), "+v"(r.b[1]), "+v"(r.b[2])); return r; }
template <int OFF> DI bf16x8 lds_at(const LP& p) { return *(const LDSP bf16x8*)(p.b[OFF >> 16] + (OFF & 65535)); }
#define SBAR() __builtin_amdgcn_sched_barrier(0)
#define PIN(x) asm volatile("" : "+v"(x))
template <class T> DI void pin(T& x) { PIN(x); }
template <int N> DI void wait_vm() { asm volatile("s_waitcnt vmcnt(%0)" ::"i"(N) : "memory"); }
DI void bar() { asm volatile("s_barrier" ::: "memory"); }
DI void lgkm_bar() { asm volatile("s_waitcnt lgkmcnt(0)\n\ts_barrier" ::: "memory"); }
template <int C> DI float dpp_add(float v) { return v + __builtin_bit_cast(float, __builtin_amdgcn_update_dpp(0, __builtin_bit_cast(int, v), C, 0xf, 0xf, true)); }
DI float swap16_add(float v) { const auto r = __builtin_amdgcn_permlane16_swap(__float_as_uint(v), __float_as_uint(v), false, false); return __uint_as_float(r[0]) + __uint_as_float(r[1]); }
DI float swap32_add(float v) { const auto r = __builtin_amdgcn_permlane32_swap(__float_as_uint(v), __float_as_uint(v), false, false); return __uint_as_float(r[0]) + __uint_as_float(r[1]); }
DI float wave_sum(float v) {
    v = dpp_add<0xB1>(v); v = dpp_add<0x4E>(v); v = dpp_add<0x141>(v); v = dpp_add<0x140>(v);
    return swap32_add(swap16_add(v));
}

#define ABL 0
#define AA 0
#define PRIO 1
#define GLDS(v, s, off) "global_load_lds_dwordx4 %[" #v "], %[" #s "] offset:" #off "\n\t"
#define GLD1(v, s, off) "global_load_lds_dwordx4 %[" #v "], %[" #s "] offset:" #off " sc1\n\t"
#define POLW " sc1"
#define POLA " sc1"
#define GLDW(v, s, off) "global_load_lds_dwordx4 %[" #v "], %[" #s "] offset:" #off POLW "\n\t"
#define GLDA(v, s, off) "global_load_lds_dwordx4 %[" #v "], %[" #s "] offset:" #off POLA "\n\t"
#define SETM0(m) "s_mov_b32 m0, %[" #m "]\n\ts_nop 0\n\t"

typedef __attribute__((address_space(1))) unsigned* gu32;
DI void flag_st(char* ws, unsigned off, unsigned v) { __hip_atomic_store((gu32)(ws + WS_FLAG + off), v, __ATOMIC_RELAXED, __HIP_MEMORY_SCOPE_AGENT); }
DI u32x4 ld16(rsrc_t r, unsigned off) { return __builtin_amdgcn_raw_buffer_load_b128(r, off, 0, 16); }
DI f32x4 ld16f(rsrc_t r, unsigned off) { return __builtin_bit_cast(f32x4, ld16(r, off)); }
DI void async_ld(u32x4& d, unsigned v, const char* s) { asm volatile("global_load_dwordx4 %0, %1, %2 sc1" : "=v"(d) : "v"(v), "s"(s) : "memory"); }
DI void async_ld(unsigned& d, unsigned v, const char* s) { asm volatile("global_load_dword %0, %1, %2 sc1" : "=v"(d) : "v"(v), "s"(s) : "memory"); }
DI void async_ld(f32x2& d, unsigned v, const char* s) { asm volatile("global_load_dwordx2 %0, %1, %2 sc1" : "=v"(d) : "v"(v), "s"(s) : "memory"); }
DI void touch(unsigned& d, unsigned v, const char* s) { asm volatile("global_load_dword %0, %1, %2 sc1" : "+v"(d) : "v"(v), "s"(s) : "memory"); }
DI void panel_post(char* ws, int fl, int p, int j, unsigned v, int lane) { if (lane < 16) flag_st(ws, fl + (p * 16 + lane) * 128 + j * 4, v); }
DI void panel_wait(char* ws, int fl, int p, int j, unsigned v, int lane) {
    const gu32 f = (gu32)(ws + WS_FLAG + fl + (p * 16 + j) * 128) + (lane & 15);
    for (int spin = 0; spin < (1 << 22) && !__all(__hip_atomic_load(f, __ATOMIC_RELAXED, __HIP_MEMORY_SCOPE_AGENT) >= v); ++spin) __builtin_amdgcn_s_sleep(1);
    asm volatile("" ::: "memory");
}
DI void grid_post(char* ws, int fl, unsigned v, int lane) { if (lane < 8) flag_st(ws, fl + lane * 1024 + blockIdx.x * 4, v); }
DI bool grid_ok(u32x4 f, unsigned v) { return __all(f[0] >= v && f[1] >= v && f[2] >= v && f[3] >= v); }
DI void grid_wait(char* ws, int fl, unsigned v, int lane) {
    for (int spin = 0; spin < (1 << 22) && !grid_ok(ld16(mk_rsrc(ws), WS_FLAG + fl + (blockIdx.x & 7) * 1024 + lane * 16), v); ++spin) { __builtin_amdgcn_s_sleep(1); asm volatile("" ::: "memory"); }
    asm volatile("" ::: "memory");
}

DI void tile_issue(const u16* src, int ld, unsigned ldsb, int lane) {
    const unsigned vo = (unsigned)((lane >> 3) * ld * 2 + (lane & 7) * 16);
#pragma unroll
    for (int i = 0; i < 4; ++i) {
        const u16* s = src + (size_t)i * 8 * ld;
        const unsigned m = ldsb + i * 1024;
        asm volatile(SETM0(m) GLDS(v, s, 0)::[m] "s"(m), [v] "v"(vo), [s] "s"(s) : "memory");
    }
}
template <bool PERM> DI void tile_store32(lptr st, rsrc_t r, unsigned dst, int lane) {
    const lptr p = st + ((PERM ? 4 : 8) * (lane >> 5) + ((lane & 15) >> 2)) * 128 + ((lane >> 4) & 1) * 32 + (lane & 3) * 8;
    bf16x8 f[4];
#pragma unroll
    for (int i = 0; i < 4; ++i) { const lptr q = p + (i >> 1) * 2048 + (i & 1) * 64; f[i] = cat(lds_tr(q), lds_tr(q + (PERM ? 1024 : 512))); }
#pragma unroll
    for (int i = 0; i < 4; ++i) st16(r, i * 1024 + lane * 16, dst, __builtin_bit_cast(u32x4, f[i]));
}

__global__ void __launch_bounds__(512) k_prep(const u16* __restrict__ c, const u16* __restrict__ wmod, const u16* __restrict__ wq, char* __restrict__ ws) {
    extern __shared__ __attribute__((aligned(16))) char smem[];
    const lptr lds = (lptr)smem;
    const int t = threadIdx.x, lane = t & 63, wid = __builtin_amdgcn_readfirstlane(t >> 6), b = blockIdx.x;
    if (wid == 0) STAMP(ws, 0, 0);
    TXP(ws, 0);
    if (t < FL_END / 1024) *(unsigned*)(ws + WS_FLAG + (b * (FL_END / 1024) + t) * 4) = 0u;
    if (b >= 192) {
        const int j = (b - 192) & 15, kg = (b - 192) >> 4;
        tile_issue(wq + (size_t)(kg * 256 + wid * 32) * D_ + j * 64, D_, (unsigned)(uintptr_t)lds + wid * 4096, lane);
        wait_vm<0>();
        tile_store32<false>(lds + wid * 4096, mk_rsrc(ws), WS_WQ + j * 131072 + (kg * 8 + wid) * 4096, lane);
        if (wid == 0) STAMP(ws, 0, 1);
        return;
    }
    const int cg = b % 48, kg = b / 48, ch = t & 15, rg = t >> 4;
    u32x4 w[8]; u16 cv[8];
#pragma unroll
    for (int i = 0; i < 8; ++i) w[i] = __builtin_nontemporal_load((const u32x4*)(wmod + (size_t)(kg * 256 + i * 32 + rg) * 6144 + cg * 128 + ch * 8));
#pragma unroll
    for (int i = 0; i < 8; ++i) cv[i] = c[kg * 256 + i * 32 + rg];
    float acc[8] = {0, 0, 0, 0, 0, 0, 0, 0};
#pragma unroll
    for (int i = 0; i < 8; ++i) {
        const float f = bf2f(cv[i]);
        const float s = rbf(f / (1.f + __expf(-f)));
#pragma unroll
        for (int j = 0; j < 4; ++j) { acc[2 * j] += s * bflo(w[i][j]); acc[2 * j + 1] += s * bfhi(w[i][j]); }
    }
#pragma unroll
    for (int j = 0; j < 8; ++j) acc[j] = swap32_add(swap16_add(acc[j]));
    if (lane < 16) {
        *(LDSP f32x4*)(lds + wid * 512 + ch * 32) = (f32x4){acc[0], acc[1], acc[2], acc[3]};
        *(LDSP f32x4*)(lds + wid * 512 + ch * 32 + 16) = (f32x4){acc[4], acc[5], acc[6], acc[7]};
    }
    lgkm_bar();
    if (t < 128) {
        float v = 0.f;
#pragma unroll
        for (int i = 0; i < 8; ++i) v += *(const LDSP float*)(lds + i * 512 + t * 4);
        ((float*)(ws + WS_MODP))[kg * 6144 + cg * 128 + t] = v;
    }
    if (wid == 0) STAMP(ws, 0, 1);
}

#define R0 3
constexpr int ring_issued(int i, int NST, int NS) { int n = R0 < NS - 1 ? R0 : NS - 1; for (int k = 0; k < i; ++k) { n += 2; if (n > k + NS) n = k + NS; if (n > NST) n = NST; } return n; }
template <int I> DI void tcap(TS& T) { TCAP(T, I); }
struct NoEx { static constexpr int n(int) { return 0; } };
template <int NST, int NS, class CNT, class EX, int PA, class F, class X>
DI void ring_loader(F issue, X extra) {
    auto range = [&]<int S0, int... I>(std::integer_sequence<int, I...>) { (issue.template operator()<S0 + I>(), ...); };
    auto sync = [&]<int I>() {
        constexpr int have = ring_issued(I, NST, NS);
        constexpr int from = PA >= 0 && I == PA + 2 ? ring_issued(PA + 1, NST, NS) : I + 1;
        constexpr int out = [] { int c = PA >= 0 && I == PA + 1; for (int s = from; s < have; ++s) c += CNT::n(s); for (int i = 0; i < I; ++i) c += EX::n(i); return c; }();
        static_assert(out < 64 && I < have && (EX::n(I) == 0 || ring_issued(I + 1, NST, NS) == NST), "");
        wait_vm<out>();
        bar();
        range.template operator()<have>(std::make_integer_sequence<int, ring_issued(I + 1, NST, NS) - have>{});
        extra.template operator()<I>();
    };
    range.template operator()<0>(std::make_integer_sequence<int, ring_issued(0, NST, NS)>{});
    [&]<int... I>(std::integer_sequence<int, I...>) { (sync.template operator()<I>(), ...); }(std::make_integer_sequence<int, NST>{});
}
constexpr int cmin(int a, int b) { return a < b ? a : b; }
constexpr int pair_issued(int m, int NST, int NS) {
    int n = 3;
    for (int k = -1; k < m; ++k) n = cmin(cmin(n + 4, k < 0 ? NS : 2 * k + 1 + NS), NST);
    return n;
}
template <int NST, int NS, class CNT, class EX, int PA, class F, class X>
DI void pair_loader(F issue, X extra) {
    auto range = [&]<int S0, int... I>(std::integer_sequence<int, I...>) { (issue.template operator()<S0 + I>(), ...); };
    auto sync = [&]<int M>() {
        constexpr int need = M < 0 ? 0 : cmin(2 * M + 2, NST - 1), have = pair_issued(M, NST, NS);
        constexpr int from = M == PA + 2 ? pair_issued(PA + 1, NST, NS) : need + 1;
        constexpr int out = [] { int c = M == PA + 1; for (int s = from; s < have; ++s) c += CNT::n(s); for (int i = 0; i < M; ++i) c += EX::n(i); return c; }();
        static_assert(out < 64 && need < have && (EX::n(M) == 0 || pair_issued(M + 1, NST, NS) == NST), "");
        wait_vm<out>();
        extra.template operator()<M, 0>();
        bar();
        range.template operator()<have>(std::make_integer_sequence<int, pair_issued(M + 1, NST, NS) - have>{});
        extra.template operator()<M, 1>();
    };
    range.template operator()<0>(std::make_integer_sequence<int, 3>{});
    [&]<int... I>(std::integer_sequence<int, I...>) { (sync.template operator()<I - 1>(), ...); }(std::make_integer_sequence<int, NST / 2 + 1>{});
}
constexpr int G64_STAGE = 24576;
template <int PRE> struct G64Cnt { static constexpr int n(int s) { return s < PRE ? 4 : 6; } };
DI void g64_w(const char* sw, unsigned ldsb, int stage, int slot, int lw, int lane) {
    const unsigned vw = lw * 2048 + lane * 16, mw = ldsb + slot * G64_STAGE + 16384 + lw * 2048;
    const char* s = sw + stage * 8192;
    if (ABL != 2) asm volatile(SETM0(mw) GLDW(vw, s, 0) GLDW(vw, s, 1024)::[mw] "s"(mw), [vw] "v"(vw), [s] "s"(s) : "memory");
}
template <int NST, int NS, int PRE, class EX, int PA, class X>
DI void g64_loader(const char* sa, const char* sw, unsigned ldsb, int lw, int lane, X extra) {
    const unsigned va = lw * 4096 + lane * 16;
    ring_loader<NST, NS, G64Cnt<PRE>, EX, PA>([&]<int S>() {
        const unsigned ma = ldsb + (S % NS) * G64_STAGE + lw * 4096;
        if (ABL != 2) asm volatile(SETM0(ma) GLDA(va, sa, 0) GLDA(va, sa, 1024) GLDA(va, sa, 2048) GLDA(va, sa, 3072)::[ma] "s"(ma), [va] "v"(va), [sa] "s"(sa) : "memory");
        if constexpr (S >= PRE) g64_w(sw, ldsb, S, S % NS, lw, lane);
        sa += 16384;
    }, extra);
}

struct G64Frag { bf16x8 a[4][2], w[4]; };
template <int NST, int NS, class H>
DI void g64_compute(lptr lds, int cw, int lane, f32x16 (&acc)[2], H hook) {
    const LP pa = mk_lp(lds + (lane >> 5) * 2048 + ((cw & 1) * 64 + (lane & 31)) * 16), pw = mk_lp(lds + (cw >> 1) * 1024 + lane * 16);
    G64Frag fr[2];
    auto rd = [&]<int S, int I>() {
        constexpr int so = (S % NS) * G64_STAGE;
        if constexpr (S < NST && I < 12) {
            if constexpr (I % 3 == 0) fr[S & 1].w[I / 3] = lds_at<so + 16384 + (I / 3) * 2048>(pw);
            else fr[S & 1].a[I / 3][I % 3 - 1] = lds_at<so + (I / 3) * 4096 + (I % 3 - 1) * 512>(pa);
        }
    };
    auto step = [&]<int S>() {
        if constexpr (S + 1 < NST) { lgkm_bar(); SBAR(); }
        if constexpr (ABL != 1)
            [&]<int... U>(std::integer_sequence<int, U...>) {
                ((acc[U % 2] = mfma32(fr[S & 1].w[U / 2], fr[S & 1].a[U / 2][U % 2], acc[U % 2]), SBAR(),
                  rd.template operator()<S + 1, 2 * U>(), rd.template operator()<S + 1, 2 * U + 1>(), SBAR()), ...);
            }(std::make_integer_sequence<int, 8>{});
        hook(S);
    };
    bar();
    [&]<int... I>(std::integer_sequence<int, I...>) { (rd.template operator()<0, I>(), ...); }(std::make_integer_sequence<int, 12>{});
    hook(-1);
    [&]<int... S>(std::integer_sequence<int, S...>) { (step.template operator()<S>(), ...); }(std::make_integer_sequence<int, NST>{});
}
DI void tile_map(int b, int& p, int& j) { const int x = b & 7, i = b >> 3; p = (x >> 1) * 4 + (i >> 3); j = (x & 1) * 8 + (i & 7); }

constexpr int K2_W = 98304, K2_MOD = 2 * G64_STAGE, K2_RED = 3 * G64_STAGE;
DI int k2_h(int kc, int tk) { return (kc >> 6) * G64_STAGE + (kc & 63) * 256 + (tk ^ (kc & 7)) * 16; }
DI void ph_ln_kv(const u16* __restrict__ x, const u16* __restrict__ wk, const u16* __restrict__ wv, char* __restrict__ ws, lptr lds, int lane, int wid, int p, int j) {
    const int t = threadIdx.x, b = blockIdx.x, tg = p * 8 + (j >> 1), which = j & 1;
    const rsrc_t rws = mk_rsrc(ws);
    const float* modp = (const float*)(ws + WS_MODP);
    if (wid == 0) STAMP(ws, 1, 0);
    TXP(ws, 1);
    u32x4 xr[2][2]; f32x4 mp[4];
    {
        const u16* xp = x + (size_t)(tg * 16 + wid * 2) * D_;
        const unsigned vx = lane * 16, vm = t * 16;
        asm volatile("global_load_dwordx4 %0, %8, %10\n\tglobal_load_dwordx4 %1, %8, %10 offset:1024\n\t"
                     "global_load_dwordx4 %2, %8, %10 offset:2048\n\tglobal_load_dwordx4 %3, %8, %10 offset:3072\n\t"
                     "global_load_dwordx4 %4, %9, %11\n\tglobal_load_dwordx4 %5, %9, %12\n\tglobal_load_dwordx4 %6, %9, %13\n\tglobal_load_dwordx4 %7, %9, %14"
                     : "=&v"(xr[0][0]), "=&v"(xr[0][1]), "=&v"(xr[1][0]), "=&v"(xr[1][1]), "=&v"(mp[0]), "=&v"(mp[1]), "=&v"(mp[2]), "=&v"(mp[3])
                     : "v"(vx), "v"(vm), "s"(xp), "s"(modp), "s"(modp + 6144), "s"(modp + 2 * 6144), "s"(modp + 3 * 6144) : "memory");
    }
    float mf[4] = {0, 0, 0, 0};
    if (t < 24) {
#pragma unroll
        for (int i = 0; i < 4; ++i) mf[i] = modp[i * 6144 + b * 24 + t];
    }
    {
        const u16* W = (which ? wv : wk) + wid * 4096;
        const int row = lane >> 2, chn = (lane & 3) ^ (((row >> 3) & 1) << 1);
        const unsigned vo = (unsigned)(row * 64 + chn * 16);
#pragma unroll
        for (int q = 0; q < 2; ++q) {
            const unsigned m = (unsigned)(uintptr_t)lds + K2_W + wid * 8192 + q * 4096;
            const u16* sb_ = W + q * 2048;
            asm volatile(SETM0(m) GLDS(v, s, 0) GLDS(v, s, 1024) GLDS(v, s, 2048) GLDS(v, s, 3072)::[m] "s"(m), [v] "v"(vo), [s] "s"(sb_) : "memory");
        }
    }
    asm volatile("s_waitcnt vmcnt(8)" : "+v"(xr[0][0]), "+v"(xr[0][1]), "+v"(xr[1][0]), "+v"(xr[1][1]), "+v"(mp[0]), "+v"(mp[1]), "+v"(mp[2]), "+v"(mp[3])::"memory");
    if (wid >= 4) {
#pragma unroll
        for (int i = 0; i < 4; ++i) g64_w(ws + WS_WQ + (size_t)j * 131072, (unsigned)(uintptr_t)lds, i, i, wid - 4, lane);
    }
    {
        f32x4 m = (mp[0] + mp[1]) + (mp[2] + mp[3]);
#pragma unroll
        for (int e = 0; e < 4; ++e) { m[e] = rbf(m[e]); if (t >= 256) m[e] = rbf(1.f + m[e]); }
        *(LDSP f32x4*)(lds + K2_MOD + t * 16) = m;
    }
    float v[2][16], rstd[2];
#pragma unroll
    for (int r = 0; r < 2; ++r) {
#pragma unroll
        for (int h = 0; h < 2; ++h)
#pragma unroll
            for (int j = 0; j < 4; ++j) { v[r][h * 8 + 2 * j] = bflo(xr[r][h][j]); v[r][h * 8 + 2 * j + 1] = bfhi(xr[r][h][j]); }
        float s = 0;
#pragma unroll
        for (int j = 0; j < 16; ++j) s += v[r][j];
        const float mean = wave_sum(s) * (1.f / 1024.f);
        float q = 0;
#pragma unroll
        for (int j = 0; j < 16; ++j) { v[r][j] -= mean; q += v[r][j] * v[r][j]; }
        rstd[r] = __builtin_amdgcn_rsqf(wave_sum(q) * (1.f / 1024.f) + 1e-5f);
    }
    if (wid == 0) STAMP(ws, 1, 1);
    lgkm_bar();
#pragma unroll
    for (int h = 0; h < 2; ++h) {
        f32x4 sa[2], a1[2];
#pragma unroll
        for (int q = 0; q < 2; ++q) {
            sa[q] = *(const LDSP f32x4*)(lds + K2_MOD + (h * 512 + lane * 8 + q * 4) * 4);
            a1[q] = *(const LDSP f32x4*)(lds + K2_MOD + 4096 + (h * 512 + lane * 8 + q * 4) * 4);
        }
#pragma unroll
        for (int r = 0; r < 2; ++r) {
            u32x4 o;
#pragma unroll
            for (int j = 0; j < 4; ++j) {
                const int e = 2 * j;
                o[j] = pk2(v[r][h * 8 + e] * rstd[r] * a1[e >> 2][e & 3] + sa[e >> 2][e & 3], v[r][h * 8 + e + 1] * rstd[r] * a1[e >> 2][(e & 3) + 1] + sa[e >> 2][(e & 3) + 1]);
            }
            *(LDSP u32x4*)(lds + k2_h(h * 64 + lane, wid * 2 + r)) = o;
        }
    }
    lgkm_bar();
    {
        const unsigned hAT = WS_H + (tg >> 3) * 262144 + (tg & 7) * 256;
#pragma unroll
        for (int i = 0; i < 2; ++i) {
            const int kc = which * 64 + i * 32 + (t >> 4), tk = t & 15;
            st16(rws, kc * 2048 + tk * 16, hAT, *(const LDSP u32x4*)(lds + k2_h(kc, tk)));
        }
    }
    wait_vm<0>();
    bar();
    if (wid == 7) panel_post(ws, FL_H, p, j, 1u, lane);
    if (wid == 0) STAMP(ws, 1, 2);
    f32x4 acc[2] = {{0, 0, 0, 0}, {0, 0, 0, 0}};
    {
        const int g = lane >> 4, tk = lane & 15, q = (lane & 15) >> 2, p = lane & 3;
#pragma unroll
        for (int s = 0; s < 4; ++s) {
            const int kc = wid * 16 + s * 4 + g;
            const bf16x8 hf = lds_b128(lds + k2_h(kc, tk));
            const int row = kc * 8 + q;
#pragma unroll
            for (int nb = 0; nb < 2; ++nb) {
                const lptr wp = lds + K2_W + row * 64 + ((nb ^ (kc & 1)) * 32) + p * 8;
                const bf16x8 wf = cat(lds_tr(wp), lds_tr(wp + 256));
                acc[nb] = which ? mfma16(hf, wf, acc[nb]) : mfma16(wf, hf, acc[nb]);
            }
        }
    }
#pragma unroll
    for (int nb = 0; nb < 2; ++nb) *(LDSP f32x4*)(lds + K2_RED + ((wid * 2 + nb) * 64 + lane) * 16) = acc[nb];
    lgkm_bar();
    if (t < 128) {
        const int nb = t >> 6;
        f32x4 r = *(const LDSP f32x4*)(lds + K2_RED + (nb * 64 + lane) * 16);
#pragma unroll
        for (int w = 1; w < 8; ++w) r += *(const LDSP f32x4*)(lds + K2_RED + ((w * 2 + nb) * 64 + lane) * 16);
        u32x2 o = {pk2(r[0], r[1]), pk2(r[2], r[3])};
        const int g = lane >> 4, c = lane & 15, kt = tg >> 1;
        if (which) {
            __builtin_amdgcn_raw_buffer_store_b64(o, rws, WS_VT + kt * 2048 + ((tg & 1) * 2 + (g & 1)) * 512 + (nb * 16 + c) * 16 + (g >> 1) * 8, 0, 16);
        } else {
            __builtin_amdgcn_raw_buffer_store_b64(o, rws, WS_KT + kt * 2048 + (nb * 2 + (g & 1)) * 512 + ((tg & 1) * 16 + c) * 16 + (g >> 1) * 8, 0, 16);
        }
    }
    if (t < 24) {
        const int col = b * 24 + t, part = col >> 10, k = col & 1023;
        float v = rbf((mf[0] + mf[1]) + (mf[2] + mf[3]));
        if (part == 1 || part == 4) v = rbf(1.f + v);
        __builtin_amdgcn_raw_buffer_store_b32(__float_as_uint(v), rws, WS_MOD + col * 4, 0, 16);
        if (part == 3 || part == 4)
            __builtin_amdgcn_raw_buffer_store_b16((u16)(__float_as_uint(v) >> 16), rws,
                WS_BUT + ((((k >> 5) * 2 + (part == 3)) * 4 + 2 * ((k >> 4) & 1) + ((k >> 2) & 1)) * 8 + (k & 3) + 4 * ((k >> 3) & 1)) * 2, 0, 16);
    }
    if (wid == 0) STAMP(ws, 1, 3);
}

constexpr int QA_NS = 4;
constexpr int QA_KV0 = QA_NS * G64_STAGE;
constexpr int QA_LDS = QA_KV0 + 65536;
constexpr float QSCALE = 0.17677669529663687f * 1.4426950408889634f;

constexpr int OP_NS = 6;
#define OPRE 4
#define PREW 0
#define KVL 8
#define KVC 11
#define KVN 4
#define KVP 1
#define PROG 1
DI void ph_qattn(const u16* __restrict__ wo, const u16* __restrict__ w1, const u16* __restrict__ w2, char* __restrict__ ws, lptr lds, int lane, int wid, int p, int j, TS& TL) {
    const unsigned ldsb = (unsigned)(uintptr_t)lds;
    const char* KT = ws + WS_KT; const char* VT = ws + WS_VT;
    const unsigned vkv = lane * 16;
    auto kv_piece = [&](int chunk, unsigned buf, int w, int i) {
        const char* s = ((i & 4) ? VT : KT) + chunk * 32768 + w * 4096 + (i & 3) * 1024;
        const unsigned m = ldsb + buf + (i >> 2) * 32768 + w * 4096 + (i & 3) * 1024;
        asm volatile(SETM0(m) GLD1(v, s, 0)::[m] "s"(m), [v] "v"(vkv), [s] "s"(s) : "memory");
    };
    const int b = blockIdx.x;
    const bool big = b < 224;
    const rsrc_t rws = mk_rsrc(ws);
    const lptr stg = lds + 65536 + wid * 4096;
    const int jA = b & 15, gA = big ? (b >> 4) : ((b - 224) >> 4);
    const int gB = big ? (b / 56) : 2 + ((b - 224) >> 4), cB = big ? (b % 56) : jA;
    u32x4 butl, kfl;
    const unsigned vbut = (lane & 1) * 64 + (lane >> 4) * 16;
    const char* sbut = ws + WS_BUT + (gB * 8 + wid) * 128;
    if (wid == 0) STAMP(ws, 2, 0);
    TXP(ws, 2);
    if (wid >= 4) {
        panel_wait(ws, FL_H, p, j, 1u, lane);
        g64_loader<16, QA_NS, QA_NS, NoEx, -1>(ws + WS_H + (size_t)p * 262144, ws + WS_WQ + (size_t)j * 131072, ldsb, wid - 4, lane, []<int I>() {});
        async_ld(butl, vbut, sbut);
        asm volatile("s_waitcnt vmcnt(0)" : "+v"(butl)::"memory");
        bar();
    } else {
        f32x16 r[2] = {};
        g64_compute<16, QA_NS>(lds, wid, lane, r, [&](int i) {
            if (i == KVP) wait_vm<0>();
            if (i == KVP + 1 && wid == 0) grid_post(ws, FL_KV, 1u, lane);
            if (i == KVL) async_ld(kfl, lane * 16, ws + WS_FLAG + FL_KV + (b & 7) * 1024);
            if (i == KVC) {
                asm volatile("s_waitcnt vmcnt(0)" : "+v"(kfl)::"memory");
                if (!grid_ok(kfl, 1u)) grid_wait(ws, FL_KV, 1u, lane);
                async_ld(butl, vbut, sbut);
            }
            if (i >= KVC && i < KVC + 16 / KVN) {
#pragma unroll
                for (int q = 0; q < KVN; ++q) kv_piece(0, QA_KV0, wid + 4 * (q & 1), ((i - KVC) * KVN + q) >> 1);
            }
        });
        if (wid == 0) STAMP(ws, 2, 1);
#pragma unroll
        for (int tb = 0; tb < 2; ++tb)
#pragma unroll
            for (int s = 0; s < 2; ++s) {
                u32x4 o;
#pragma unroll
                for (int i = 0; i < 4; ++i) o[i] = pk2(r[tb][8 * s + 2 * i] * QSCALE, r[tb][8 * s + 2 * i + 1] * QSCALE);
                *(LDSP u32x4*)(lds + 32768 + ((wid * 2 + tb) * 2 + s) * 1024 + lane * 16) = o;
            }
        asm volatile("s_waitcnt vmcnt(0)" : "+v"(butl)::"memory");
        lgkm_bar();
    }
    const int qg = wid & 3, kh = wid >> 2;
    const int lo = kh * 16384 + lane * 16;
    bf16x8 qA0, qA1, qB0, qB1, k0, k1, v0, v1;
    {
        const lptr qp = lds + 32768 + qg * 4096 + lane * 16;
        qA0 = lds_b128(qp); qA1 = lds_b128(qp + 1024); qB0 = lds_b128(qp + 2048); qB1 = lds_b128(qp + 3072);
        k0 = lds_b128(lds + QA_KV0 + lo); k1 = lds_b128(lds + QA_KV0 + lo + 1024);
    }
    bf16x8 sel;
    {
        const unsigned on = ((lane & 15) == ((lane >> 4) & 1)) ? 0x3f803f80u : 0u;
        sel = __builtin_bit_cast(bf16x8, (u32x4){on, on, on, on});
    }
    f32x16 oA = {}, oB = {}, sA, sB;
    f32x4 lA = {}, lB = {};
    const f32x16 zero = {};
    u32x4 pA0, pA1, pB0 = {0, 0, 0, 0}, pB1 = {0, 0, 0, 0};
    bf16x8 but = __builtin_bit_cast(bf16x8, (lane & 15) < 2 ? butl : (u32x4){0, 0, 0, 0});
    PIN(but);
    lgkm_bar();
    if (wid == 0) STAMP(ws, 2, 2);
    TXP(ws, 3);
#define EXP2(E, d, S, i) do { E[d] = __builtin_amdgcn_exp2f(S[i]); E[d + 1] = __builtin_amdgcn_exp2f(S[i + 1]); PIN(E); } while (0)
#define EXP4(E, S, i) do { E[0] = __builtin_amdgcn_exp2f(S[i]); E[1] = __builtin_amdgcn_exp2f(S[i + 1]); E[2] = __builtin_amdgcn_exp2f(S[i + 2]); E[3] = __builtin_amdgcn_exp2f(S[i + 3]); } while (0)
#define PK2(P, d, E) do { P[d] = pk2(E[0], E[1]); P[d + 1] = pk2(E[2], E[3]); PIN(P); } while (0)
#define BF(x) __builtin_bit_cast(bf16x8, x)
#define HALF(SX, PX0, PX1, SY, QY0, QY1, OZ, LZ, PZ0, PZ1, R0, R1, R2, R3) do { \
        SY = mfma32(k0, QY0, zero); SBAR(); PK2(PZ1, 2, e); R0; SBAR(); EXP4(e, SX, 0); PIN(e); SBAR(); \
        SY = mfma32(k1, QY1, SY); SBAR(); PK2(PX0, 0, e); R1; SBAR(); EXP2(e, 0, SX, 4); SBAR(); LZ = mfma16(sel, BF(PZ0), LZ); SBAR(); EXP2(e, 2, SX, 6); SBAR(); \
        OZ = mfma32(v0, BF(PZ0), OZ); SBAR(); PK2(PX0, 2, e); R2; SBAR(); EXP4(e, SX, 8); PIN(e); SBAR(); \
        OZ = mfma32(v1, BF(PZ1), OZ); SBAR(); PK2(PX1, 0, e); R3; SBAR(); EXP2(e, 0, SX, 12); SBAR(); LZ = mfma16(sel, BF(PZ1), LZ); SBAR(); EXP2(e, 2, SX, 14); SBAR(); } while (0)
    sA = mfma32(k0, qA0, zero); sA = mfma32(k1, qA1, sA);
    v0 = k0; v1 = k1;
    f32x4 e = {0, 0, 0, 0};
    bf16x8 w0, w1_, sf[4];
    f32x4 ut[4];
    u32x4 sfl;
    unsigned junk = 0;
    const lptr tp32 = stg + (4 * (lane >> 5) + ((lane & 15) >> 2)) * 128 + ((lane >> 4) & 1) * 32 + (lane & 3) * 8;
    const lptr tp16 = stg + (16 * (lane >> 5) + 4 * ((lane >> 4) & 1) + ((lane & 15) >> 2)) * 128 + (lane & 3) * 8;
    unsigned dstA = big ? WS_W2 + (jA >> 1) * 917504 + (gA * 8 + wid) * 8192 + (jA & 1) * 4096 : WS_WO + jA * 131072 + (gA * 8 + wid) * 4096, dstB[4];
#pragma unroll
    for (int i = 0; i < 4; ++i) {
        const int nbk = cB * 4 + i, jt = nbk / 14;
        dstB[i] = big ? WS_W1 + jt * 458752 + (gB * 8 + wid) * 14336 + (nbk - jt * 14) * 1024 : WS_WO + cB * 131072 + (gB * 8 + wid) * 4096 + i * 1024;
        asm volatile("" : "+s"(dstB[i]));
    }
    const int ldB = big ? F_ : D_;
    const u16* srcA = (big ? w2 : wo) + jA * 64 + (size_t)(gA * 256 + wid * 32) * D_;
    const u16* srcB = (big ? w1 : wo) + cB * 64 + (size_t)(gB * 256 + wid * 32) * ldB;
    asm volatile("" : "+s"(dstA), "+s"(srcA), "+s"(srcB));
    auto side = [&]<int C, int IT, int I>() {
        if constexpr (C < 3 && IT < 2 && !(AA & 2)) kv_piece(C + 1, (C & 1) ? QA_KV0 : 0, wid, IT * 4 + I);
        if constexpr (AA & 1) return;
        if constexpr ((C == 0 && IT == 2) || (C == 1 && IT == 4)) {
            const int ld = C == 0 ? D_ : ldB;
            const u16* s = (C == 0 ? srcA : srcB) + I * 8 * ld;
            const unsigned vo = (unsigned)((lane >> 3) * ld * 2 + (lane & 7) * 16), m = ldsb + 65536 + wid * 4096 + I * 1024;
            asm volatile(SETM0(m) GLDS(v, s, 0)::[m] "s"(m), [v] "v"(vo), [s] "s"(s) : "memory");
        }
        if constexpr ((C == 1 || C == 2) && IT == 2) {
            if constexpr (I == 0) wait_vm<8>();
            const lptr q = !big ? tp32 + (I >> 1) * 2048 + (I & 1) * 64 : (C == 2 ? tp16 + I * 32 : tp16 + (lane & 3) * 8 + (I >> 1) * 64 + (I & 1) * 8);
            sf[I] = cat(lds_tr(q), lds_tr(q + 1024));
        }
        if constexpr ((C == 1 || C == 2) && IT == 3) {
            const unsigned dst = C == 1 ? dstA + I * 1024 : dstB[I];
            if constexpr (C == 2) ut[I] = mfma16(sf[I], but, (f32x4){0, 0, 0, 0});
            st16(rws, lane * 16, dst, __builtin_bit_cast(u32x4, sf[I]));
        }
        if constexpr (C == 2 && IT == 4) *(LDSP f32x4*)(stg + I * 1024 + lane * 16) = ut[I];
        if constexpr (PREW && C == 3 && IT == 2 && I < 2)
            touch(junk, (I * 64 + lane < 112 ? I * 64 + lane : 0) * 64, ws + WS_G + p * (F_ / 8 * 2048) + j * 57344 + wid * 7168);
        if constexpr (C == 0 && IT == 3 && I < 2) st16(rws, lane * 16, WS_EX + blockIdx.x * 16384 + wid * 2048 + I * 1024, (u32x4){~0u, ~0u, ~0u, ~0u});
        if constexpr (C == 3 && IT == 0 && I == 0) { if (wid == 0) grid_post(ws, FL_SIDE, 1u, lane); }
        if constexpr (C == 3 && IT == 0) {
            if (wid == 0) {
                const lptr q = lds + 65536 + I * 8192 + (lane >> 4) * 1024 + ((lane >> 2) & 3) * 256 + (lane & 3) * 4;
                ut[I] = (f32x4){*(const LDSP float*)q, *(const LDSP float*)(q + 16), *(const LDSP float*)(q + 4096), *(const LDSP float*)(q + 4096 + 16)};
            }
        }
        if constexpr (C == 3 && IT == 1 && I == 1) {
            if (wid == 0 && big) {
                const f32x4 a = (ut[0] + ut[1]) + (ut[2] + ut[3]);
#pragma unroll
                for (int i = 0; i < 2; ++i) __builtin_amdgcn_raw_buffer_store_b32(__float_as_uint(a[i] + a[2 + i]), rws, WS_UTP + ((gB * 2 + i) * F_ + cB * 64 + lane) * 4, 0, 16);
            }
        }
        if constexpr (C == 3 && IT == 6 && I == 0) {
            if (wid == 0) { wait_vm<0>(); grid_post(ws, FL_SIDE, 2u, lane); }
            async_ld(sfl, lane * 16, ws + WS_FLAG + FL_SIDE + (blockIdx.x & 7) * 1024);
        }
    };
#define SIDE(I) side.template operator()<C, IT, I>()
    auto step = [&]<int C, int IT>() {
        const lptr kp = lds + ((C & 1) ? 0 : QA_KV0) + lo, kn = lds + ((C & 1) ? QA_KV0 : 0) + lo;
        if constexpr (PRIO && IT == 5) __builtin_amdgcn_s_setprio(2);
        if constexpr (PRIO && IT == 6) __builtin_amdgcn_s_setprio(1);
        if constexpr (IT < 7) {
            HALF(sA, pA0, pA1, sB, qB0, qB1, oB, lB, pB0, pB1, SIDE(0), k0 = lds_b128(kp + (IT + 1) * 2048),
                 k1 = lds_b128(kp + (IT + 1) * 2048 + 1024), v0 = lds_b128(kp + 32768 + IT * 2048));
            if constexpr (PRIO && IT == 6) __builtin_amdgcn_s_setprio(0);
            HALF(sB, pB0, pB1, sA, qA0, qA1, oA, lA, pA0, pA1, v1 = lds_b128(kp + 32768 + IT * 2048 + 1024),
                 SIDE(1); if constexpr (IT == 6) w0 = lds_b128(kp + 32768 + 7 * 2048), SIDE(2); if constexpr (IT == 6) w1_ = lds_b128(kp + 32768 + 7 * 2048 + 1024), SIDE(3));
        } else {
            if constexpr (C < 3 && !(AA & 4)) {
                wait_vm<(AA & 1) || C == 2 ? 0 : (C == 1 ? 8 : 6)>();
                lgkm_bar();
                if constexpr (PRIO) __builtin_amdgcn_s_setprio(3);
            }
            HALF(sA, pA0, pA1, sB, qB0, qB1, oB, lB, pB0, pB1, (void)0, if constexpr (C < 3) k0 = lds_b128(kn), if constexpr (C < 3) k1 = lds_b128(kn + 1024), (void)0);
            v0 = w0; v1 = w1_;
            HALF(sB, pB0, pB1, sA, qA0, qA1, oA, lA, pA0, pA1, (void)0, (void)0, (void)0, (void)0);
        }
    };
    if constexpr (PRIO) __builtin_amdgcn_s_setprio(3);
    [&]<int... N>(std::integer_sequence<int, N...>) { (step.template operator()<N / 8, N % 8>(), ...); }(std::make_integer_sequence<int, 32>{});
    PK2(pB1, 2, e);
    oB = mfma32(v0, BF(pB0), oB); oB = mfma32(v1, BF(pB1), oB);
    lB = mfma16(sel, BF(pB0), lB); lB = mfma16(sel, BF(pB1), lB);
    PIN(lA); PIN(lB); PIN(oA); PIN(oB);
    if (wid == 0) STAMP(ws, 2, 3);
    const lptr ex = lds + QA_KV0;
    {
        const f32x16& so = kh ? oA : oB;
        const f32x4& sl = kh ? lA : lB;
#pragma unroll
        for (int i = 0; i < 4; ++i) *(LDSP f32x4*)(ex + wid * 4096 + i * 1024 + lane * 16) = (f32x4){so[4 * i], so[4 * i + 1], so[4 * i + 2], so[4 * i + 3]};
        const f32x4& ml = kh ? lB : lA;
        if (lane < 16) {
            *(LDSP float*)(ex + 32768 + wid * 256 + lane * 4) = sl[0]; *(LDSP float*)(ex + 32768 + wid * 256 + 64 + lane * 4) = sl[1];
            *(LDSP float*)(ex + 32768 + wid * 256 + 128 + lane * 4) = ml[0]; *(LDSP float*)(ex + 32768 + wid * 256 + 192 + lane * 4) = ml[1];
        }
    }
    lgkm_bar();
    tcap<0>(TL);
    asm volatile("s_waitcnt vmcnt(0)" : "+v"(sfl), "+v"(junk)::"memory");
    if (wid >= 4) {
        if (!grid_ok(sfl, lane >= 56)) grid_wait(ws, FL_SIDE, lane >= 56, lane);
#pragma unroll
        for (int i = 0; i < OPRE; ++i) g64_w(ws + WS_WO + (size_t)j * 131072, ldsb, i, i, wid - 4, lane);
    }
    f32x16 o = kh ? oB : oA;
    float l;
    {
        l = *(const LDSP float*)(ex + 32768 + wid * 256 + 128 + (lane & 31) * 4) + *(const LDSP float*)(ex + 32768 + (wid ^ 4) * 256 + (lane & 31) * 4);
#pragma unroll
        for (int i = 0; i < 4; ++i) {
            const f32x4 v = *(const LDSP f32x4*)(ex + (wid ^ 4) * 4096 + i * 1024 + lane * 16);
#pragma unroll
            for (int e = 0; e < 4; ++e) o[4 * i + e] += v[e];
        }
    }
    float chk = l * 0.f;
#pragma unroll
    for (int i = 0; i < 16; ++i) chk = __builtin_fmaf(o[i], 0.f, chk);
    const bool bad = AA ? false : (!(chk == 0.f) || !(l >= 0x1p-90f));
    if (__builtin_expect(__any(bad), 0)) {
        const bf16x8 q0 = kh ? qB0 : qA0, q1 = kh ? qB1 : qA1;
        float m = -INFINITY; l = 0.f; o = zero;
        for (int kt = 0; kt < 64; ++kt) {
            const bf16x8 a0 = BF(ld16(rws, WS_KT + kt * 2048 + lane * 16)), a1 = BF(ld16(rws, WS_KT + kt * 2048 + 1024 + lane * 16));
            const bf16x8 b0 = BF(ld16(rws, WS_VT + kt * 2048 + lane * 16)), b1 = BF(ld16(rws, WS_VT + kt * 2048 + 1024 + lane * 16));
            f32x16 s = mfma32(a0, q0, zero); s = mfma32(a1, q1, s);
            float mx = s[0];
#pragma unroll
            for (int i = 1; i < 16; ++i) mx = fmaxf(mx, s[i]);
            mx = fmaxf(mx, __shfl_xor(mx, 32));
            const float mn = fmaxf(m, mx), al = __builtin_amdgcn_exp2f(m - mn);
            m = mn;
            u32x4 p0, p1; float rs = 0.f;
#pragma unroll
            for (int i = 0; i < 4; ++i) {
                p0[i] = pk2(__builtin_amdgcn_exp2f(s[2 * i] - m), __builtin_amdgcn_exp2f(s[2 * i + 1] - m));
                p1[i] = pk2(__builtin_amdgcn_exp2f(s[8 + 2 * i] - m), __builtin_amdgcn_exp2f(s[9 + 2 * i] - m));
                rs += bflo(p0[i]) + bfhi(p0[i]) + bflo(p1[i]) + bfhi(p1[i]);
            }
            rs += __shfl_xor(rs, 32);
            l = l * al + rs;
#pragma unroll
            for (int i = 0; i < 16; ++i) o[i] *= al;
            o = mfma32(b0, BF(p0), o); o = mfma32(b1, BF(p1), o);
        }
    }
    {
        const float inv = __builtin_amdgcn_rcpf(l);
        const unsigned oAT = WS_O + p * 262144 + (2 * j + (qg >> 1)) * 8192 + ((qg & 1) * 2 + kh) * 512, vo = (lane >> 5) * 2048 + (lane & 31) * 16;
#pragma unroll
        for (int s = 0; s < 2; ++s) {
            u32x4 w;
#pragma unroll
            for (int i = 0; i < 4; ++i) w[i] = pk2(o[8 * s + 2 * i] * inv, o[8 * s + 2 * i + 1] * inv);
            st16(rws, vo, oAT + s * 4096, w);
        }
    }
    wait_vm<0>();
    lgkm_bar();
    if (wid == 0) panel_post(ws, FL_O, p, j, 1u, lane);
    if (wid == 0) STAMP(ws, 2, 4);
}

constexpr int F1_NS = 7, F1_STAGE = 22528, F1_NST = 32, F1_AUX = F1_NS * F1_STAGE;
template <int NW> DI void f1_w(const char* sw, unsigned ldsb, int stage, int pc0, int lane) {
    const unsigned vw = pc0 * 1024 + lane * 16, mw = ldsb + (stage % F1_NS) * F1_STAGE + 8192 + pc0 * 1024;
    const char* s = sw + stage * 14336;
    if constexpr (ABL == 2) {} else if constexpr (NW == 4)
        asm volatile(SETM0(mw) GLDW(vw, s, 0) GLDW(vw, s, 1024) GLDW(vw, s, 2048) GLDW(vw, s, 3072)::[mw] "s"(mw), [vw] "v"(vw), [s] "s"(s) : "memory");
    else
        asm volatile(SETM0(mw) GLDW(vw, s, 0) GLDW(vw, s, 1024) GLDW(vw, s, 2048)::[mw] "s"(mw), [vw] "v"(vw), [s] "s"(s) : "memory");
}
template <int NW> struct OpEx { static constexpr int n(int i) { return i < 11 ? 0 : i == 12 ? 2 * NW : NW; } };
template <int NW>
DI void op_loader(char* ws, lptr lds, int lane, int lw, int p, int j, TS& TL) {
    const unsigned ldsb = (unsigned)(uintptr_t)lds;
    const char* sw = ws + WS_WO + (size_t)j * 131072;
    const char* s1 = ws + WS_W1 + (size_t)j * 458752;
    const int pc0 = lw < 2 ? lw * 4 : 8 + (lw - 2) * 3;
    tcap<1>(TL);
#pragma unroll
    for (int i = OPRE; i < OP_NS; ++i) g64_w(sw, ldsb, i, i, lw, lane);
    panel_wait(ws, FL_O, p, j, 1u, lane);
    tcap<2>(TL);
    u32x4 fl;
    g64_loader<16, OP_NS, OP_NS, OpEx<NW>, 8>(ws + WS_O + (size_t)p * 262144, sw, ldsb, lw, lane, [&]<int I>() {
        if constexpr (I == 8) async_ld(fl, lane * 16, ws + WS_FLAG + FL_SIDE + (blockIdx.x & 7) * 1024);
        if constexpr (I == 10) { pin(fl); if (!grid_ok(fl, 2u)) grid_wait(ws, FL_SIDE, 2u, lane); }
        if constexpr (I == 0) { tcap<3>(TL); }
        if constexpr (I == 15) { tcap<4>(TL); }
        if constexpr (I >= 11) f1_w<NW>(s1, ldsb, I < 13 ? I - 7 : I - 13, pc0, lane);
        if constexpr (I == 12) f1_w<NW>(s1, ldsb, 6, pc0, lane);
    });
}
DI void op_compute(const u16* __restrict__ x, char* __restrict__ ws, lptr lds, int lane, int wid, int p, int j) {
    if (wid == 0) STAMP(ws, 3, 0);
    TXP(ws, 4);
    const int th = wid & 1, nb = wid >> 1, hi = lane >> 5;
    const int n0 = j * 64 + nb * 32 + 4 * hi;
    u32x4 xq[2][2]; f32x4 ga[4], a2[4];
    f32x16 r[2] = {};
    if (wid == 0) STAMP(ws, 3, 1);
    g64_compute<16, OP_NS>(lds, wid, lane, r, [&](int i) {
        if (i == -1) {
#pragma unroll
            for (int tb = 0; tb < 2; ++tb)
#pragma unroll
                for (int h = 0; h < 2; ++h) xq[tb][h] = *(const u32x4*)(x + (size_t)(p * 128 + th * 64 + tb * 32 + (lane & 31)) * D_ + j * 64 + nb * 32 + h * 16 + hi * 8);
#pragma unroll
            for (int q = 0; q < 4; ++q) {
                ga[q] = ld16f(mk_rsrc(ws), WS_MOD + (2048 + n0 + 8 * q) * 4);
                a2[q] = ld16f(mk_rsrc(ws), WS_MOD + (4096 + n0 + 8 * q) * 4);
            }
            SBAR();
        }
    });
    if (wid == 0) STAMP(ws, 3, 2);
    u32x4 xk[2][2];
#pragma unroll
    for (int tb = 0; tb < 2; ++tb) {
        u32x4 (&xo)[2] = xk[tb]; u32x4 yo[2]; float v[16]; float s = 0.f;
        u32x2 xr[2][4];
#pragma unroll
        for (int h = 0; h < 2; ++h) {
            const auto s0 = __builtin_amdgcn_permlane32_swap(xq[tb][h][0], xq[tb][h][2], false, false);
            const auto s1 = __builtin_amdgcn_permlane32_swap(xq[tb][h][1], xq[tb][h][3], false, false);
            xr[tb][2 * h] = (u32x2){s0[0], s1[0]}; xr[tb][2 * h + 1] = (u32x2){s0[1], s1[1]};
        }
#pragma unroll
        for (int i = 0; i < 4; ++i) {
            const float x0 = bflo(xr[tb][i][0]), x1 = bfhi(xr[tb][i][0]), x2 = bflo(xr[tb][i][1]), x3 = bfhi(xr[tb][i][1]);
            const unsigned a = pk2(x0 + ga[i][0] * r[tb][4 * i], x1 + ga[i][1] * r[tb][4 * i + 1]);
            const unsigned b = pk2(x2 + ga[i][2] * r[tb][4 * i + 2], x3 + ga[i][3] * r[tb][4 * i + 3]);
            xo[i >> 1][(i & 1) * 2] = a; xo[i >> 1][(i & 1) * 2 + 1] = b;
            v[4 * i] = bflo(a); v[4 * i + 1] = bfhi(a); v[4 * i + 2] = bflo(b); v[4 * i + 3] = bfhi(b);
            yo[i >> 1][(i & 1) * 2] = pk2(v[4 * i] * a2[i][0], v[4 * i + 1] * a2[i][1]);
            yo[i >> 1][(i & 1) * 2 + 1] = pk2(v[4 * i + 2] * a2[i][2], v[4 * i + 3] * a2[i][3]);
            s += (v[4 * i] + v[4 * i + 1]) + (v[4 * i + 2] + v[4 * i + 3]);
        }
        const unsigned off = p * 262144 + (j * 2 + nb) * 8192 + (th * 64 + tb * 32) * 16, vo = hi * 2048 + (lane & 31) * 16;
#pragma unroll
        for (int h = 0; h < 2; ++h) {
            st16(mk_rsrc(ws), vo, WS_Y + off + h * 4096, yo[h]);
        }
        const float mean = swap32_add(s) * (1.f / 32.f);
        float q = 0.f;
#pragma unroll
        for (int i = 0; i < 16; ++i) { const float d = v[i] - mean; q += d * d; }
        q = swap32_add(q);
        if (hi == 0) __builtin_amdgcn_raw_buffer_store_b64((u32x2){__float_as_uint(mean), __float_as_uint(q)}, mk_rsrc(ws),
                                                           WS_ST + ((j * 2 + nb) * S_ + p * 128 + th * 64 + tb * 32 + lane) * 8, 0, 16);
    }
    wait_vm<0>();
    bar();
    if (wid == 0) panel_post(ws, FL_Y, p, j, 1u, lane);
#pragma unroll
    for (int tb = 0; tb < 2; ++tb)
#pragma unroll
        for (int h = 0; h < 2; ++h) {
            const auto s0 = __builtin_amdgcn_permlane32_swap(xk[tb][h][0], xk[tb][h][2], false, false);
            const auto s1 = __builtin_amdgcn_permlane32_swap(xk[tb][h][1], xk[tb][h][3], false, false);
            st16(mk_rsrc(ws), hi * 2048 + (lane & 31) * 16, WS_X2 + p * 262144 + (j * 2 + nb) * 8192 + (th * 64 + tb * 32) * 16 + h * 4096, (u32x4){s0[0], s1[0], s0[1], s1[1]});
        }
    if (wid == 0) STAMP(ws, 3, 3);
}

#define F2_D 6
#define F2_XP 1
#define F2_MP 4
constexpr int F2_NS = 10, F2_STAGE = 16384, F2_MAIN = 56 - F2_D, F2_NST = 56 + F2_D;
#define F2_EB 4
#define F2_PS 6
#define F2_PA 8
constexpr int F2_LATE = 7 + 7 * F2_EB;
static_assert(F2_LATE <= F2_MAIN, "");
DI unsigned f2_off(int s) {
    const int o = s >= 56, i = o ? s - F2_D : s;
    const int r = i < 7 ? 0 : i < F2_LATE ? 1 + (i - 7) / F2_EB : 1 + (i - F2_LATE) / (7 - F2_EB);
    const int st = i < 7 ? i : i < F2_LATE ? (i - 7) % F2_EB : F2_EB + (i - F2_LATE) % (7 - F2_EB);
    const unsigned b = blockIdx.x, jl = (b >> 3) & 7, q = r ? r - 1 + (r - 1 >= jl) : jl;
    return ((((b & 1) ^ o) * 8 + q) * 7 + st) * 8192;
}
DI void f2_w(char* ws, unsigned ldsb, int i, int lw, int lane) {
    const int b = blockIdx.x;
    const unsigned mw = ldsb + i * F2_STAGE + 8192 + lw * 2048, va = lw * 2048 + lane * 16;
    const char* s = ws + WS_W2 + (size_t)((b >> 3) & 7) * 917504 + f2_off(i);
    if (ABL != 2) asm volatile(SETM0(mw) GLDW(va, s, 0) GLDW(va, s, 1024)::[mw] "s"(mw), [va] "v"(va), [s] "s"(s) : "memory");
}
struct F1Ex { static constexpr int n(int m) { return !PROG || m < 13 ? 0 : m == 14 ? 4 : 6; } };
template <int NW> struct F1Cnt { static constexpr int n(int s) { return (s < F1_NS ? 2 : 2 + NW) + (s == 2 ? 16 : 0); } };
template <int NW>
DI void f1_loader(char* ws, int p, int j, lptr lds, int lw, int lane, TS& TL) {
    const char* sa = ws + WS_Y + (size_t)p * 262144;
    const char* sw = ws + WS_W1 + (size_t)j * 458752;
    const unsigned ldsb = (unsigned)(uintptr_t)lds;
    const int pc0 = lw < 2 ? lw * 4 : 8 + (lw - 2) * 3;
    const unsigned va = lw * 2048 + lane * 16;
#pragma unroll
    for (int i = 0; i < F1_NS; ++i) if (i != 3 && !PROG) f1_w<NW>(sw, ldsb, i, pc0, lane);
    if (NW == 3) {
        if (lane < 56) {
            const unsigned o = WS_UTP + ((lw - 2) * F_ + j * 224 + lane * 4) * 4;
            const rsrc_t r = mk_rsrc(ws);
            const f32x4 u = (ld16f(r, o) + ld16f(r, o + 2 * F_ * 4)) + (ld16f(r, o + 4 * F_ * 4) + ld16f(r, o + 6 * F_ * 4));
            *(LDSP f32x4*)(lds + F1_AUX + (lw - 2) * 1024 + lane * 16) = u * (lw == 2 ? 1.f : -1.4426950408889634f);
        }
    }
    bar();
    tcap<5>(TL);
    f1_w<NW>(sw, ldsb, 3, pc0, lane);
    panel_wait(ws, FL_Y, p, j, 1u, lane);
    tcap<6>(TL);
    f32x2 sp[16];
    unsigned fl;
    pair_loader<F1_NST, F1_NS, F1Cnt<NW>, F1Ex, 8>([&]<int S>() {
        const unsigned ma = ldsb + (S % F1_NS) * F1_STAGE + lw * 2048;
        if constexpr (ABL != 2) asm volatile(SETM0(ma) GLDA(va, sa, 0) GLDA(va, sa, 1024)::[ma] "s"(ma), [va] "v"(va), [sa] "s"(sa) : "memory");
        if constexpr (S >= F1_NS) f1_w<NW>(sw, ldsb, S, pc0, lane);
        sa += 8192;
        if constexpr (S == 2) {
            const char* s = ws + WS_ST + (size_t)(p * 128 + lw * 32) * 8;
            const unsigned v0 = (lane >> 5) * 16 * (S_ * 8) + (lane & 31) * 8;
#pragma unroll
            for (int i = 0; i < 16; ++i) async_ld(sp[i], v0 + i * (S_ * 8), s);
        }
    }, [&]<int M, int PH>() {
        if constexpr (M == 0 && PH == 0) {
#pragma unroll
            for (int i = 0; i < 16; ++i) PIN(sp[i]);
        }
        if constexpr (PROG && M == 13 && PH == 1) { f2_w(ws, ldsb, 5, lw, lane); f2_w(ws, ldsb, 6, lw, lane); f2_w(ws, ldsb, 7, lw, lane); }
        if constexpr (PROG && M == 14 && PH == 1) { f2_w(ws, ldsb, 0, lw, lane); f2_w(ws, ldsb, 8, lw, lane); }
        if constexpr (PROG && M == 15 && PH == 1) { f2_w(ws, ldsb, 1, lw, lane); f2_w(ws, ldsb, 2, lw, lane); f2_w(ws, ldsb, 3, lw, lane); }
        if constexpr (M == -1 && PH == 1) { tcap<7>(TL); }
        if constexpr (M == 15 && PH == 1) { tcap<8>(TL); }
        if constexpr (M == 8 && PH == 1) async_ld(fl, (lane & 15) * 4, ws + WS_FLAG + FL_Y + (p * 16 + j) * 128);
        if constexpr (M == 10 && PH == 1) { pin(fl); if (!__all(fl >= 2u)) panel_wait(ws, FL_Y, p, j, 2u, lane); }
        if constexpr (M == 2 && PH == 1) {
            float s = 0.f;
#pragma unroll
            for (int i = 0; i < 16; ++i) s += sp[i][0];
            const float mu = swap32_add(s) * (1.f / 32.f);
            float q = 0.f;
#pragma unroll
            for (int i = 0; i < 16; ++i) { const float d = sp[i][0] - mu; q += sp[i][1] + 32.f * d * d; }
            const float rl = -1.4426950408889634f * __builtin_amdgcn_rsqf(swap32_add(q) * (1.f / 1024.f) + 1e-5f);
            if (lane < 32) *(LDSP f32x2*)(lds + F1_AUX + 2048 + (lw * 32 + lane) * 8) = (f32x2){mu, rl};
            asm volatile("s_waitcnt lgkmcnt(0)" ::: "memory");
        }
    });
}
struct F1Frag { bf16x8 a[4], w[7]; };
DI void f1_compute(char* __restrict__ ws, lptr lds, int lane, int wid, int p, int j) {
    if (wid == 0) STAMP(ws, 4, 0);
    TXP(ws, 5);
    const int th = wid & 1, nh = wid >> 1, g = lane >> 4;
    f32x4 acc[7][4] = {};
    const LP pa = mk_lp(lds + g * 2048 + (th * 64 + (lane & 15)) * 16), pw = mk_lp(lds + nh * 1024 + lane * 16);
    F1Frag fr[2];
    auto rd = [&]<int S, int I>() {
        constexpr int so = (S % F1_NS) * F1_STAGE;
        if constexpr (S < F1_NST && I < 11) {
            if constexpr (I >= 1 && I <= 4) fr[S & 1].a[I - 1] = lds_at<so + (I - 1) * 256>(pa);
            else fr[S & 1].w[I ? I - 4 : 0] = lds_at<so + 8192 + (I ? I - 4 : 0) * 2048>(pw);
        }
    };
    auto step = [&]<int S>() {
        if constexpr (S == 6) wait_vm<0>();
        if constexpr (S % 2 == 0) { lgkm_bar(); SBAR(); }
        if constexpr (S == 6) { if (wid == 0) panel_post(ws, FL_Y, p, j, 2u, lane); SBAR(); }
        if constexpr (ABL != 1)
            [&]<int... U>(std::integer_sequence<int, U...>) {
                ((acc[U / 4][U % 4] = mfma16(fr[S & 1].w[U / 4], fr[S & 1].a[U % 4], acc[U / 4][U % 4]), SBAR(), rd.template operator()<S + 1, U>(), SBAR()), ...);
            }(std::make_integer_sequence<int, 28>{});
    };
    if (wid == 0) STAMP(ws, 4, 1);
    bar();
    if (wid == 0) STAMP(ws, 4, 2);
    [&]<int... I>(std::integer_sequence<int, I...>) { (rd.template operator()<0, I>(), ...); }(std::make_integer_sequence<int, 11>{});
    [&]<int... S>(std::integer_sequence<int, S...>) { (step.template operator()<S>(), ...); }(std::make_integer_sequence<int, F1_NST>{});
    if (wid == 0) STAMP(ws, 4, 3);
    f32x2 st[4];
#pragma unroll
    for (int tb = 0; tb < 4; ++tb) st[tb] = *(const LDSP f32x2*)(lds + F1_AUX + 2048 + (th * 64 + tb * 16 + (lane & 15)) * 8);
    const lptr pu = lds + F1_AUX + (nh * 16 + 4 * g) * 4;
    const unsigned gp = WS_G + p * (F_ / 8 * 2048) + j * 57344, vo = nh * 4096 + th * 1024 + (g & 1) * 2048 + ((lane >> 5) * 16 + (lane & 15)) * 16;
    const LP pg = mk_lp(lds + vo);
    f32x4 u = *(const LDSP f32x4*)pu, tt = *(const LDSP f32x4*)(pu + 1024);
    const f32x2 cl = {-1.4426950408889634f, -1.4426950408889634f};
#pragma unroll
    for (int b = 0; b < 7; ++b) {
        f32x4 un, tn;
        if (b < 6) { un = *(const LDSP f32x4*)(pu + (b + 1) * 128); tn = *(const LDSP f32x4*)(pu + 1024 + (b + 1) * 128); }
        unsigned w[4][2];
#pragma unroll
        for (int tb = 0; tb < 4; ++tb)
#pragma unroll
            for (int e = 0; e < 2; ++e) {
                const f32x2 a = __builtin_elementwise_fma((f32x2){u[2 * e], u[2 * e + 1]}, (f32x2){-st[tb][0], -st[tb][0]}, (f32x2){acc[b][tb][2 * e], acc[b][tb][2 * e + 1]});
                const f32x2 z = __builtin_elementwise_fma(a, (f32x2){st[tb][1], st[tb][1]}, (f32x2){tt[2 * e], tt[2 * e + 1]});
                const f32x2 dn = __builtin_elementwise_fma((f32x2){__builtin_amdgcn_exp2f(z[0]), __builtin_amdgcn_exp2f(z[1])}, cl, cl);
                const f32x2 o = z * (f32x2){__builtin_amdgcn_rcpf(dn[0]), __builtin_amdgcn_rcpf(dn[1])};
                w[tb][e] = pk2(o[0], o[1]);
            }
#pragma unroll
        for (int tp = 0; tp < 2; ++tp) {
            const auto s0 = __builtin_amdgcn_permlane32_swap(w[2 * tp][0], w[2 * tp + 1][0], false, false);
            const auto s1 = __builtin_amdgcn_permlane32_swap(w[2 * tp][1], w[2 * tp + 1][1], false, false);
            const u32x4 o = {s0[0], s1[0], s0[1], s1[1]};
            st16(mk_rsrc(ws), vo + tp * 512, gp + b * 8192, o);
            *(LDSP u32x4*)(pg.b[(b * F2_STAGE) >> 16] + ((b * F2_STAGE) & 65535) + tp * 512) = o;
        }
        u = un; tt = tn;
        if (b == 1) bar();
        SBAR();
    }
    wait_vm<2 * (7 - F2_EB)>();
    lgkm_bar();
    if (wid == 0) panel_post(ws, FL_G, p, j, 1u, lane);
    if (wid == 0) STAMP(ws, 4, 4);
}

struct F2Frag { bf16x8 a[4], w[4]; };
constexpr int f2_issued(int m) { return m <= 2 ? F2_NS : cmin(2 * m - 1 + F2_NS, F2_NST); }
constexpr int f2_cnt(int s) { return s < 7 ? 0 : s < F2_NS ? 2 : s < F2_MAIN ? 4 : 3; }
DI void f2_loader(char* ws, lptr lds, int lane, int lw, int p, int j, TS& TL) {
    const int b = blockIdx.x, kh = b & 1, jn = (b >> 3) & 7;
    const unsigned ldsb = (unsigned)(uintptr_t)lds;
    const char* ga = ws + WS_G + (size_t)p * (F_ / 8 * 2048);
    const char* gw = ws + WS_W2 + (size_t)jn * 917504;
    const unsigned va0 = lw * 2048 + lane * 16, vk0 = ((lw >> 1) * 4 + kh * 2 + (lw & 1)) * 1024 + lane * 16;
    bar();
    f2_w(ws, ldsb, 4, lw, lane);
    wait_vm<0>();
    bar();
    tcap<9>(TL);
    f2_w(ws, ldsb, 9, lw, lane);
    panel_wait(ws, FL_G, p, j, ((lane >> 3) & 1) == kh, lane);
    tcap<10>(TL);
    auto issue = [&]<int S>() {
        const unsigned ma = ldsb + (S % F2_NS) * F2_STAGE + lw * 2048, off = f2_off(S), va = va0, vk = vk0;
        const char* sa = ga + off; const char* sw = gw + off;
        if constexpr (ABL == 2) {} else if constexpr (S < F2_NS) {
            asm volatile(SETM0(ma) GLDA(va, sa, 0) GLDA(va, sa, 1024)::[ma] "s"(ma), [va] "v"(va), [sa] "s"(sa) : "memory");
        } else if constexpr (S < F2_MAIN) {
            const unsigned mw = ma + 8192;
            asm volatile(SETM0(ma) GLDA(va, sa, 0) GLDA(va, sa, 1024) SETM0(mw) GLDW(va, sw, 0) GLDW(va, sw, 1024)
                         ::[ma] "s"(ma), [mw] "s"(mw), [va] "v"(va), [sa] "s"(sa), [sw] "s"(sw) : "memory");
        } else {
            const unsigned mw = ldsb + (S % F2_NS) * F2_STAGE + 8192 + lw * 1024;
            asm volatile(SETM0(ma) GLDA(va, sa, 0) GLDA(va, sa, 1024) SETM0(mw) GLDW(vk, sw, 0)
                         ::[ma] "s"(ma), [mw] "s"(mw), [va] "v"(va), [vk] "v"(vk), [sa] "s"(sa), [sw] "s"(sw) : "memory");
        }
    };
    auto range = [&]<int S0, int... I>(std::integer_sequence<int, I...>) { (issue.template operator()<S0 + I>(), ...); };
    range.template operator()<7>(std::make_integer_sequence<int, F2_NS - 7>{});
    unsigned fl;
    static_assert(f2_issued(F2_PA + 2) <= F2_LATE, "");
    auto sync = [&]<int M>() {
        constexpr int need = cmin(2 * M + 2, F2_NST - 1), have = f2_issued(M);
        constexpr int from = M == F2_PA + 2 ? f2_issued(F2_PA + 1) : need + 1;
        constexpr int out = [] { int c = M == F2_PA + 1 ? 1 : 0; for (int s = from; s < have; ++s) c += f2_cnt(s); return c; }();
        static_assert(out < 64 && need < have, "");
        wait_vm<out>();
        bar();
        if constexpr (M == F2_PA + 2) {
            pin(fl);
            if (!__all(fl >= 2u)) panel_wait(ws, FL_G, p, j, 2u, lane);
        }
        range.template operator()<have>(std::make_integer_sequence<int, f2_issued(M + 1) - have>{});
        if constexpr (M == F2_PA) async_ld(fl, (lane & 15) * 4, ws + WS_FLAG + FL_G + (p * 16 + j) * 128);
        if constexpr (M == 2) tcap<11>(TL);
        if constexpr (M == F2_PA + 2) tcap<12>(TL);
    };
    [&]<int... I>(std::integer_sequence<int, I...>) { (sync.template operator()<I + 2>(), ...); }(std::make_integer_sequence<int, F2_NST / 2 - 2>{});
}
DI void f2_compute(char* __restrict__ ws, u16* __restrict__ out, lptr lds, int lane, int wid, int p, int j) {
    const int b = blockIdx.x, kh = b & 1, jn = (b >> 3) & 7;
    if (wid == 0) STAMP(ws, 5, 0);
    TXP(ws, 6);
    const int th = wid & 1, nh = wid >> 1, g = lane >> 4;
    f32x4 acc[4][4] = {};
    const LP pa = mk_lp(lds + g * 2048 + (th * 64 + (lane & 15)) * 16);
    const LP pk = mk_lp(lds + (nh * 4 + kh * 2) * 1024 + lane * 16), ps = mk_lp(lds + (nh * 4 + (kh ^ 1) * 2) * 1024 + lane * 16);
    F2Frag fr[2];
    LP pt;
    auto rd = [&]<int S, int I>() {
        constexpr int so = (S % F2_NS) * F2_STAGE;
        if constexpr (S < F2_NST && I < (S < F2_MAIN ? 8 : 6)) {
            if constexpr (I >= 1 && I <= 4) fr[S & 1].a[I - 1] = lds_at<so + (I - 1) * 256>(pa);
            else if constexpr (I >= 6) fr[S & 1].w[I - 4] = lds_at<so + 8192 + (I - 6) * 1024>(ps);
            else fr[S & 1].w[I ? 1 : 0] = lds_at<so + 8192 + (I ? 1024 : 0)>(S < F2_MAIN ? pk : pt);
        }
    };
    auto step = [&]<int S>() {
        if constexpr (ABL != 1)
            [&]<int... U>(std::integer_sequence<int, U...>) {
                ((acc[U / 4][U % 4] = mfma16(fr[S & 1].w[U / 4], fr[S & 1].a[U % 4], acc[U / 4][U % 4]), SBAR(), rd.template operator()<S + 1, U>(), SBAR()), ...);
            }(std::make_integer_sequence<int, S < F2_MAIN ? 16 : 8>{});
    };
    if (wid == 0) STAMP(ws, 5, 1);
    [&]<int... I>(std::integer_sequence<int, I...>) { (rd.template operator()<0, I>(), ...); }(std::make_integer_sequence<int, 8>{});
    static_assert(F2_MAIN % 2 == 0 && F2_D >= 4, "");
    const rsrc_t rws = mk_rsrc(ws);
    u32x4 xr[4]; f32x4 gb[2];
    const int c8 = jn * 16 + nh * 8 + kh * 4 + g;
    auto pair = [&]<int M>() {
        if constexpr (2 * M == F2_PS) wait_vm<0>();
        if constexpr (M >= 2) lgkm_bar();
        if constexpr (2 * M == F2_PS) { if (wid == 0) panel_post(ws, FL_G, p, j, 2u, lane); }
        if constexpr (M == F2_XP) {
            asm volatile("" ::: "memory");
#pragma unroll
            for (int tb = 0; tb < 4; ++tb) xr[tb] = ld16(rws, WS_X2 + p * 262144 + c8 * 2048 + (th * 64 + tb * 16 + (lane & 15)) * 16);
            gb[0] = ld16f(rws, WS_MOD + (5120 + c8 * 8) * 4); gb[1] = ld16f(rws, WS_MOD + (5120 + c8 * 8 + 4) * 4);
            SBAR();
        }
        SBAR();
        step.template operator()<2 * M>(); step.template operator()<2 * M + 1>();
    };
    [&]<int... M>(std::integer_sequence<int, M...>) { (pair.template operator()<M>(), ...); }(std::make_integer_sequence<int, F2_MAIN / 2 - 1>{});
    pt = mk_lp(lds + nh * 2048 + lane * 16);
    lgkm_bar(); SBAR();
    step.template operator()<F2_MAIN - 2>(); step.template operator()<F2_MAIN - 1>();
    if (wid == 0) STAMP(ws, 5, 2);
    const unsigned exo = WS_EX + wid * 4096 + lane * 16;
#pragma unroll
    for (int i = 0; i < 4; ++i) {
        u32x4 o = {pk2(acc[2][i][0], acc[2][i][1]), pk2(acc[2][i][2], acc[2][i][3]), pk2(acc[3][i][0], acc[3][i][1]), pk2(acc[3][i][2], acc[3][i][3])};
#pragma unroll
        for (int e = 0; e < 4; ++e) o[e] = o[e] < 0xfffffffeu ? o[e] : 0xfffffffeu;
        st16<16>(rws, exo + i * 1024, b * 16384, o);
    }
    SBAR();
    u32x4 pr[4];
    TS TT; TCAP(TT, 0);
    auto tail = [&]<int M>() {
        lgkm_bar(); SBAR();
        if constexpr (M == F2_MP) {
#pragma unroll
            for (int i = 0; i < 4; ++i) pr[i] = ld16(rws, exo + i * 1024 + (b ^ 1) * 16384);
            SBAR();
        }
        step.template operator()<F2_MAIN + 2 * M>(); step.template operator()<F2_MAIN + 2 * M + 1>();
    };
    [&]<int... I>(std::integer_sequence<int, I...>) { (tail.template operator()<I>(), ...); }(std::make_integer_sequence<int, F2_D>{});
    for (int spin = 0; spin < (1 << 20); ++spin) {
        unsigned m = 0;
#pragma unroll
        for (int i = 0; i < 16; ++i) m = m > pr[i >> 2][i & 3] ? m : pr[i >> 2][i & 3];
        if (!__any(m == 0xffffffffu)) break;
        asm volatile("" ::: "memory");
#pragma unroll
        for (int i = 0; i < 4; ++i) pr[i] = ld16(rws, exo + i * 1024 + (b ^ 1) * 16384);
    }
    if (wid == 0) STAMP(ws, 5, 3);
#pragma unroll
    for (int tb = 0; tb < 4; ++tb) {
        const f32x4 lo = acc[0][tb] + (f32x4){bflo(pr[tb][0]), bfhi(pr[tb][0]), bflo(pr[tb][1]), bfhi(pr[tb][1])};
        const f32x4 hi = acc[1][tb] + (f32x4){bflo(pr[tb][2]), bfhi(pr[tb][2]), bflo(pr[tb][3]), bfhi(pr[tb][3])};
        const u32x4 xv = xr[tb];
        const u32x4 o = {pk2(bflo(xv[0]) + gb[0][0] * lo[0], bfhi(xv[0]) + gb[0][1] * lo[1]), pk2(bflo(xv[1]) + gb[0][2] * lo[2], bfhi(xv[1]) + gb[0][3] * lo[3]),
                         pk2(bflo(xv[2]) + gb[1][0] * hi[0], bfhi(xv[2]) + gb[1][1] * hi[1]), pk2(bflo(xv[3]) + gb[1][2] * hi[2], bfhi(xv[3]) + gb[1][3] * hi[3])};
        st16(mk_rsrc(out), (lane & 15) * (D_ * 2) + c8 * 16, (p * 128 + th * 64 + tb * 16) * (D_ * 2), o);
    }
    if (wid == 0) STAMP(ws, 5, 4);
    TXP(ws, 7);
    if (wid == 0) TDUMP(TT, ws, 7, F2_D + 1);
}

constexpr int MAIN_LDS = 163840;
__global__ void __launch_bounds__(512) k_main(const u16* __restrict__ x, const u16* __restrict__ wk, const u16* __restrict__ wv, const u16* __restrict__ wo, const u16* __restrict__ w1, const u16* __restrict__ w2,
                                              char* __restrict__ ws, u16* __restrict__ out) {
    extern __shared__ __attribute__((aligned(16))) char smem[];
    const lptr lds = (lptr)smem;
    const int lane = threadIdx.x & 63, wid = __builtin_amdgcn_readfirstlane(threadIdx.x >> 6);
    int p, j; tile_map(blockIdx.x, p, j);
    TS TL;
    ph_ln_kv(x, wk, wv, ws, lds, lane, wid, p, j);
    ph_qattn(wo, w1, w2, ws, lds, lane, wid, p, j, TL);
    if (wid >= 4) {
        if (wid < 6) { op_loader<4>(ws, lds, lane, wid - 4, p, j, TL); f1_loader<4>(ws, p, j, lds, wid - 4, lane, TL); }
        else { op_loader<3>(ws, lds, lane, wid - 4, p, j, TL); f1_loader<3>(ws, p, j, lds, wid - 4, lane, TL); }
        f2_loader(ws, lds, lane, wid - 4, p, j, TL);
        if (wid == 4) TDUMP(TL, ws, 6, 13);
    } else {
        op_compute(x, ws, lds, lane, wid, p, j);
        f1_compute(ws, lds, lane, wid, p, j);
        f2_compute(ws, out, lds, lane, wid, p, j);
    }
}

extern "C" void kernel_launch(void* const* d_in, const int* in_sizes, int n_in, void* d_out, int out_size, void* d_ws, size_t ws_size,
                              hipStream_t stream) {
    const u16* x = (const u16*)d_in[0];
    const u16* wq = (const u16*)d_in[1];
    const u16* wk = (const u16*)d_in[2];
    const u16* wv = (const u16*)d_in[3];
    const u16* wo = (const u16*)d_in[4];
    const u16* w1 = (const u16*)d_in[6];
    const u16* w2 = (const u16*)d_in[7];
    const u16* c = (const u16*)d_in[8];
    const u16* wmod = (const u16*)d_in[9];
    char* ws = (char*)d_ws;
    (void)hipFuncSetAttribute((const void*)k_main, hipFuncAttributeMaxDynamicSharedMemorySize, MAIN_LDS);
    k_prep<<<256, 512, 32768, stream>>>(c, wmod, wq, ws);
    k_main<<<256, 512, MAIN_LDS, stream>>>(x, wk, wv, wo, w1, w2, ws, (u16*)d_out);
}
```

```cpp
#pragma clang diagnostic ignored "-Wc++20-extensions"
#include <hip/hip_runtime.h>
#include <hip/hip_bf16.h>
#include <stdint.h>

typedef unsigned short u16;
typedef __bf16 bf16x8 __attribute__((ext_vector_type(8)));
typedef __bf16 bf16x2 __attribute__((ext_vector_type(2)));
typedef short s16x4 __attribute__((ext_vector_type(4)));
typedef short s16x8 __attribute__((ext_vector_type(8)));
typedef float f32x16 __attribute__((ext_vector_type(16)));
typedef float f32x4 __attribute__((ext_vector_type(4)));
typedef float f32x2 __attribute__((ext_vector_type(2)));
typedef unsigned u32x4 __attribute__((ext_vector_type(4)));
typedef unsigned u32x2 __attribute__((ext_vector_type(2)));
#define LDSP __attribute__((address_space(3)))
typedef LDSP char* lptr;
#define DI __device__ __forceinline__

constexpr int S_ = 2048, D_ = 1024, F_ = 3584;
constexpr size_t WS_MOD = 0;
constexpr size_t WS_UT = 32768;
constexpr size_t WS_KT = 65536;
constexpr size_t WS_VT = WS_KT + 131072;
constexpr size_t WS_MODP = WS_VT + 131072;
constexpr size_t WS_BUT = WS_MODP + 98304;
constexpr size_t WS_UTP = WS_BUT + 4096;
constexpr size_t WS_H = 1u << 20;
constexpr size_t WS_O = WS_H + (4u << 20);
constexpr size_t WS_X2 = WS_O + (4u << 20);
constexpr size_t WS_Y = WS_X2 + (4u << 20);
constexpr size_t WS_G = WS_Y + (4u << 20);
constexpr size_t WS_WQ = 32u << 20;
constexpr size_t WS_WO = WS_WQ + (2u << 20);
constexpr size_t WS_W1 = WS_WO + (2u << 20);
constexpr size_t WS_W2 = WS_W1 + (7u << 20);
constexpr size_t WS_FLAG = WS_UTP + 131072;
constexpr int FL_EX = 0, FL_O = 32768, FL_Y = 65536, FL_G = 98304, FL_H = 131072, FL_SIDE = 163840, FL_KV = FL_SIDE + 8192, FL_END = FL_KV + 8192;
constexpr size_t WS_ST = 60u << 20;
constexpr size_t WS_EX = 52u << 20;
static_assert(WS_FLAG + FL_END <= WS_H && FL_END % (256 * 4) == 0, "");
static_assert(WS_UTP + 4 * 2 * 3584 * 4 <= WS_H, "");

constexpr size_t WS_DBG = 64u << 20;
#define STAMP(ws, kid, i) do {} while (0)
struct TS {};
#define TCAP(T, i) do {} while (0)
#define TDUMP(T, ws, kid, n) do {} while (0)
#define TXP(ws, i) do {} while (0)
DI float bf2f(u16 v) { return __uint_as_float((unsigned)v << 16); }
DI float bflo(unsigned v) { return __uint_as_float(v << 16); }
DI float bfhi(unsigned v) { return __uint_as_float(v & 0xffff0000u); }
DI unsigned pk2(float a, float b) { f32x2 v = {a, b}; return __builtin_bit_cast(unsigned, __builtin_convertvector(v, bf16x2)); }
DI float rbf(float a) { return bflo(pk2(a, 0.f)); }
DI f32x16 mfma32(bf16x8 a, bf16x8 b, f32x16 c) { return __builtin_amdgcn_mfma_f32_32x32x16_bf16(a, b, c, 0, 0, 0); }
DI f32x4 mfma16(bf16x8 a, bf16x8 b, f32x4 c) { return __builtin_amdgcn_mfma_f32_16x16x32_bf16(a, b, c, 0, 0, 0); }
DI bf16x8 lds_b128(lptr p) { return *(const LDSP bf16x8*)p; }
DI s16x4 lds_tr(lptr p) { return __builtin_amdgcn_ds_read_tr16_b64_v4i16((LDSP s16x4*)p); }
DI bf16x8 cat(s16x4 a, s16x4 b) { return __builtin_bit_cast(bf16x8, __builtin_shufflevector(a, b, 0, 1, 2, 3, 4, 5, 6, 7)); }
#define WT 16
typedef __amdgpu_buffer_rsrc_t rsrc_t;
DI rsrc_t mk_rsrc(void* base) { return __builtin_amdgcn_make_buffer_rsrc(base, 0, 0x7fffffff, 0x00020000); }
template <int POL = WT> DI void st16(rsrc_t r, unsigned voff, unsigned soff, u32x4 v) { __builtin_amdgcn_raw_buffer_store_b128(v, r, voff + soff, 0, POL); }
struct LP { lptr b[3]; };
DI LP mk_lp(lptr p) { LP r = {{p, p + 65536, p + 131072}}; asm volatile("" : "+v"(r.b[0]), "+v"(r.b[1]), "+v"(r.b[2])); return r; }
template <int OFF> DI bf16x8 lds_at(const LP& p) { return *(const LDSP bf16x8*)(p.b[OFF >> 16] + (OFF & 65535)); }
#define SBAR() __builtin_amdgcn_sched_barrier(0)
#define PIN(x) asm volatile("" : "+v"(x))
template <class T> DI void pin(T& x) { PIN(x); }
template <int N> DI void wait_vm() { asm volatile("s_waitcnt vmcnt(%0)" ::"i"(N) : "memory"); }
DI void bar() { asm volatile("s_barrier" ::: "memory"); }
DI void lgkm_bar() { asm volatile("s_waitcnt lgkmcnt(0)\n\ts_barrier" ::: "memory"); }
template <int C> DI float dpp_add(float v) { return v + __builtin_bit_cast(float, __builtin_amdgcn_update_dpp(0, __builtin_bit_cast(int, v), C, 0xf, 0xf, true)); }
DI float swap16_add(float v) { const auto r = __builtin_amdgcn_permlane16_swap(__float_as_uint(v), __float_as_uint(v), false, false); return __uint_as_float(r[0]) + __uint_as_float(r[1]); }
DI float swap32_add(float v) { const auto r = __builtin_amdgcn_permlane32_swap(__float_as_uint(v), __float_as_uint(v), false, false); return __uint_as_float(r[0]) + __uint_as_float(r[1]); }
DI float wave_sum(float v) {
    v = dpp_add<0xB1>(v); v = dpp_add<0x4E>(v); v = dpp_add<0x141>(v); v = dpp_add<0x140>(v);
    return swap32_add(swap16_add(v));
}

#define ABL 0
#define AA 0
#define PRIO 1
#define GLDS(v, s, off) "global_load_lds_dwordx4 %[" #v "], %[" #s "] offset:" #off "\n\t"
#define GLD1(v, s, off) "global_load_lds_dwordx4 %[" #v "], %[" #s "] offset:" #off " sc1\n\t"
#define POLW " sc1"
#define POLA " sc1"
#define GLDW(v, s, off) "global_load_lds_dwordx4 %[" #v "], %[" #s "] offset:" #off POLW "\n\t"
#define GLDA(v, s, off) "global_load_lds_dwordx4 %[" #v "], %[" #s "] offset:" #off POLA "\n\t"
#define SETM0(m) "s_mov_b32 m0, %[" #m "]\n\ts_nop 0\n\t"

typedef __attribute__((address_space(1))) unsigned* gu32;
DI void flag_st(char* ws, unsigned off, unsigned v) { __hip_atomic_store((gu32)(ws + WS_FLAG + off), v, __ATOMIC_RELAXED, __HIP_MEMORY_SCOPE_AGENT); }
DI u32x4 ld16(rsrc_t r, unsigned off) { return __builtin_amdgcn_raw_buffer_load_b128(r, off, 0, 16); }
DI f32x4 ld16f(rsrc_t r, unsigned off) { return __builtin_bit_cast(f32x4, ld16(r, off)); }
DI void async_ld(u32x4& d, unsigned v, const char* s) { asm volatile("global_load_dwordx4 %0, %1, %2 sc1" : "=v"(d) : "v"(v), "s"(s) : "memory"); }
DI void async_ld(unsigned& d, unsigned v, const char* s) { asm volatile("global_load_dword %0, %1, %2 sc1" : "=v"(d) : "v"(v), "s"(s) : "memory"); }
DI void async_ld(f32x2& d, unsigned v, const char* s) { asm volatile("global_load_dwordx2 %0, %1, %2 sc1" : "=v"(d) : "v"(v), "s"(s) : "memory"); }
DI void touch(unsigned& d, unsigned v, const char* s) { asm volatile("global_load_dword %0, %1, %2 sc1" : "+v"(d) : "v"(v), "s"(s) : "memory"); }
DI void panel_post(char* ws, int fl, int p, int j, unsigned v, int lane) { if (lane < 16) flag_st(ws, fl + (p * 16 + lane) * 128 + j * 4, v); }
DI void panel_wait(char* ws, int fl, int p, int j, unsigned v, int lane) {
    const gu32 f = (gu32)(ws + WS_FLAG + fl + (p * 16 + j) * 128) + (lane & 15);
    for (int spin = 0; spin < (1 << 22) && !__all(__hip_atomic_load(f, __ATOMIC_RELAXED, __HIP_MEMORY_SCOPE_AGENT) >= v); ++spin) __builtin_amdgcn_s_sleep(1);
    asm volatile("" ::: "memory");
}
DI void grid_post(char* ws, int fl, unsigned v, int lane) { if (lane < 8) flag_st(ws, fl + lane * 1024 + blockIdx.x * 4, v); }
DI bool grid_ok(u32x4 f, unsigned v) { return __all(f[0] >= v && f[1] >= v && f[2] >= v && f[3] >= v); }
DI void grid_wait(char* ws, int fl, unsigned v, int lane) {
    for (int spin = 0; spin < (1 << 22) && !grid_ok(ld16(mk_rsrc(ws), WS_FLAG + fl + (blockIdx.x & 7) * 1024 + lane * 16), v); ++spin) { __builtin_amdgcn_s_sleep(1); asm volatile("" ::: "memory"); }
    asm volatile("" ::: "memory");
}

DI void tile_issue(const u16* src, int ld, unsigned ldsb, int lane) {
    const unsigned vo = (unsigned)((lane >> 3) * ld * 2 + (lane & 7) * 16);
#pragma unroll
    for (int i = 0; i < 4; ++i) {
        const u16* s = src + (size_t)i * 8 * ld;
        const unsigned m = ldsb + i * 1024;
        asm volatile(SETM0(m) GLDS(v, s, 0)::[m] "s"(m), [v] "v"(vo), [s] "s"(s) : "memory");
    }
}
template <bool PERM> DI void tile_store32(lptr st, rsrc_t r, unsigned dst, int lane) {
    const lptr p = st + ((PERM ? 4 : 8) * (lane >> 5) + ((lane & 15) >> 2)) * 128 + ((lane >> 4) & 1) * 32 + (lane & 3) * 8;
    bf16x8 f[4];
#pragma unroll
    for (int i = 0; i < 4; ++i) { const lptr q = p + (i >> 1) * 2048 + (i & 1) * 64; f[i] = cat(lds_tr(q), lds_tr(q + (PERM ? 1024 : 512))); }
#pragma unroll
    for (int i = 0; i < 4; ++i) st16(r, i * 1024 + lane * 16, dst, __builtin_bit_cast(u32x4, f[i]));
}

__global__ void __launch_bounds__(512) k_prep(const u16* __restrict__ c, const u16* __restrict__ wmod, const u16* __restrict__ wq, char* __restrict__ ws) {
    extern __shared__ __attribute__((aligned(16))) char smem[];
    const lptr lds = (lptr)smem;
    const int t = threadIdx.x, lane = t & 63, wid = __builtin_amdgcn_readfirstlane(t >> 6), b = blockIdx.x;
    if (wid == 0) STAMP(ws, 0, 0);
    TXP(ws, 0);
    if (t < FL_END / 1024) *(unsigned*)(ws + WS_FLAG + (b * (FL_END / 1024) + t) * 4) = 0u;
    if (b >= 192) {
        const int j = (b - 192) & 15, kg = (b - 192) >> 4;
        tile_issue(wq + (size_t)(kg * 256 + wid * 32) * D_ + j * 64, D_, (unsigned)(uintptr_t)lds + wid * 4096, lane);
        wait_vm<0>();
        tile_store32<false>(lds + wid * 4096, mk_rsrc(ws), WS_WQ + j * 131072 + (kg * 8 + wid) * 4096, lane);
        if (wid == 0) STAMP(ws, 0, 1);
        return;
    }
    const int cg = b % 48, kg = b / 48, ch = t & 15, rg = t >> 4;
    u32x4 w[8]; u16 cv[8];
#pragma unroll
    for (int i = 0; i < 8; ++i) w[i] = __builtin_nontemporal_load((const u32x4*)(wmod + (size_t)(kg * 256 + i * 32 + rg) * 6144 + cg * 128 + ch * 8));
#pragma unroll
    for (int i = 0; i < 8; ++i) cv[i] = c[kg * 256 + i * 32 + rg];
    float acc[8] = {0, 0, 0, 0, 0, 0, 0, 0};
#pragma unroll
    for (int i = 0; i < 8; ++i) {
        const float f = bf2f(cv[i]);
        const float s = rbf(f / (1.f + __expf(-f)));
#pragma unroll
        for (int j = 0; j < 4; ++j) { acc[2 * j] += s * bflo(w[i][j]); acc[2 * j + 1] += s * bfhi(w[i][j]); }
    }
#pragma unroll
    for (int j = 0; j < 8; ++j) acc[j] = swap32_add(swap16_add(acc[j]));
    if (lane < 16) {
        *(LDSP f32x4*)(lds + wid * 512 + ch * 32) = (f32x4){acc[0], acc[1], acc[2], acc[3]};
        *(LDSP f32x4*)(lds + wid * 512 + ch * 32 + 16) = (f32x4){acc[4], acc[5], acc[6], acc[7]};
    }
    lgkm_bar();
    if (t < 128) {
        float v = 0.f;
#pragma unroll
        for (int i = 0; i < 8; ++i) v += *(const LDSP float*)(lds + i * 512 + t * 4);
        ((float*)(ws + WS_MODP))[kg * 6144 + cg * 128 + t] = v;
    }
    if (wid == 0) STAMP(ws, 0, 1);
}

#define R0 3
constexpr int ring_issued(int i, int NST, int NS) { int n = R0 < NS - 1 ? R0 : NS - 1; for (int k = 0; k < i; ++k) { n += 2; if (n > k + NS) n = k + NS; if (n > NST) n = NST; } return n; }
template <int I> DI void tcap(TS& T) { TCAP(T, I); }
struct NoEx { static constexpr int n(int) { return 0; } };
template <int NST, int NS, class CNT, class EX, int PA, class F, class X>
DI void ring_loader(F issue, X extra) {
    auto range = [&]<int S0, int... I>(std::integer_sequence<int, I...>) { (issue.template operator()<S0 + I>(), ...); };
    auto sync = [&]<int I>() {
        constexpr int have = ring_issued(I, NST, NS);
        constexpr int from = PA >= 0 && I == PA + 2 ? ring_issued(PA + 1, NST, NS) : I + 1;
        constexpr int out = [] { int c = PA >= 0 && I == PA + 1; for (int s = from; s < have; ++s) c += CNT::n(s); for (int i = 0; i < I; ++i) c += EX::n(i); return c; }();
        static_assert(out < 64 && I < have && (EX::n(I) == 0 || ring_issued(I + 1, NST, NS) == NST), "");
        wait_vm<out>();
        bar();
        range.template operator()<have>(std::make_integer_sequence<int, ring_issued(I + 1, NST, NS) - have>{});
        extra.template operator()<I>();
    };
    range.template operator()<0>(std::make_integer_sequence<int, ring_issued(0, NST, NS)>{});
    [&]<int... I>(std::integer_sequence<int, I...>) { (sync.template operator()<I>(), ...); }(std::make_integer_sequence<int, NST>{});
}
constexpr int cmin(int a, int b) { return a < b ? a : b; }
constexpr int pair_issued(int m, int NST, int NS) {
    int n = 3;
    for (int k = -1; k < m; ++k) n = cmin(cmin(n + 4, k < 0 ? NS : 2 * k + 1 + NS), NST);
    return n;
}
template <int NST, int NS, class CNT, class EX, int PA, class F, class X>
DI void pair_loader(F issue, X extra) {
    auto range = [&]<int S0, int... I>(std::integer_sequence<int, I...>) { (issue.template operator()<S0 + I>(), ...); };
    auto sync = [&]<int M>() {
        constexpr int need = M < 0 ? 0 : cmin(2 * M + 2, NST - 1), have = pair_issued(M, NST, NS);
        constexpr int from = M == PA + 2 ? pair_issued(PA + 1, NST, NS) : need + 1;
        constexpr int out = [] { int c = M == PA + 1; for (int s = from; s < have; ++s) c += CNT::n(s); for (int i = 0; i < M; ++i) c += EX::n(i); return c; }();
        static_assert(out < 64 && need < have && (EX::n(M) == 0 || pair_issued(M + 1, NST, NS) == NST), "");
        wait_vm<out>();
        extra.template operator()<M, 0>();
        bar();
        range.template operator()<have>(std::make_integer_sequence<int, pair_issued(M + 1, NST, NS) - have>{});
        extra.template operator()<M, 1>();
    };
    range.template operator()<0>(std::make_integer_sequence<int, 3>{});
    [&]<int... I>(std::integer_sequence<int, I...>) { (sync.template operator()<I - 1>(), ...); }(std::make_integer_sequence<int, NST / 2 + 1>{});
}
constexpr int G64_STAGE = 24576;
template <int PRE> struct G64Cnt { static constexpr int n(int s) { return s < PRE ? 4 : 6; } };
DI void g64_w(const char* sw, unsigned ldsb, int stage, int slot, int lw, int lane) {
    const unsigned vw = lw * 2048 + lane * 16, mw = ldsb + slot * G64_STAGE + 16384 + lw * 2048;
    const char* s = sw + stage * 8192;
    if (ABL != 2) asm volatile(SETM0(mw) GLDW(vw, s, 0) GLDW(vw, s, 1024)::[mw] "s"(mw), [vw] "v"(vw), [s] "s"(s) : "memory");
}
template <int NST, int NS, int PRE, class EX, int PA, class X>
DI void g64_loader(const char* sa, const char* sw, unsigned ldsb, int lw, int lane, X extra) {
    const unsigned va = lw * 4096 + lane * 16;
    ring_loader<NST, NS, G64Cnt<PRE>, EX, PA>([&]<int S>() {
        const unsigned ma = ldsb + (S % NS) * G64_STAGE + lw * 4096;
        if (ABL != 2) asm volatile(SETM0(ma) GLDA(va, sa, 0) GLDA(va, sa, 1024) GLDA(va, sa, 2048) GLDA(va, sa, 3072)::[ma] "s"(ma), [va] "v"(va), [sa] "s"(sa) : "memory");
        if constexpr (S >= PRE) g64_w(sw, ldsb, S, S % NS, lw, lane);
        sa += 16384;
    }, extra);
}

struct G64Frag { bf16x8 a[4][2], w[4]; };
template <int NST, int NS, class H>
DI void g64_compute(lptr lds, int cw, int lane, f32x16 (&acc)[2], H hook) {
    const LP pa = mk_lp(lds + (lane >> 5) * 2048 + ((cw & 1) * 64 + (lane & 31)) * 16), pw = mk_lp(lds + (cw >> 1) * 1024 + lane * 16);
    G64Frag fr[2];
    auto rd = [&]<int S, int I>() {
        constexpr int so = (S % NS) * G64_STAGE;
        if constexpr (S < NST && I < 12) {
            if constexpr (I % 3 == 0) fr[S & 1].w[I / 3] = lds_at<so + 16384 + (I / 3) * 2048>(pw);
            else fr[S & 1].a[I / 3][I % 3 - 1] = lds_at<so + (I / 3) * 4096 + (I % 3 - 1) * 512>(pa);
        }
    };
    auto step = [&]<int S>() {
        if constexpr (S + 1 < NST) { lgkm_bar(); SBAR(); }
        if constexpr (ABL != 1)
            [&]<int... U>(std::integer_sequence<int, U...>) {
                ((acc[U % 2] = mfma32(fr[S & 1].w[U / 2], fr[S & 1].a[U / 2][U % 2], acc[U % 2]), SBAR(),
                  rd.template operator()<S + 1, 2 * U>(), rd.template operator()<S + 1, 2 * U + 1>(), SBAR()), ...);
            }(std::make_integer_sequence<int, 8>{});
        hook(S);
    };
    bar();
    [&]<int... I>(std::integer_sequence<int, I...>) { (rd.template operator()<0, I>(), ...); }(std::make_integer_sequence<int, 12>{});
    hook(-1);
    [&]<int... S>(std::integer_sequence<int, S...>) { (step.template operator()<S>(), ...); }(std::make_integer_sequence<int, NST>{});
}
DI void tile_map(int b, int& p, int& j) { const int x = b & 7, i = b >> 3; p = (x >> 1) * 4 + (i >> 3); j = (x & 1) * 8 + (i & 7); }

constexpr int K2_W = 98304, K2_MOD = 2 * G64_STAGE, K2_RED = 3 * G64_STAGE;
DI int k2_h(int kc, int tk) { return (kc >> 6) * G64_STAGE + (kc & 63) * 256 + (tk ^ (kc & 7)) * 16; }
DI void ph_ln_kv(const u16* __restrict__ x, const u16* __restrict__ wk, const u16* __restrict__ wv, char* __restrict__ ws, lptr lds, int lane, int wid, int p, int j) {
    const int t = threadIdx.x, tg = p * 8 + (j >> 1), which = j & 1;
    const rsrc_t rws = mk_rsrc(ws);
    if (wid == 0) STAMP(ws, 1, 0);
    TXP(ws, 1);
    const float* modp = (const float*)(ws + WS_MODP);
    u32x4 xr[2][2]; f32x4 mp[4];
    {
        const u16* xp = x + (size_t)(tg * 16 + wid * 2) * D_;
        const unsigned vx = lane * 16, vm = t * 16;
        asm volatile("global_load_dwordx4 %4, %9, %11\n\tglobal_load_dwordx4 %5, %9, %12\n\tglobal_load_dwordx4 %6, %9, %13\n\tglobal_load_dwordx4 %7, %9, %14\n\t"
                     "global_load_dwordx4 %0, %8, %10\n\tglobal_load_dwordx4 %1, %8, %10 offset:1024\n\t"
                     "global_load_dwordx4 %2, %8, %10 offset:2048\n\tglobal_load_dwordx4 %3, %8, %10 offset:3072\n\ts_barrier"
                     : "=&v"(xr[0][0]), "=&v"(xr[0][1]), "=&v"(xr[1][0]), "=&v"(xr[1][1]), "=&v"(mp[0]), "=&v"(mp[1]), "=&v"(mp[2]), "=&v"(mp[3])
                     : "v"(vx), "v"(vm), "s"(xp), "s"(modp), "s"(modp + 6144), "s"(modp + 2 * 6144), "s"(modp + 3 * 6144) : "memory");
    }
    {
        const u16* W = (which ? wv : wk) + wid * 4096;
        const int row = lane >> 2, chn = (lane & 3) ^ (((row >> 3) & 1) << 1);
        const unsigned vo = (unsigned)(row * 64 + chn * 16);
#pragma unroll
        for (int q = 0; q < 2; ++q) {
            const unsigned m = (unsigned)(uintptr_t)lds + K2_W + wid * 8192 + q * 4096;
            const u16* sb_ = W + q * 2048;
            asm volatile(SETM0(m) GLDS(v, s, 0) GLDS(v, s, 1024) GLDS(v, s, 2048) GLDS(v, s, 3072)::[m] "s"(m), [v] "v"(vo), [s] "s"(sb_) : "memory");
        }
    }
    float mf[4] = {0, 0, 0, 0};
    asm volatile("s_waitcnt vmcnt(12)" : "+v"(mp[0]), "+v"(mp[1]), "+v"(mp[2]), "+v"(mp[3])::"memory");
    {
        f32x4 m = (mp[0] + mp[1]) + (mp[2] + mp[3]);
        if (t >= 256) {
#pragma unroll
            for (int e = 0; e < 4; ++e) m[e] = 1.f + rbf(m[e]);
        }
        *(LDSP u32x2*)(lds + K2_MOD + t * 8) = (u32x2){pk2(m[0], m[1]), pk2(m[2], m[3])};
    }
    asm volatile("s_waitcnt vmcnt(8)" : "+v"(xr[0][0]), "+v"(xr[0][1]), "+v"(xr[1][0]), "+v"(xr[1][1])::"memory");
    if (t < 24) {
#pragma unroll
        for (int i = 0; i < 4; ++i) mf[i] = modp[i * 6144 + blockIdx.x * 24 + t];
    }
    f32x2 v[2][8]; float rstd[2];
#pragma unroll
    for (int r = 0; r < 2; ++r) {
#pragma unroll
        for (int j = 0; j < 8; ++j) v[r][j] = (f32x2){bflo(xr[r][j >> 2][j & 3]), bfhi(xr[r][j >> 2][j & 3])};
        const f32x2 s = ((v[r][0] + v[r][1]) + (v[r][2] + v[r][3])) + ((v[r][4] + v[r][5]) + (v[r][6] + v[r][7]));
        const float mean = wave_sum(s[0] + s[1]) * (1.f / 1024.f);
        f32x2 q = {0.f, 0.f};
#pragma unroll
        for (int j = 0; j < 8; ++j) { v[r][j] -= mean; q = __builtin_elementwise_fma(v[r][j], v[r][j], q); }
        rstd[r] = __builtin_amdgcn_rsqf(wave_sum(q[0] + q[1]) * (1.f / 1024.f) + 1e-5f);
    }
    if (wid == 0) STAMP(ws, 1, 1);
    lgkm_bar();
#pragma unroll
    for (int h = 0; h < 2; ++h) {
        const u32x4 ms = *(const LDSP u32x4*)(lds + K2_MOD + h * 1024 + lane * 16), ma = *(const LDSP u32x4*)(lds + K2_MOD + 2048 + h * 1024 + lane * 16);
        u32x4 o[2];
#pragma unroll
        for (int j = 0; j < 4; ++j) {
            const f32x2 sa = {bflo(ms[j]), bfhi(ms[j])}, a1 = {bflo(ma[j]), bfhi(ma[j])};
#pragma unroll
            for (int r = 0; r < 2; ++r) { const f32x2 y = __builtin_elementwise_fma(v[r][h * 4 + j] * rstd[r], a1, sa); o[r][j] = pk2(y[0], y[1]); }
        }
#pragma unroll
        for (int r = 0; r < 2; ++r) *(LDSP u32x4*)(lds + k2_h(h * 64 + lane, wid * 2 + r)) = o[r];
    }
    lgkm_bar();
    {
        const unsigned hAT = WS_H + (tg >> 3) * 262144 + (tg & 7) * 256;
#pragma unroll
        for (int i = 0; i < 2; ++i) {
            const int kc = which * 64 + i * 32 + (t >> 4), tk = t & 15;
            st16(rws, kc * 2048 + tk * 16, hAT, *(const LDSP u32x4*)(lds + k2_h(kc, tk)));
        }
    }
    wait_vm<2>();
    f32x4 acc[2] = {{0, 0, 0, 0}, {0, 0, 0, 0}};
    {
        const int g = lane >> 4, tk = lane & 15, q = (lane & 15) >> 2, p = lane & 3;
#pragma unroll
        for (int s = 0; s < 4; ++s) {
            const int kc = wid * 16 + s * 4 + g;
            const bf16x8 hf = lds_b128(lds + k2_h(kc, tk));
            const int row = kc * 8 + q;
#pragma unroll
            for (int nb = 0; nb < 2; ++nb) {
                const lptr wp = lds + K2_W + row * 64 + ((nb ^ (kc & 1)) * 32) + p * 8;
                const bf16x8 wf = cat(lds_tr(wp), lds_tr(wp + 256));
                acc[nb] = which ? mfma16(hf, wf, acc[nb]) : mfma16(wf, hf, acc[nb]);
            }
        }
    }
#pragma unroll
    for (int nb = 0; nb < 2; ++nb) *(LDSP f32x4*)(lds + K2_RED + ((wid * 2 + nb) * 64 + lane) * 16) = acc[nb];
    wait_vm<0>();
    lgkm_bar();
    if (wid == 3) panel_post(ws, FL_H, p, j, 1u, lane);
    if (wid >= 4) {
#pragma unroll
        for (int i = 0; i < 4; ++i) g64_w(ws + WS_WQ + (size_t)j * 131072, (unsigned)(uintptr_t)lds, i, i, wid - 4, lane);
    }
    if (wid == 0) STAMP(ws, 1, 2);
    if (t < 128) {
        const int nb = t >> 6;
        f32x4 r = *(const LDSP f32x4*)(lds + K2_RED + (nb * 64 + lane) * 16);
#pragma unroll
        for (int w = 1; w < 8; ++w) r += *(const LDSP f32x4*)(lds + K2_RED + ((w * 2 + nb) * 64 + lane) * 16);
        u32x2 o = {pk2(r[0], r[1]), pk2(r[2], r[3])};
        const int g = lane >> 4, c = lane & 15, kt = tg >> 1;
        if (which) {
            __builtin_amdgcn_raw_buffer_store_b64(o, rws, WS_VT + kt * 2048 + ((tg & 1) * 2 + (g & 1)) * 512 + (nb * 16 + c) * 16 + (g >> 1) * 8, 0, 16);
        } else {
            __builtin_amdgcn_raw_buffer_store_b64(o, rws, WS_KT + kt * 2048 + (nb * 2 + (g & 1)) * 512 + ((tg & 1) * 16 + c) * 16 + (g >> 1) * 8, 0, 16);
        }
    }
    if (t < 24) {
        const int col = blockIdx.x * 24 + t, part = col >> 10, k = col & 1023;
        float v = rbf((mf[0] + mf[1]) + (mf[2] + mf[3]));
        if (part == 1 || part == 4) v = rbf(1.f + v);
        __builtin_amdgcn_raw_buffer_store_b32(__float_as_uint(v), rws, WS_MOD + col * 4, 0, 16);
        if (part == 3 || part == 4)
            __builtin_amdgcn_raw_buffer_store_b16((u16)(__float_as_uint(v) >> 16), rws,
                WS_BUT + ((((k >> 5) * 2 + (part == 3)) * 4 + 2 * ((k >> 4) & 1) + ((k >> 2) & 1)) * 8 + (k & 3) + 4 * ((k >> 3) & 1)) * 2, 0, 16);
    }
    if (wid == 0) STAMP(ws, 1, 3);
}

constexpr int QA_NS = 4;
constexpr int QA_KV0 = QA_NS * G64_STAGE;
constexpr int QA_LDS = QA_KV0 + 65536;
constexpr float QSCALE = 0.17677669529663687f * 1.4426950408889634f;

constexpr int OP_NS = 6;
#define OPRE 4
#define PREW 0
#define KVL 8
#define KVC 11
#define KVN 4
#define KVP 1
#define PROG 1
DI void ph_qattn(const u16* __restrict__ wo, const u16* __restrict__ w1, const u16* __restrict__ w2, char* __restrict__ ws, lptr lds, int lane, int wid, int p, int j, TS& TL) {
    const unsigned ldsb = (unsigned)(uintptr_t)lds;
    const char* KT = ws + WS_KT; const char* VT = ws + WS_VT;
    const unsigned vkv = lane * 16;
    auto kv_piece = [&](int chunk, unsigned buf, int w, int i) {
        const char* s = ((i & 4) ? VT : KT) + chunk * 32768 + w * 4096 + (i & 3) * 1024;
        const unsigned m = ldsb + buf + (i >> 2) * 32768 + w * 4096 + (i & 3) * 1024;
        asm volatile(SETM0(m) GLD1(v, s, 0)::[m] "s"(m), [v] "v"(vkv), [s] "s"(s) : "memory");
    };
    const int b = blockIdx.x;
    const bool big = b < 224;
    const rsrc_t rws = mk_rsrc(ws);
    const lptr stg = lds + 65536 + wid * 4096;
    const int jA = b & 15, gA = big ? (b >> 4) : ((b - 224) >> 4);
    const int gB = big ? (b / 56) : 2 + ((b - 224) >> 4), cB = big ? (b % 56) : jA;
    u32x4 butl, kfl;
    const unsigned vbut = (lane & 1) * 64 + (lane >> 4) * 16;
    const char* sbut = ws + WS_BUT + (gB * 8 + wid) * 128;
    if (wid == 0) STAMP(ws, 2, 0);
    TXP(ws, 2);
    if (wid >= 4) {
        panel_wait(ws, FL_H, p, j, 1u, lane);
        g64_loader<16, QA_NS, QA_NS, NoEx, -1>(ws + WS_H + (size_t)p * 262144, ws + WS_WQ + (size_t)j * 131072, ldsb, wid - 4, lane, []<int I>() {});
        async_ld(butl, vbut, sbut);
        asm volatile("s_waitcnt vmcnt(0)" : "+v"(butl)::"memory");
        bar();
    } else {
        f32x16 r[2] = {};
        g64_compute<16, QA_NS>(lds, wid, lane, r, [&](int i) {
            if (i == KVP) wait_vm<0>();
            if (i == KVP + 1 && wid == 0) grid_post(ws, FL_KV, 1u, lane);
            if (i == KVL) async_ld(kfl, lane * 16, ws + WS_FLAG + FL_KV + (b & 7) * 1024);
            if (i == KVC) {
                asm volatile("s_waitcnt vmcnt(0)" : "+v"(kfl)::"memory");
                if (!grid_ok(kfl, 1u)) grid_wait(ws, FL_KV, 1u, lane);
                async_ld(butl, vbut, sbut);
            }
            if (i >= KVC && i < KVC + 16 / KVN) {
#pragma unroll
                for (int q = 0; q < KVN; ++q) kv_piece(0, QA_KV0, wid + 4 * (q & 1), ((i - KVC) * KVN + q) >> 1);
            }
        });
        if (wid == 0) STAMP(ws, 2, 1);
#pragma unroll
        for (int tb = 0; tb < 2; ++tb)
#pragma unroll
            for (int s = 0; s < 2; ++s) {
                u32x4 o;
#pragma unroll
                for (int i = 0; i < 4; ++i) o[i] = pk2(r[tb][8 * s + 2 * i] * QSCALE, r[tb][8 * s + 2 * i + 1] * QSCALE);
                *(LDSP u32x4*)(lds + 32768 + ((wid * 2 + tb) * 2 + s) * 1024 + lane * 16) = o;
            }
        asm volatile("s_waitcnt vmcnt(0)" : "+v"(butl)::"memory");
        lgkm_bar();
    }
    const int qg = wid & 3, kh = wid >> 2;
    const int lo = kh * 16384 + lane * 16;
    bf16x8 qA0, qA1, qB0, qB1, k0, k1, v0, v1;
    {
        const lptr qp = lds + 32768 + qg * 4096 + lane * 16;
        qA0 = lds_b128(qp); qA1 = lds_b128(qp + 1024); qB0 = lds_b128(qp + 2048); qB1 = lds_b128(qp + 3072);
        k0 = lds_b128(lds + QA_KV0 + lo); k1 = lds_b128(lds + QA_KV0 + lo + 1024);
    }
    bf16x8 sel;
    {
        const unsigned on = ((lane & 15) == ((lane >> 4) & 1)) ? 0x3f803f80u : 0u;
        sel = __builtin_bit_cast(bf16x8, (u32x4){on, on, on, on});
    }
    f32x16 oA = {}, oB = {}, sA, sB;
    f32x4 lA = {}, lB = {};
    const f32x16 zero = {};
    u32x4 pA0, pA1, pB0 = {0, 0, 0, 0}, pB1 = {0, 0, 0, 0};
    bf16x8 but = __builtin_bit_cast(bf16x8, (lane & 15) < 2 ? butl : (u32x4){0, 0, 0, 0});
    PIN(but);
    lgkm_bar();
    if (wid == 0) STAMP(ws, 2, 2);
    TXP(ws, 3);
#define EXP2(E, d, S, i) do { E[d] = __builtin_amdgcn_exp2f(S[i]); E[d + 1] = __builtin_amdgcn_exp2f(S[i + 1]); PIN(E); } while (0)
#define EXP4(E, S, i) do { E[0] = __builtin_amdgcn_exp2f(S[i]); E[1] = __builtin_amdgcn_exp2f(S[i + 1]); E[2] = __builtin_amdgcn_exp2f(S[i + 2]); E[3] = __builtin_amdgcn_exp2f(S[i + 3]); } while (0)
#define PK2(P, d, E) do { P[d] = pk2(E[0], E[1]); P[d + 1] = pk2(E[2], E[3]); PIN(P); } while (0)
#define BF(x) __builtin_bit_cast(bf16x8, x)
#define HALF(SX, PX0, PX1, SY, QY0, QY1, OZ, LZ, PZ0, PZ1, R0, R1, R2, R3) do { \
        SY = mfma32(k0, QY0, zero); SBAR(); PK2(PZ1, 2, e); R0; SBAR(); EXP4(e, SX, 0); PIN(e); SBAR(); \
        SY = mfma32(k1, QY1, SY); SBAR(); PK2(PX0, 0, e); R1; SBAR(); EXP2(e, 0, SX, 4); SBAR(); LZ = mfma16(sel, BF(PZ0), LZ); SBAR(); EXP2(e, 2, SX, 6); SBAR(); \
        OZ = mfma32(v0, BF(PZ0), OZ); SBAR(); PK2(PX0, 2, e); R2; SBAR(); EXP4(e, SX, 8); PIN(e); SBAR(); \
        OZ = mfma32(v1, BF(PZ1), OZ); SBAR(); PK2(PX1, 0, e); R3; SBAR(); EXP2(e, 0, SX, 12); SBAR(); LZ = mfma16(sel, BF(PZ1), LZ); SBAR(); EXP2(e, 2, SX, 14); SBAR(); } while (0)
    sA = mfma32(k0, qA0, zero); sA = mfma32(k1, qA1, sA);
    v0 = k0; v1 = k1;
    f32x4 e = {0, 0, 0, 0};
    bf16x8 w0, w1_, sf[4];
    f32x4 ut[4];
    u32x4 sfl;
    unsigned junk = 0;
    const lptr tp32 = stg + (4 * (lane >> 5) + ((lane & 15) >> 2)) * 128 + ((lane >> 4) & 1) * 32 + (lane & 3) * 8;
    const lptr tp16 = stg + (16 * (lane >> 5) + 4 * ((lane >> 4) & 1) + ((lane & 15) >> 2)) * 128 + (lane & 3) * 8;
    unsigned dstA = big ? WS_W2 + (jA >> 1) * 917504 + (gA * 8 + wid) * 8192 + (jA & 1) * 4096 : WS_WO + jA * 131072 + (gA * 8 + wid) * 4096, dstB[4];
#pragma unroll
    for (int i = 0; i < 4; ++i) {
        const int nbk = cB * 4 + i, jt = nbk / 14;
        dstB[i] = big ? WS_W1 + jt * 458752 + (gB * 8 + wid) * 14336 + (nbk - jt * 14) * 1024 : WS_WO + cB * 131072 + (gB * 8 + wid) * 4096 + i * 1024;
        asm volatile("" : "+s"(dstB[i]));
    }
    const int ldB = big ? F_ : D_;
    const u16* srcA = (big ? w2 : wo) + jA * 64 + (size_t)(gA * 256 + wid * 32) * D_;
    const u16* srcB = (big ? w1 : wo) + cB * 64 + (size_t)(gB * 256 + wid * 32) * ldB;
    asm volatile("" : "+s"(dstA), "+s"(srcA), "+s"(srcB));
    auto side = [&]<int C, int IT, int I>() {
        if constexpr (C < 3 && IT < 2 && !(AA & 2)) kv_piece(C + 1, (C & 1) ? QA_KV0 : 0, wid, IT * 4 + I);
        if constexpr (AA & 1) return;
        if constexpr ((C == 0 && IT == 2) || (C == 1 && IT == 4)) {
            const int ld = C == 0 ? D_ : ldB;
            const u16* s = (C == 0 ? srcA : srcB) + I * 8 * ld;
            const unsigned vo = (unsigned)((lane >> 3) * ld * 2 + (lane & 7) * 16), m = ldsb + 65536 + wid * 4096 + I * 1024;
            asm volatile(SETM0(m) GLDS(v, s, 0)::[m] "s"(m), [v] "v"(vo), [s] "s"(s) : "memory");
        }
        if constexpr ((C == 1 || C == 2) && IT == 2) {
            if constexpr (I == 0) wait_vm<8>();
            const lptr q = !big ? tp32 + (I >> 1) * 2048 + (I & 1) * 64 : (C == 2 ? tp16 + I * 32 : tp16 + (lane & 3) * 8 + (I >> 1) * 64 + (I & 1) * 8);
            sf[I] = cat(lds_tr(q), lds_tr(q + 1024));
        }
        if constexpr ((C == 1 || C == 2) && IT == 3) {
            const unsigned dst = C == 1 ? dstA + I * 1024 : dstB[I];
            if constexpr (C == 2) ut[I] = mfma16(sf[I], but, (f32x4){0, 0, 0, 0});
            st16(rws, lane * 16, dst, __builtin_bit_cast(u32x4, sf[I]));
        }
        if constexpr (C == 2 && IT == 4) *(LDSP f32x4*)(stg + I * 1024 + lane * 16) = ut[I];
        if constexpr (PREW && C == 3 && IT == 2 && I < 2)
            touch(junk, (I * 64 + lane < 112 ? I * 64 + lane : 0) * 64, ws + WS_G + p * (F_ / 8 * 2048) + j * 57344 + wid * 7168);
        if constexpr (C == 0 && IT == 3 && I < 2) st16(rws, lane * 16, WS_EX + blockIdx.x * 16384 + wid * 2048 + I * 1024, (u32x4){~0u, ~0u, ~0u, ~0u});
        if constexpr (C == 3 && IT == 0 && I == 0) { if (wid == 0) grid_post(ws, FL_SIDE, 1u, lane); }
        if constexpr (C == 3 && IT == 0) {
            if (wid == 0) {
                const lptr q = lds + 65536 + I * 8192 + (lane >> 4) * 1024 + ((lane >> 2) & 3) * 256 + (lane & 3) * 4;
                ut[I] = (f32x4){*(const LDSP float*)q, *(const LDSP float*)(q + 16), *(const LDSP float*)(q + 4096), *(const LDSP float*)(q + 4096 + 16)};
            }
        }
        if constexpr (C == 3 && IT == 1 && I == 1) {
            if (wid == 0 && big) {
                const f32x4 a = (ut[0] + ut[1]) + (ut[2] + ut[3]);
#pragma unroll
                for (int i = 0; i < 2; ++i) __builtin_amdgcn_raw_buffer_store_b32(__float_as_uint(a[i] + a[2 + i]), rws, WS_UTP + ((gB * 2 + i) * F_ + cB * 64 + lane) * 4, 0, 16);
            }
        }
        if constexpr (C == 3 && IT == 6 && I == 0) {
            if (wid == 0) { wait_vm<0>(); grid_post(ws, FL_SIDE, 2u, lane); }
            async_ld(sfl, lane * 16, ws + WS_FLAG + FL_SIDE + (blockIdx.x & 7) * 1024);
        }
    };
#define SIDE(I) side.template operator()<C, IT, I>()
    auto step = [&]<int C, int IT>() {
        const lptr kp = lds + ((C & 1) ? 0 : QA_KV0) + lo, kn = lds + ((C & 1) ? QA_KV0 : 0) + lo;
        if constexpr (PRIO && IT == 5) __builtin_amdgcn_s_setprio(2);
        if constexpr (PRIO && IT == 6) __builtin_amdgcn_s_setprio(1);
        if constexpr (IT < 7) {
            HALF(sA, pA0, pA1, sB, qB0, qB1, oB, lB, pB0, pB1, SIDE(0), k0 = lds_b128(kp + (IT + 1) * 2048),
                 k1 = lds_b128(kp + (IT + 1) * 2048 + 1024), v0 = lds_b128(kp + 32768 + IT * 2048));
            if constexpr (PRIO && IT == 6) __builtin_amdgcn_s_setprio(0);
            HALF(sB, pB0, pB1, sA, qA0, qA1, oA, lA, pA0, pA1, v1 = lds_b128(kp + 32768 + IT * 2048 + 1024),
                 SIDE(1); if constexpr (IT == 6) w0 = lds_b128(kp + 32768 + 7 * 2048), SIDE(2); if constexpr (IT == 6) w1_ = lds_b128(kp + 32768 + 7 * 2048 + 1024), SIDE(3));
        } else {
            if constexpr (C < 3 && !(AA & 4)) {
                wait_vm<(AA & 1) || C == 2 ? 0 : (C == 1 ? 8 : 6)>();
                lgkm_bar();
                if constexpr (PRIO) __builtin_amdgcn_s_setprio(3);
            }
            HALF(sA, pA0, pA1, sB, qB0, qB1, oB, lB, pB0, pB1, (void)0, if constexpr (C < 3) k0 = lds_b128(kn), if constexpr (C < 3) k1 = lds_b128(kn + 1024), (void)0);
            v0 = w0; v1 = w1_;
            HALF(sB, pB0, pB1, sA, qA0, qA1, oA, lA, pA0, pA1, (void)0, (void)0, (void)0, (void)0);
        }
    };
    if constexpr (PRIO) __builtin_amdgcn_s_setprio(3);
    [&]<int... N>(std::integer_sequence<int, N...>) { (step.template operator()<N / 8, N % 8>(), ...); }(std::make_integer_sequence<int, 32>{});
    PK2(pB1, 2, e);
    oB = mfma32(v0, BF(pB0), oB); oB = mfma32(v1, BF(pB1), oB);
    lB = mfma16(sel, BF(pB0), lB); lB = mfma16(sel, BF(pB1), lB);
    PIN(lA); PIN(lB); PIN(oA); PIN(oB);
    if (wid == 0) STAMP(ws, 2, 3);
    const lptr ex = lds + QA_KV0;
    {
        const f32x16& so = kh ? oA : oB;
        const f32x4& sl = kh ? lA : lB;
#pragma unroll
        for (int i = 0; i < 4; ++i) *(LDSP f32x4*)(ex + wid * 4096 + i * 1024 + lane * 16) = (f32x4){so[4 * i], so[4 * i + 1], so[4 * i + 2], so[4 * i + 3]};
        const f32x4& ml = kh ? lB : lA;
        if (lane < 16) {
            *(LDSP float*)(ex + 32768 + wid * 256 + lane * 4) = sl[0]; *(LDSP float*)(ex + 32768 + wid * 256 + 64 + lane * 4) = sl[1];
            *(LDSP float*)(ex + 32768 + wid * 256 + 128 + lane * 4) = ml[0]; *(LDSP float*)(ex + 32768 + wid * 256 + 192 + lane * 4) = ml[1];
        }
    }
    lgkm_bar();
    tcap<0>(TL);
    asm volatile("s_waitcnt vmcnt(0)" : "+v"(sfl), "+v"(junk)::"memory");
    if (wid >= 4) {
        if (!grid_ok(sfl, lane >= 56)) grid_wait(ws, FL_SIDE, lane >= 56, lane);
#pragma unroll
        for (int i = 0; i < OPRE; ++i) g64_w(ws + WS_WO + (size_t)j * 131072, ldsb, i, i, wid - 4, lane);
    }
    f32x16 o = kh ? oB : oA;
    float l;
    {
        l = *(const LDSP float*)(ex + 32768 + wid * 256 + 128 + (lane & 31) * 4) + *(const LDSP float*)(ex + 32768 + (wid ^ 4) * 256 + (lane & 31) * 4);
#pragma unroll
        for (int i = 0; i < 4; ++i) {
            const f32x4 v = *(const LDSP f32x4*)(ex + (wid ^ 4) * 4096 + i * 1024 + lane * 16);
#pragma unroll
            for (int e = 0; e < 4; ++e) o[4 * i + e] += v[e];
        }
    }
    float chk = l * 0.f;
#pragma unroll
    for (int i = 0; i < 16; ++i) chk = __builtin_fmaf(o[i], 0.f, chk);
    const bool bad = AA ? false : (!(chk == 0.f) || !(l >= 0x1p-90f));
    if (__builtin_expect(__any(bad), 0)) {
        const bf16x8 q0 = kh ? qB0 : qA0, q1 = kh ? qB1 : qA1;
        float m = -INFINITY; l = 0.f; o = zero;
        for (int kt = 0; kt < 64; ++kt) {
            const bf16x8 a0 = BF(ld16(rws, WS_KT + kt * 2048 + lane * 16)), a1 = BF(ld16(rws, WS_KT + kt * 2048 + 1024 + lane * 16));
            const bf16x8 b0 = BF(ld16(rws, WS_VT + kt * 2048 + lane * 16)), b1 = BF(ld16(rws, WS_VT + kt * 2048 + 1024 + lane * 16));
            f32x16 s = mfma32(a0, q0, zero); s = mfma32(a1, q1, s);
            float mx = s[0];
#pragma unroll
            for (int i = 1; i < 16; ++i) mx = fmaxf(mx, s[i]);
            mx = fmaxf(mx, __shfl_xor(mx, 32));
            const float mn = fmaxf(m, mx), al = __builtin_amdgcn_exp2f(m - mn);
            m = mn;
            u32x4 p0, p1; float rs = 0.f;
#pragma unroll
            for (int i = 0; i < 4; ++i) {
                p0[i] = pk2(__builtin_amdgcn_exp2f(s[2 * i] - m), __builtin_amdgcn_exp2f(s[2 * i + 1] - m));
                p1[i] = pk2(__builtin_amdgcn_exp2f(s[8 + 2 * i] - m), __builtin_amdgcn_exp2f(s[9 + 2 * i] - m));
                rs += bflo(p0[i]) + bfhi(p0[i]) + bflo(p1[i]) + bfhi(p1[i]);
            }
            rs += __shfl_xor(rs, 32);
            l = l * al + rs;
#pragma unroll
            for (int i = 0; i < 16; ++i) o[i] *= al;
            o = mfma32(b0, BF(p0), o); o = mfma32(b1, BF(p1), o);
        }
    }
    {
        const float inv = __builtin_amdgcn_rcpf(l);
        const unsigned oAT = WS_O + p * 262144 + (2 * j + (qg >> 1)) * 8192 + ((qg & 1) * 2 + kh) * 512, vo = (lane >> 5) * 2048 + (lane & 31) * 16;
#pragma unroll
        for (int s = 0; s < 2; ++s) {
            u32x4 w;
#pragma unroll
            for (int i = 0; i < 4; ++i) w[i] = pk2(o[8 * s + 2 * i] * inv, o[8 * s + 2 * i + 1] * inv);
            st16(rws, vo, oAT + s * 4096, w);
        }
    }
    wait_vm<0>();
    lgkm_bar();
    if (wid == 0) panel_post(ws, FL_O, p, j, 1u, lane);
    if (wid == 0) STAMP(ws, 2, 4);
}

constexpr int F1_NS = 7, F1_STAGE = 22528, F1_NST = 32, F1_AUX = F1_NS * F1_STAGE;
template <int NW> DI void f1_w(const char* sw, unsigned ldsb, int stage, int pc0, int lane) {
    const unsigned vw = pc0 * 1024 + lane * 16, mw = ldsb + (stage % F1_NS) * F1_STAGE + 8192 + pc0 * 1024;
    const char* s = sw + stage * 14336;
    if constexpr (ABL == 2) {} else if constexpr (NW == 4)
        asm volatile(SETM0(mw) GLDW(vw, s, 0) GLDW(vw, s, 1024) GLDW(vw, s, 2048) GLDW(vw, s, 3072)::[mw] "s"(mw), [vw] "v"(vw), [s] "s"(s) : "memory");
    else
        asm volatile(SETM0(mw) GLDW(vw, s, 0) GLDW(vw, s, 1024) GLDW(vw, s, 2048)::[mw] "s"(mw), [vw] "v"(vw), [s] "s"(s) : "memory");
}
template <int NW> struct OpEx { static constexpr int n(int i) { return i < 11 ? 0 : i == 12 ? 2 * NW : NW; } };
template <int NW>
DI void op_loader(char* ws, lptr lds, int lane, int lw, int p, int j, TS& TL) {
    const unsigned ldsb = (unsigned)(uintptr_t)lds;
    const char* sw = ws + WS_WO + (size_t)j * 131072;
    const char* s1 = ws + WS_W1 + (size_t)j * 458752;
    const int pc0 = lw < 2 ? lw * 4 : 8 + (lw - 2) * 3;
    tcap<1>(TL);
#pragma unroll
    for (int i = OPRE; i < OP_NS; ++i) g64_w(sw, ldsb, i, i, lw, lane);
    panel_wait(ws, FL_O, p, j, 1u, lane);
    tcap<2>(TL);
    u32x4 fl;
    g64_loader<16, OP_NS, OP_NS, OpEx<NW>, 8>(ws + WS_O + (size_t)p * 262144, sw, ldsb, lw, lane, [&]<int I>() {
        if constexpr (I == 8) async_ld(fl, lane * 16, ws + WS_FLAG + FL_SIDE + (blockIdx.x & 7) * 1024);
        if constexpr (I == 10) { pin(fl); if (!grid_ok(fl, 2u)) grid_wait(ws, FL_SIDE, 2u, lane); }
        if constexpr (I == 0) { tcap<3>(TL); }
        if constexpr (I == 15) { tcap<4>(TL); }
        if constexpr (I >= 11) f1_w<NW>(s1, ldsb, I < 13 ? I - 7 : I - 13, pc0, lane);
        if constexpr (I == 12) f1_w<NW>(s1, ldsb, 6, pc0, lane);
    });
}
DI void op_compute(const u16* __restrict__ x, char* __restrict__ ws, lptr lds, int lane, int wid, int p, int j) {
    if (wid == 0) STAMP(ws, 3, 0);
    TXP(ws, 4);
    const int th = wid & 1, nb = wid >> 1, hi = lane >> 5;
    const int n0 = j * 64 + nb * 32 + 4 * hi;
    u32x4 xq[2][2]; f32x4 ga[4], a2[4];
    f32x16 r[2] = {};
    if (wid == 0) STAMP(ws, 3, 1);
    g64_compute<16, OP_NS>(lds, wid, lane, r, [&](int i) {
        if (i == -1) {
#pragma unroll
            for (int tb = 0; tb < 2; ++tb)
#pragma unroll
                for (int h = 0; h < 2; ++h) xq[tb][h] = *(const u32x4*)(x + (size_t)(p * 128 + th * 64 + tb * 32 + (lane & 31)) * D_ + j * 64 + nb * 32 + h * 16 + hi * 8);
#pragma unroll
            for (int q = 0; q < 4; ++q) {
                ga[q] = ld16f(mk_rsrc(ws), WS_MOD + (2048 + n0 + 8 * q) * 4);
                a2[q] = ld16f(mk_rsrc(ws), WS_MOD + (4096 + n0 + 8 * q) * 4);
            }
            SBAR();
        }
    });
    if (wid == 0) STAMP(ws, 3, 2);
    u32x4 xk[2][2];
#pragma unroll
    for (int tb = 0; tb < 2; ++tb) {
        u32x4 (&xo)[2] = xk[tb]; u32x4 yo[2]; float v[16]; float s = 0.f;
        u32x2 xr[2][4];
#pragma unroll
        for (int h = 0; h < 2; ++h) {
            const auto s0 = __builtin_amdgcn_permlane32_swap(xq[tb][h][0], xq[tb][h][2], false, false);
            const auto s1 = __builtin_amdgcn_permlane32_swap(xq[tb][h][1], xq[tb][h][3], false, false);
            xr[tb][2 * h] = (u32x2){s0[0], s1[0]}; xr[tb][2 * h + 1] = (u32x2){s0[1], s1[1]};
        }
#pragma unroll
        for (int i = 0; i < 4; ++i) {
            const float x0 = bflo(xr[tb][i][0]), x1 = bfhi(xr[tb][i][0]), x2 = bflo(xr[tb][i][1]), x3 = bfhi(xr[tb][i][1]);
            const unsigned a = pk2(x0 + ga[i][0] * r[tb][4 * i], x1 + ga[i][1] * r[tb][4 * i + 1]);
            const unsigned b = pk2(x2 + ga[i][2] * r[tb][4 * i + 2], x3 + ga[i][3] * r[tb][4 * i + 3]);
            xo[i >> 1][(i & 1) * 2] = a; xo[i >> 1][(i & 1) * 2 + 1] = b;
            v[4 * i] = bflo(a); v[4 * i + 1] = bfhi(a); v[4 * i + 2] = bflo(b); v[4 * i + 3] = bfhi(b);
            yo[i >> 1][(i & 1) * 2] = pk2(v[4 * i] * a2[i][0], v[4 * i + 1] * a2[i][1]);
            yo[i >> 1][(i & 1) * 2 + 1] = pk2(v[4 * i + 2] * a2[i][2], v[4 * i + 3] * a2[i][3]);
            s += (v[4 * i] + v[4 * i + 1]) + (v[4 * i + 2] + v[4 * i + 3]);
        }
        const unsigned off = p * 262144 + (j * 2 + nb) * 8192 + (th * 64 + tb * 32) * 16, vo = hi * 2048 + (lane & 31) * 16;
#pragma unroll
        for (int h = 0; h < 2; ++h) {
            st16(mk_rsrc(ws), vo, WS_Y + off + h * 4096, yo[h]);
        }
        const float mean = swap32_add(s) * (1.f / 32.f);
        float q = 0.f;
#pragma unroll
        for (int i = 0; i < 16; ++i) { const float d = v[i] - mean; q += d * d; }
        q = swap32_add(q);
        if (hi == 0) __builtin_amdgcn_raw_buffer_store_b64((u32x2){__float_as_uint(mean), __float_as_uint(q)}, mk_rsrc(ws),
                                                           WS_ST + ((j * 2 + nb) * S_ + p * 128 + th * 64 + tb * 32 + lane) * 8, 0, 16);
    }
    wait_vm<0>();
    bar();
    if (wid == 0) panel_post(ws, FL_Y, p, j, 1u, lane);
#pragma unroll
    for (int tb = 0; tb < 2; ++tb)
#pragma unroll
        for (int h = 0; h < 2; ++h) {
            const auto s0 = __builtin_amdgcn_permlane32_swap(xk[tb][h][0], xk[tb][h][2], false, false);
            const auto s1 = __builtin_amdgcn_permlane32_swap(xk[tb][h][1], xk[tb][h][3], false, false);
            st16(mk_rsrc(ws), hi * 2048 + (lane & 31) * 16, WS_X2 + p * 262144 + (j * 2 + nb) * 8192 + (th * 64 + tb * 32) * 16 + h * 4096, (u32x4){s0[0], s1[0], s0[1], s1[1]});
        }
    if (wid == 0) STAMP(ws, 3, 3);
}

#define F2_D 6
#define F2_XP 1
#define F2_MP 4
constexpr int F2_NS = 10, F2_STAGE = 16384, F2_MAIN = 56 - F2_D, F2_NST = 56 + F2_D;
#define F2_EB 4
#define F2_PS 6
#define F2_PA 8
constexpr int F2_LATE = 7 + 7 * F2_EB;
static_assert(F2_LATE <= F2_MAIN, "");
DI unsigned f2_off(int s) {
    const int o = s >= 56, i = o ? s - F2_D : s;
    const int r = i < 7 ? 0 : i < F2_LATE ? 1 + (i - 7) / F2_EB : 1 + (i - F2_LATE) / (7 - F2_EB);
    const int st = i < 7 ? i : i < F2_LATE ? (i - 7) % F2_EB : F2_EB + (i - F2_LATE) % (7 - F2_EB);
    const unsigned b = blockIdx.x, jl = (b >> 3) & 7, q = r ? r - 1 + (r - 1 >= jl) : jl;
    return ((((b & 1) ^ o) * 8 + q) * 7 + st) * 8192;
}
DI void f2_w(char* ws, unsigned ldsb, int i, int lw, int lane) {
    const int b = blockIdx.x;
    const unsigned mw = ldsb + i * F2_STAGE + 8192 + lw * 2048, va = lw * 2048 + lane * 16;
    const char* s = ws + WS_W2 + (size_t)((b >> 3) & 7) * 917504 + f2_off(i);
    if (ABL != 2) asm volatile(SETM0(mw) GLDW(va, s, 0) GLDW(va, s, 1024)::[mw] "s"(mw), [va] "v"(va), [s] "s"(s) : "memory");
}
struct F1Ex { static constexpr int n(int m) { return !PROG || m < 13 ? 0 : m == 14 ? 4 : 6; } };
template <int NW> struct F1Cnt { static constexpr int n(int s) { return (s < F1_NS ? 2 : 2 + NW) + (s == 2 ? 16 : 0); } };
template <int NW>
DI void f1_loader(char* ws, int p, int j, lptr lds, int lw, int lane, TS& TL) {
    const char* sa = ws + WS_Y + (size_t)p * 262144;
    const char* sw = ws + WS_W1 + (size_t)j * 458752;
    const unsigned ldsb = (unsigned)(uintptr_t)lds;
    const int pc0 = lw < 2 ? lw * 4 : 8 + (lw - 2) * 3;
    const unsigned va = lw * 2048 + lane * 16;
#pragma unroll
    for (int i = 0; i < F1_NS; ++i) if (i != 3 && !PROG) f1_w<NW>(sw, ldsb, i, pc0, lane);
    if (NW == 3) {
        if (lane < 56) {
            const unsigned o = WS_UTP + ((lw - 2) * F_ + j * 224 + lane * 4) * 4;
            const rsrc_t r = mk_rsrc(ws);
            const f32x4 u = (ld16f(r, o) + ld16f(r, o + 2 * F_ * 4)) + (ld16f(r, o + 4 * F_ * 4) + ld16f(r, o + 6 * F_ * 4));
            *(LDSP f32x4*)(lds + F1_AUX + (lw - 2) * 1024 + lane * 16) = u * (lw == 2 ? 1.f : -1.4426950408889634f);
        }
    }
    bar();
    tcap<5>(TL);
    f1_w<NW>(sw, ldsb, 3, pc0, lane);
    panel_wait(ws, FL_Y, p, j, 1u, lane);
    tcap<6>(TL);
    f32x2 sp[16];
    unsigned fl;
    pair_loader<F1_NST, F1_NS, F1Cnt<NW>, F1Ex, 8>([&]<int S>() {
        const unsigned ma = ldsb + (S % F1_NS) * F1_STAGE + lw * 2048;
        if constexpr (ABL != 2) asm volatile(SETM0(ma) GLDA(va, sa, 0) GLDA(va, sa, 1024)::[ma] "s"(ma), [va] "v"(va), [sa] "s"(sa) : "memory");
        if constexpr (S >= F1_NS) f1_w<NW>(sw, ldsb, S, pc0, lane);
        sa += 8192;
        if constexpr (S == 2) {
            const char* s = ws + WS_ST + (size_t)(p * 128 + lw * 32) * 8;
            const unsigned v0 = (lane >> 5) * 16 * (S_ * 8) + (lane & 31) * 8;
#pragma unroll
            for (int i = 0; i < 16; ++i) async_ld(sp[i], v0 + i * (S_ * 8), s);
        }
    }, [&]<int M, int PH>() {
        if constexpr (M == 0 && PH == 0) {
#pragma unroll
            for (int i = 0; i < 16; ++i) PIN(sp[i]);
        }
        if constexpr (PROG && M == 13 && PH == 1) { f2_w(ws, ldsb, 5, lw, lane); f2_w(ws, ldsb, 6, lw, lane); f2_w(ws, ldsb, 7, lw, lane); }
        if constexpr (PROG && M == 14 && PH == 1) { f2_w(ws, ldsb, 0, lw, lane); f2_w(ws, ldsb, 8, lw, lane); }
        if constexpr (PROG && M == 15 && PH == 1) { f2_w(ws, ldsb, 1, lw, lane); f2_w(ws, ldsb, 2, lw, lane); f2_w(ws, ldsb, 3, lw, lane); }
        if constexpr (M == -1 && PH == 1) { tcap<7>(TL); }
        if constexpr (M == 15 && PH == 1) { tcap<8>(TL); }
        if constexpr (M == 8 && PH == 1) async_ld(fl, (lane & 15) * 4, ws + WS_FLAG + FL_Y + (p * 16 + j) * 128);
        if constexpr (M == 10 && PH == 1) { pin(fl); if (!__all(fl >= 2u)) panel_wait(ws, FL_Y, p, j, 2u, lane); }
        if constexpr (M == 2 && PH == 1) {
            float s = 0.f;
#pragma unroll
            for (int i = 0; i < 16; ++i) s += sp[i][0];
            const float mu = swap32_add(s) * (1.f / 32.f);
            float q = 0.f;
#pragma unroll
            for (int i = 0; i < 16; ++i) { const float d = sp[i][0] - mu; q += sp[i][1] + 32.f * d * d; }
            const float rl = -1.4426950408889634f * __builtin_amdgcn_rsqf(swap32_add(q) * (1.f / 1024.f) + 1e-5f);
            if (lane < 32) *(LDSP f32x2*)(lds + F1_AUX + 2048 + (lw * 32 + lane) * 8) = (f32x2){mu, rl};
            asm volatile("s_waitcnt lgkmcnt(0)" ::: "memory");
        }
    });
}
struct F1Frag { bf16x8 a[4], w[7]; };
DI void f1_compute(char* __restrict__ ws, lptr lds, int lane, int wid, int p, int j) {
    if (wid == 0) STAMP(ws, 4, 0);
    TXP(ws, 5);
    const int th = wid & 1, nh = wid >> 1, g = lane >> 4;
    f32x4 acc[7][4] = {};
    const LP pa = mk_lp(lds + g * 2048 + (th * 64 + (lane & 15)) * 16), pw = mk_lp(lds + nh * 1024 + lane * 16);
    F1Frag fr[2];
    auto rd = [&]<int S, int I>() {
        constexpr int so = (S % F1_NS) * F1_STAGE;
        if constexpr (S < F1_NST && I < 11) {
            if constexpr (I >= 1 && I <= 4) fr[S & 1].a[I - 1] = lds_at<so + (I - 1) * 256>(pa);
            else fr[S & 1].w[I ? I - 4 : 0] = lds_at<so + 8192 + (I ? I - 4 : 0) * 2048>(pw);
        }
    };
    auto step = [&]<int S>() {
        if constexpr (S == 6) wait_vm<0>();
        if constexpr (S % 2 == 0) { lgkm_bar(); SBAR(); }
        if constexpr (S == 6) { if (wid == 0) panel_post(ws, FL_Y, p, j, 2u, lane); SBAR(); }
        if constexpr (ABL != 1)
            [&]<int... U>(std::integer_sequence<int, U...>) {
                ((acc[U / 4][U % 4] = mfma16(fr[S & 1].w[U / 4], fr[S & 1].a[U % 4], acc[U / 4][U % 4]), SBAR(), rd.template operator()<S + 1, U>(), SBAR()), ...);
            }(std::make_integer_sequence<int, 28>{});
    };
    if (wid == 0) STAMP(ws, 4, 1);
    bar();
    if (wid == 0) STAMP(ws, 4, 2);
    [&]<int... I>(std::integer_sequence<int, I...>) { (rd.template operator()<0, I>(), ...); }(std::make_integer_sequence<int, 11>{});
    [&]<int... S>(std::integer_sequence<int, S...>) { (step.template operator()<S>(), ...); }(std::make_integer_sequence<int, F1_NST>{});
    if (wid == 0) STAMP(ws, 4, 3);
    f32x2 st[4];
#pragma unroll
    for (int tb = 0; tb < 4; ++tb) st[tb] = *(const LDSP f32x2*)(lds + F1_AUX + 2048 + (th * 64 + tb * 16 + (lane & 15)) * 8);
    const lptr pu = lds + F1_AUX + (nh * 16 + 4 * g) * 4;
    const unsigned gp = WS_G + p * (F_ / 8 * 2048) + j * 57344, vo = nh * 4096 + th * 1024 + (g & 1) * 2048 + ((lane >> 5) * 16 + (lane & 15)) * 16;
    const LP pg = mk_lp(lds + vo);
    f32x4 u = *(const LDSP f32x4*)pu, tt = *(const LDSP f32x4*)(pu + 1024);
    const f32x2 cl = {-1.4426950408889634f, -1.4426950408889634f};
#pragma unroll
    for (int b = 0; b < 7; ++b) {
        f32x4 un, tn;
        if (b < 6) { un = *(const LDSP f32x4*)(pu + (b + 1) * 128); tn = *(const LDSP f32x4*)(pu + 1024 + (b + 1) * 128); }
        unsigned w[4][2];
#pragma unroll
        for (int tb = 0; tb < 4; ++tb)
#pragma unroll
            for (int e = 0; e < 2; ++e) {
                const f32x2 a = __builtin_elementwise_fma((f32x2){u[2 * e], u[2 * e + 1]}, (f32x2){-st[tb][0], -st[tb][0]}, (f32x2){acc[b][tb][2 * e], acc[b][tb][2 * e + 1]});
                const f32x2 z = __builtin_elementwise_fma(a, (f32x2){st[tb][1], st[tb][1]}, (f32x2){tt[2 * e], tt[2 * e + 1]});
                const f32x2 dn = __builtin_elementwise_fma((f32x2){__builtin_amdgcn_exp2f(z[0]), __builtin_amdgcn_exp2f(z[1])}, cl, cl);
                const f32x2 o = z * (f32x2){__builtin_amdgcn_rcpf(dn[0]), __builtin_amdgcn_rcpf(dn[1])};
                w[tb][e] = pk2(o[0], o[1]);
            }
#pragma unroll
        for (int tp = 0; tp < 2; ++tp) {
            const auto s0 = __builtin_amdgcn_permlane32_swap(w[2 * tp][0], w[2 * tp + 1][0], false, false);
            const auto s1 = __builtin_amdgcn_permlane32_swap(w[2 * tp][1], w[2 * tp + 1][1], false, false);
            const u32x4 o = {s0[0], s1[0], s0[1], s1[1]};
            st16(mk_rsrc(ws), vo + tp * 512, gp + b * 8192, o);
            *(LDSP u32x4*)(pg.b[(b * F2_STAGE) >> 16] + ((b * F2_STAGE) & 65535) + tp * 512) = o;
        }
        u = un; tt = tn;
        if (b == 1) bar();
        SBAR();
    }
    wait_vm<2 * (7 - F2_EB)>();
    lgkm_bar();
    if (wid == 0) panel_post(ws, FL_G, p, j, 1u, lane);
    if (wid == 0) STAMP(ws, 4, 4);
}

struct F2Frag { bf16x8 a[4], w[4]; };
constexpr int f2_issued(int m) { return m <= 2 ? F2_NS : cmin(2 * m - 1 + F2_NS, F2_NST); }
constexpr int f2_cnt(int s) { return s < 7 ? 0 : s < F2_NS ? 2 : s < F2_MAIN ? 4 : 3; }
DI void f2_loader(char* ws, lptr lds, int lane, int lw, int p, int j, TS& TL) {
    const int b = blockIdx.x, kh = b & 1, jn = (b >> 3) & 7;
    const unsigned ldsb = (unsigned)(uintptr_t)lds;
    const char* ga = ws + WS_G + (size_t)p * (F_ / 8 * 2048);
    const char* gw = ws + WS_W2 + (size_t)jn * 917504;
    const unsigned va0 = lw * 2048 + lane * 16, vk0 = ((lw >> 1) * 4 + kh * 2 + (lw & 1)) * 1024 + lane * 16;
    bar();
    f2_w(ws, ldsb, 4, lw, lane);
    wait_vm<0>();
    bar();
    tcap<9>(TL);
    f2_w(ws, ldsb, 9, lw, lane);
    panel_wait(ws, FL_G, p, j, ((lane >> 3) & 1) == kh, lane);
    tcap<10>(TL);
    auto issue = [&]<int S>() {
        const unsigned ma = ldsb + (S % F2_NS) * F2_STAGE + lw * 2048, off = f2_off(S), va = va0, vk = vk0;
        const char* sa = ga + off; const char* sw = gw + off;
        if constexpr (ABL == 2) {} else if constexpr (S < F2_NS) {
            asm volatile(SETM0(ma) GLDA(va, sa, 0) GLDA(va, sa, 1024)::[ma] "s"(ma), [va] "v"(va), [sa] "s"(sa) : "memory");
        } else if constexpr (S < F2_MAIN) {
            const unsigned mw = ma + 8192;
            asm volatile(SETM0(ma) GLDA(va, sa, 0) GLDA(va, sa, 1024) SETM0(mw) GLDW(va, sw, 0) GLDW(va, sw, 1024)
                         ::[ma] "s"(ma), [mw] "s"(mw), [va] "v"(va), [sa] "s"(sa), [sw] "s"(sw) : "memory");
        } else {
            const unsigned mw = ldsb + (S % F2_NS) * F2_STAGE + 8192 + lw * 1024;
            asm volatile(SETM0(ma) GLDA(va, sa, 0) GLDA(va, sa, 1024) SETM0(mw) GLDW(vk, sw, 0)
                         ::[ma] "s"(ma), [mw] "s"(mw), [va] "v"(va), [vk] "v"(vk), [sa] "s"(sa), [sw] "s"(sw) : "memory");
        }
    };
    auto range = [&]<int S0, int... I>(std::integer_sequence<int, I...>) { (issue.template operator()<S0 + I>(), ...); };
    range.template operator()<7>(std::make_integer_sequence<int, F2_NS - 7>{});
    unsigned fl;
    static_assert(f2_issued(F2_PA + 2) <= F2_LATE, "");
    auto sync = [&]<int M>() {
        constexpr int need = cmin(2 * M + 2, F2_NST - 1), have = f2_issued(M);
        constexpr int from = M == F2_PA + 2 ? f2_issued(F2_PA + 1) : need + 1;
        constexpr int out = [] { int c = M == F2_PA + 1 ? 1 : 0; for (int s = from; s < have; ++s) c += f2_cnt(s); return c; }();
        static_assert(out < 64 && need < have, "");
        wait_vm<out>();
        bar();
        if constexpr (M == F2_PA + 2) {
            pin(fl);
            if (!__all(fl >= 2u)) panel_wait(ws, FL_G, p, j, 2u, lane);
        }
        range.template operator()<have>(std::make_integer_sequence<int, f2_issued(M + 1) - have>{});
        if constexpr (M == F2_PA) async_ld(fl, (lane & 15) * 4, ws + WS_FLAG + FL_G + (p * 16 + j) * 128);
        if constexpr (M == 2) tcap<11>(TL);
        if constexpr (M == F2_PA + 2) tcap<12>(TL);
    };
    [&]<int... I>(std::integer_sequence<int, I...>) { (sync.template operator()<I + 2>(), ...); }(std::make_integer_sequence<int, F2_NST / 2 - 2>{});
}
DI void f2_compute(char* __restrict__ ws, u16* __restrict__ out, lptr lds, int lane, int wid, int p, int j) {
    const int b = blockIdx.x, kh = b & 1, jn = (b >> 3) & 7;
    if (wid == 0) STAMP(ws, 5, 0);
    TXP(ws, 6);
    const int th = wid & 1, nh = wid >> 1, g = lane >> 4;
    f32x4 acc[4][4] = {};
    const LP pa = mk_lp(lds + g * 2048 + (th * 64 + (lane & 15)) * 16);
    const LP pk = mk_lp(lds + (nh * 4 + kh * 2) * 1024 + lane * 16), ps = mk_lp(lds + (nh * 4 + (kh ^ 1) * 2) * 1024 + lane * 16);
    F2Frag fr[2];
    LP pt;
    auto rd = [&]<int S, int I>() {
        constexpr int so = (S % F2_NS) * F2_STAGE;
        if constexpr (S < F2_NST && I < (S < F2_MAIN ? 8 : 6)) {
            if constexpr (I >= 1 && I <= 4) fr[S & 1].a[I - 1] = lds_at<so + (I - 1) * 256>(pa);
            else if constexpr (I >= 6) fr[S & 1].w[I - 4] = lds_at<so + 8192 + (I - 6) * 1024>(ps);
            else fr[S & 1].w[I ? 1 : 0] = lds_at<so + 8192 + (I ? 1024 : 0)>(S < F2_MAIN ? pk : pt);
        }
    };
    auto step = [&]<int S>() {
        if constexpr (ABL != 1)
            [&]<int... U>(std::integer_sequence<int, U...>) {
                ((acc[U / 4][U % 4] = mfma16(fr[S & 1].w[U / 4], fr[S & 1].a[U % 4], acc[U / 4][U % 4]), SBAR(), rd.template operator()<S + 1, U>(), SBAR()), ...);
            }(std::make_integer_sequence<int, S < F2_MAIN ? 16 : 8>{});
    };
    if (wid == 0) STAMP(ws, 5, 1);
    [&]<int... I>(std::integer_sequence<int, I...>) { (rd.template operator()<0, I>(), ...); }(std::make_integer_sequence<int, 8>{});
    static_assert(F2_MAIN % 2 == 0 && F2_D >= 4, "");
    const rsrc_t rws = mk_rsrc(ws);
    u32x4 xr[4]; f32x4 gb[2];
    const int c8 = jn * 16 + nh * 8 + kh * 4 + g;
    auto pair = [&]<int M>() {
        if constexpr (2 * M == F2_PS) wait_vm<0>();
        if constexpr (M >= 2) lgkm_bar();
        if constexpr (2 * M == F2_PS) { if (wid == 0) panel_post(ws, FL_G, p, j, 2u, lane); }
        if constexpr (M == F2_XP) {
            asm volatile("" ::: "memory");
#pragma unroll
            for (int tb = 0; tb < 4; ++tb) xr[tb] = ld16(rws, WS_X2 + p * 262144 + c8 * 2048 + (th * 64 + tb * 16 + (lane & 15)) * 16);
            gb[0] = ld16f(rws, WS_MOD + (5120 + c8 * 8) * 4); gb[1] = ld16f(rws, WS_MOD + (5120 + c8 * 8 + 4) * 4);
            SBAR();
        }
        SBAR();
        step.template operator()<2 * M>(); step.template operator()<2 * M + 1>();
    };
    [&]<int... M>(std::integer_sequence<int, M...>) { (pair.template operator()<M>(), ...); }(std::make_integer_sequence<int, F2_MAIN / 2 - 1>{});
    pt = mk_lp(lds + nh * 2048 + lane * 16);
    lgkm_bar(); SBAR();
    step.template operator()<F2_MAIN - 2>(); step.template operator()<F2_MAIN - 1>();
    if (wid == 0) STAMP(ws, 5, 2);
    const unsigned exo = WS_EX + wid * 4096 + lane * 16;
#pragma unroll
    for (int i = 0; i < 4; ++i) {
        u32x4 o = {pk2(acc[2][i][0], acc[2][i][1]), pk2(acc[2][i][2], acc[2][i][3]), pk2(acc[3][i][0], acc[3][i][1]), pk2(acc[3][i][2], acc[3][i][3])};
#pragma unroll
        for (int e = 0; e < 4; ++e) o[e] = o[e] < 0xfffffffeu ? o[e] : 0xfffffffeu;
        st16<16>(rws, exo + i * 1024, b * 16384, o);
    }
    SBAR();
    u32x4 pr[4];
    TS TT; TCAP(TT, 0);
    auto tail = [&]<int M>() {
        lgkm_bar(); SBAR();
        if constexpr (M == F2_MP) {
#pragma unroll
            for (int i = 0; i < 4; ++i) pr[i] = ld16(rws, exo + i * 1024 + (b ^ 1) * 16384);
            SBAR();
        }
        step.template operator()<F2_MAIN + 2 * M>(); step.template operator()<F2_MAIN + 2 * M + 1>();
    };
    [&]<int... I>(std::integer_sequence<int, I...>) { (tail.template operator()<I>(), ...); }(std::make_integer_sequence<int, F2_D>{});
    for (int spin = 0; spin < (1 << 20); ++spin) {
        unsigned m = 0;
#pragma unroll
        for (int i = 0; i < 16; ++i) m = m > pr[i >> 2][i & 3] ? m : pr[i >> 2][i & 3];
        if (!__any(m == 0xffffffffu)) break;
        asm volatile("" ::: "memory");
#pragma unroll
        for (int i = 0; i < 4; ++i) pr[i] = ld16(rws, exo + i * 1024 + (b ^ 1) * 16384);
    }
    if (wid == 0) STAMP(ws, 5, 3);
#pragma unroll
    for (int tb = 0; tb < 4; ++tb) {
        const f32x4 lo = acc[0][tb] + (f32x4){bflo(pr[tb][0]), bfhi(pr[tb][0]), bflo(pr[tb][1]), bfhi(pr[tb][1])};
        const f32x4 hi = acc[1][tb] + (f32x4){bflo(pr[tb][2]), bfhi(pr[tb][2]), bflo(pr[tb][3]), bfhi(pr[tb][3])};
        const u32x4 xv = xr[tb];
        const u32x4 o = {pk2(bflo(xv[0]) + gb[0][0] * lo[0], bfhi(xv[0]) + gb[0][1] * lo[1]), pk2(bflo(xv[1]) + gb[0][2] * lo[2], bfhi(xv[1]) + gb[0][3] * lo[3]),
                         pk2(bflo(xv[2]) + gb[1][0] * hi[0], bfhi(xv[2]) + gb[1][1] * hi[1]), pk2(bflo(xv[3]) + gb[1][2] * hi[2], bfhi(xv[3]) + gb[1][3] * hi[3])};
        st16(mk_rsrc(out), (lane & 15) * (D_ * 2) + c8 * 16, (p * 128 + th * 64 + tb * 16) * (D_ * 2), o);
    }
    if (wid == 0) STAMP(ws, 5, 4);
    TXP(ws, 7);
    if (wid == 0) TDUMP(TT, ws, 7, F2_D + 1);
}

constexpr int MAIN_LDS = 163840;
__global__ void __launch_bounds__(512) k_main(const u16* __restrict__ x, const u16* __restrict__ wk, const u16* __restrict__ wv, const u16* __restrict__ wo, const u16* __restrict__ w1, const u16* __restrict__ w2,
                                              char* __restrict__ ws, u16* __restrict__ out) {
    extern __shared__ __attribute__((aligned(16))) char smem[];
    const lptr lds = (lptr)smem;
    const int lane = threadIdx.x & 63, wid = __builtin_amdgcn_readfirstlane(threadIdx.x >> 6);
    int p, j; tile_map(blockIdx.x, p, j);
    TS TL;
    ph_ln_kv(x, wk, wv, ws, lds, lane, wid, p, j);
    ph_qattn(wo, w1, w2, ws, lds, lane, wid, p, j, TL);
    if (wid >= 4) {
        if (wid < 6) { op_loader<4>(ws, lds, lane, wid - 4, p, j, TL); f1_loader<4>(ws, p, j, lds, wid - 4, lane, TL); }
        else { op_loader<3>(ws, lds, lane, wid - 4, p, j, TL); f1_loader<3>(ws, p, j, lds, wid - 4, lane, TL); }
        f2_loader(ws, lds, lane, wid - 4, p, j, TL);
        if (wid == 4) TDUMP(TL, ws, 6, 13);
    } else {
        op_compute(x, ws, lds, lane, wid, p, j);
        f1_compute(ws, lds, lane, wid, p, j);
        f2_compute(ws, out, lds, lane, wid, p, j);
    }
}

extern "C" void kernel_launch(void* const* d_in, const int* in_sizes, int n_in, void* d_out, int out_size, void* d_ws, size_t ws_size,
                              hipStream_t stream) {
    const u16* x = (const u16*)d_in[0];
    const u16* wq = (const u16*)d_in[1];
    const u16* wk = (const u16*)d_in[2];
    const u16* wv = (const u16*)d_in[3];
    const u16* wo = (const u16*)d_in[4];
    const u16* w1 = (const u16*)d_in[6];
    const u16* w2 = (const u16*)d_in[7];
    const u16* c = (const u16*)d_in[8];
    const u16* wmod = (const u16*)d_in[9];
    char* ws = (char*)d_ws + ((ws_size - (64u << 20)) & ~(size_t)0x1fffff);
    (void)hipFuncSetAttribute((const void*)k_main, hipFuncAttributeMaxDynamicSharedMemorySize, MAIN_LDS);
    k_prep<<<256, 512, 32768, stream>>>(c, wmod, wq, ws);
    k_main<<<256, 512, MAIN_LDS, stream>>>(x, wk, wv, wo, w1, w2, ws, (u16*)d_out);
}
```

```cpp
#pragma clang diagnostic ignored "-Wc++20-extensions"
#include <hip/hip_runtime.h>
#include <hip/hip_bf16.h>
#include <stdint.h>

typedef unsigned short u16;
typedef __bf16 bf16x8 __attribute__((ext_vector_type(8)));
typedef __bf16 bf16x2 __attribute__((ext_vector_type(2)));
typedef short s16x4 __attribute__((ext_vector_type(4)));
typedef short s16x8 __attribute__((ext_vector_type(8)));
typedef float f32x16 __attribute__((ext_vector_type(16)));
typedef float f32x4 __attribute__((ext_vector_type(4)));
typedef float f32x2 __attribute__((ext_vector_type(2)));
typedef unsigned u32x4 __attribute__((ext_vector_type(4)));
typedef unsigned u32x2 __attribute__((ext_vector_type(2)));
#define LDSP __attribute__((address_space(3)))
typedef LDSP char* lptr;
#define DI __device__ __forceinline__

constexpr int S_ = 2048, D_ = 1024, F_ = 3584;
constexpr size_t WS_MOD = 0;
constexpr size_t WS_UT = 32768;
constexpr size_t WS_KT = 65536;
constexpr size_t WS_VT = WS_KT + 131072;
constexpr size_t WS_MODP = WS_VT + 131072;
constexpr size_t WS_BUT = WS_MODP + 98304;
constexpr size_t WS_UTP = WS_BUT + 4096;
constexpr size_t WS_H = 1u << 20;
constexpr size_t WS_O = WS_H + (4u << 20);
constexpr size_t WS_X2 = WS_O + (4u << 20);
constexpr size_t WS_Y = WS_X2 + (4u << 20);
constexpr size_t WS_G = WS_Y + (4u << 20);
constexpr size_t WS_WQ = 32u << 20;
constexpr size_t WS_WO = WS_WQ + (2u << 20);
constexpr size_t WS_W1 = WS_WO + (2u << 20);
constexpr size_t WS_W2 = WS_W1 + (7u << 20);
constexpr size_t WS_FLAG = WS_UTP + 131072;
constexpr int FL_EX = 0, FL_O = 32768, FL_Y = 65536, FL_G = 98304, FL_H = 131072, FL_SIDE = 163840, FL_KV = FL_SIDE + 8192, FL_END = FL_KV + 8192;
constexpr size_t WS_ST = 60u << 20;
constexpr size_t WS_EX = 52u << 20;
static_assert(WS_FLAG + FL_END <= WS_H && FL_END % (256 * 4) == 0, "");
static_assert(WS_UTP + 4 * 2 * 3584 * 4 <= WS_H, "");

DI float bf2f(u16 v) { return __uint_as_float((unsigned)v << 16); }
DI float bflo(unsigned v) { return __uint_as_float(v << 16); }
DI float bfhi(unsigned v) { return __uint_as_float(v & 0xffff0000u); }
DI unsigned pk2(float a, float b) { f32x2 v = {a, b}; return __builtin_bit_cast(unsigned, __builtin_convertvector(v, bf16x2)); }
DI float rbf(float a) { return bflo(pk2(a, 0.f)); }
DI f32x16 mfma32(bf16x8 a, bf16x8 b, f32x16 c) { return __builtin_amdgcn_mfma_f32_32x32x16_bf16(a, b, c, 0, 0, 0); }
DI f32x4 mfma16(bf16x8 a, bf16x8 b, f32x4 c) { return __builtin_amdgcn_mfma_f32_16x16x32_bf16(a, b, c, 0, 0, 0); }
DI bf16x8 lds_b128(lptr p) { return *(const LDSP bf16x8*)p; }
DI s16x4 lds_tr(lptr p) { return __builtin_amdgcn_ds_read_tr16_b64_v4i16((LDSP s16x4*)p); }
DI bf16x8 cat(s16x4 a, s16x4 b) { return __builtin_bit_cast(bf16x8, __builtin_shufflevector(a, b, 0, 1, 2, 3, 4, 5, 6, 7)); }
#define WT 16
typedef __amdgpu_buffer_rsrc_t rsrc_t;
DI rsrc_t mk_rsrc(void* base) { return __builtin_amdgcn_make_buffer_rsrc(base, 0, 0x7fffffff, 0x00020000); }
template <int POL = WT> DI void st16(rsrc_t r, unsigned voff, unsigned soff, u32x4 v) { __builtin_amdgcn_raw_buffer_store_b128(v, r, voff + soff, 0, POL); }
struct LP { lptr b[3]; };
DI LP mk_lp(lptr p) { LP r = {{p, p + 65536, p + 131072}}; asm volatile("" : "+v"(r.b[0]), "+v"(r.b[1]), "+v"(r.b[2])); return r; }
template <int OFF> DI bf16x8 lds_at(const LP& p) { return *(const LDSP bf16x8*)(p.b[OFF >> 16] + (OFF & 65535)); }
#define SBAR() __builtin_amdgcn_sched_barrier(0)
#define PIN(x) asm volatile("" : "+v"(x))
template <class T> DI void pin(T& x) { PIN(x); }
template <int N> DI void wait_vm() { asm volatile("s_waitcnt vmcnt(%0)" ::"i"(N) : "memory"); }
DI void bar() { asm volatile("s_barrier" ::: "memory"); }
DI void lgkm_bar() { asm volatile("s_waitcnt lgkmcnt(0)\n\ts_barrier" ::: "memory"); }
template <int C> DI float dpp_add(float v) { return v + __builtin_bit_cast(float, __builtin_amdgcn_update_dpp(0, __builtin_bit_cast(int, v), C, 0xf, 0xf, true)); }
DI float swap16_add(float v) { const auto r = __builtin_amdgcn_permlane16_swap(__float_as_uint(v), __float_as_uint(v), false, false); return __uint_as_float(r[0]) + __uint_as_float(r[1]); }
DI float swap32_add(float v) { const auto r = __builtin_amdgcn_permlane32_swap(__float_as_uint(v), __float_as_uint(v), false, false); return __uint_as_float(r[0]) + __uint_as_float(r[1]); }
DI float wave_sum(float v) {
    v = dpp_add<0xB1>(v); v = dpp_add<0x4E>(v); v = dpp_add<0x141>(v); v = dpp_add<0x140>(v);
    return swap32_add(swap16_add(v));
}

#define PRIO 1
#define GLDS(v, s, off) "global_load_lds_dwordx4 %[" #v "], %[" #s "] offset:" #off "\n\t"
#define GLDN(v, s, off) "global_load_lds_dwordx4 %[" #v "], %[" #s "] offset:" #off " nt\n\t"
#define GLD1(v, s, off) "global_load_lds_dwordx4 %[" #v "], %[" #s "] offset:" #off " sc1\n\t"
#define POLW " sc1"
#define POLA " sc1"
#define GLDW(v, s, off) "global_load_lds_dwordx4 %[" #v "], %[" #s "] offset:" #off POLW "\n\t"
#define GLDA(v, s, off) "global_load_lds_dwordx4 %[" #v "], %[" #s "] offset:" #off POLA "\n\t"
#define SETM0(m) "s_mov_b32 m0, %[" #m "]\n\ts_nop 0\n\t"

typedef __attribute__((address_space(1))) unsigned* gu32;
DI void flag_st(char* ws, unsigned off, unsigned v) { __hip_atomic_store((gu32)(ws + WS_FLAG + off), v, __ATOMIC_RELAXED, __HIP_MEMORY_SCOPE_AGENT); }
DI u32x4 ld16(rsrc_t r, unsigned off) { return __builtin_amdgcn_raw_buffer_load_b128(r, off, 0, 16); }
DI f32x4 ld16f(rsrc_t r, unsigned off) { return __builtin_bit_cast(f32x4, ld16(r, off)); }
DI void async_ld(u32x4& d, unsigned v, const char* s) { asm volatile("global_load_dwordx4 %0, %1, %2 sc1" : "=v"(d) : "v"(v), "s"(s) : "memory"); }
DI void async_ld(unsigned& d, unsigned v, const char* s) { asm volatile("global_load_dword %0, %1, %2 sc1" : "=v"(d) : "v"(v), "s"(s) : "memory"); }
DI void async_ld(f32x2& d, unsigned v, const char* s) { asm volatile("global_load_dwordx2 %0, %1, %2 sc1" : "=v"(d) : "v"(v), "s"(s) : "memory"); }
DI void panel_post(char* ws, int fl, int p, int j, unsigned v, int lane) { if (lane < 16) flag_st(ws, fl + (p * 16 + lane) * 128 + j * 4, v); }
DI void panel_wait(char* ws, int fl, int p, int j, unsigned v, int lane) {
    const gu32 f = (gu32)(ws + WS_FLAG + fl + (p * 16 + j) * 128) + (lane & 15);
    for (int spin = 0; spin < (1 << 22) && !__all(__hip_atomic_load(f, __ATOMIC_RELAXED, __HIP_MEMORY_SCOPE_AGENT) >= v); ++spin) __builtin_amdgcn_s_sleep(1);
    asm volatile("" ::: "memory");
}
DI void grid_post(char* ws, int fl, unsigned v, int lane) { if (lane < 8) flag_st(ws, fl + lane * 1024 + blockIdx.x * 4, v); }
DI bool grid_ok(u32x4 f, unsigned v) { return __all(f[0] >= v && f[1] >= v && f[2] >= v && f[3] >= v); }
DI void grid_wait(char* ws, int fl, unsigned v, int lane) {
    for (int spin = 0; spin < (1 << 22) && !grid_ok(ld16(mk_rsrc(ws), WS_FLAG + fl + (blockIdx.x & 7) * 1024 + lane * 16), v); ++spin) { __builtin_amdgcn_s_sleep(1); asm volatile("" ::: "memory"); }
    asm volatile("" ::: "memory");
}

DI void tile_issue(const u16* src, int ld, unsigned ldsb, int lane) {
    const unsigned vo = (unsigned)((lane >> 3) * ld * 2 + (lane & 7) * 16);
#pragma unroll
    for (int i = 0; i < 4; ++i) {
        const u16* s = src + (size_t)i * 8 * ld;
        const unsigned m = ldsb + i * 1024;
        asm volatile(SETM0(m) GLDN(v, s, 0)::[m] "s"(m), [v] "v"(vo), [s] "s"(s) : "memory");
    }
}
template <bool PERM> DI void tile_store32(lptr st, rsrc_t r, unsigned dst, int lane) {
    const lptr p = st + ((PERM ? 4 : 8) * (lane >> 5) + ((lane & 15) >> 2)) * 128 + ((lane >> 4) & 1) * 32 + (lane & 3) * 8;
    bf16x8 f[4];
#pragma unroll
    for (int i = 0; i < 4; ++i) { const lptr q = p + (i >> 1) * 2048 + (i & 1) * 64; f[i] = cat(lds_tr(q), lds_tr(q + (PERM ? 1024 : 512))); }
#pragma unroll
    for (int i = 0; i < 4; ++i) st16(r, i * 1024 + lane * 16, dst, __builtin_bit_cast(u32x4, f[i]));
}

__global__ void __launch_bounds__(512) k_prep(const u16* __restrict__ c, const u16* __restrict__ wmod, const u16* __restrict__ wq, char* __restrict__ ws) {
    extern __shared__ __attribute__((aligned(16))) char smem[];
    const lptr lds = (lptr)smem;
    const int t = threadIdx.x, lane = t & 63, wid = __builtin_amdgcn_readfirstlane(t >> 6), b = blockIdx.x;
    if (t < FL_END / 1024) *(unsigned*)(ws + WS_FLAG + (b * (FL_END / 1024) + t) * 4) = 0u;
    if (b >= 192) {
        const int j = (b - 192) & 15, kg = (b - 192) >> 4;
        tile_issue(wq + (size_t)(kg * 256 + wid * 32) * D_ + j * 64, D_, (unsigned)(uintptr_t)lds + wid * 4096, lane);
        wait_vm<0>();
        tile_store32<false>(lds + wid * 4096, mk_rsrc(ws), WS_WQ + j * 131072 + (kg * 8 + wid) * 4096, lane);
        return;
    }
    const int cg = b % 48, kg = b / 48, ch = t & 15, rg = t >> 4;
    u32x4 w[8]; u16 cv[8];
#pragma unroll
    for (int i = 0; i < 8; ++i) w[i] = __builtin_nontemporal_load((const u32x4*)(wmod + (size_t)(kg * 256 + i * 32 + rg) * 6144 + cg * 128 + ch * 8));
#pragma unroll
    for (int i = 0; i < 8; ++i) cv[i] = c[kg * 256 + i * 32 + rg];
    float acc[8] = {0, 0, 0, 0, 0, 0, 0, 0};
#pragma unroll
    for (int i = 0; i < 8; ++i) {
        const float f = bf2f(cv[i]);
        const float s = rbf(f / (1.f + __expf(-f)));
#pragma unroll
        for (int j = 0; j < 4; ++j) { acc[2 * j] += s * bflo(w[i][j]); acc[2 * j + 1] += s * bfhi(w[i][j]); }
    }
#pragma unroll
    for (int j = 0; j < 8; ++j) acc[j] = swap32_add(swap16_add(acc[j]));
    if (lane < 16) {
        *(LDSP f32x4*)(lds + wid * 512 + ch * 32) = (f32x4){acc[0], acc[1], acc[2], acc[3]};
        *(LDSP f32x4*)(lds + wid * 512 + ch * 32 + 16) = (f32x4){acc[4], acc[5], acc[6], acc[7]};
    }
    lgkm_bar();
    if (t < 128) {
        float v = 0.f;
#pragma unroll
        for (int i = 0; i < 8; ++i) v += *(const LDSP float*)(lds + i * 512 + t * 4);
        ((float*)(ws + WS_MODP))[kg * 6144 + cg * 128 + t] = v;
    }
}

#define R0 3
constexpr int ring_issued(int i, int NST, int NS) { int n = R0 < NS - 1 ? R0 : NS - 1; for (int k = 0; k < i; ++k) { n += 2; if (n > k + NS) n = k + NS; if (n > NST) n = NST; } return n; }
struct NoEx { static constexpr int n(int) { return 0; } };
template <int NST, int NS, class CNT, class EX, int PA, class F, class X>
DI void ring_loader(F issue, X extra) {
    auto range = [&]<int S0, int... I>(std::integer_sequence<int, I...>) { (issue.template operator()<S0 + I>(), ...); };
    auto sync = [&]<int I>() {
        constexpr int have = ring_issued(I, NST, NS);
        constexpr int from = PA >= 0 && I == PA + 2 ? ring_issued(PA + 1, NST, NS) : I + 1;
        constexpr int out = [] { int c = PA >= 0 && I == PA + 1; for (int s = from; s < have; ++s) c += CNT::n(s); for (int i = 0; i < I; ++i) c += EX::n(i); return c; }();
        static_assert(out < 64 && I < have && (EX::n(I) == 0 || ring_issued(I + 1, NST, NS) == NST), "");
        wait_vm<out>();
        bar();
        range.template operator()<have>(std::make_integer_sequence<int, ring_issued(I + 1, NST, NS) - have>{});
        extra.template operator()<I>();
    };
    range.template operator()<0>(std::make_integer_sequence<int, ring_issued(0, NST, NS)>{});
    [&]<int... I>(std::integer_sequence<int, I...>) { (sync.template operator()<I>(), ...); }(std::make_integer_sequence<int, NST>{});
}
constexpr int cmin(int a, int b) { return a < b ? a : b; }
constexpr int pair_issued(int m, int NST, int NS) {
    int n = 3;
    for (int k = -1; k < m; ++k) n = cmin(cmin(n + 4, k < 0 ? NS : 2 * k + 1 + NS), NST);
    return n;
}
template <int NST, int NS, class CNT, class EX, int PA, class F, class X>
DI void pair_loader(F issue, X extra) {
    auto range = [&]<int S0, int... I>(std::integer_sequence<int, I...>) { (issue.template operator()<S0 + I>(), ...); };
    auto sync = [&]<int M>() {
        constexpr int need = M < 0 ? 0 : cmin(2 * M + 2, NST - 1), have = pair_issued(M, NST, NS);
        constexpr int from = M == PA + 2 ? pair_issued(PA + 1, NST, NS) : need + 1;
        constexpr int out = [] { int c = M == PA + 1; for (int s = from; s < have; ++s) c += CNT::n(s); for (int i = 0; i < M; ++i) c += EX::n(i); return c; }();
        static_assert(out < 64 && need < have && (EX::n(M) == 0 || pair_issued(M + 1, NST, NS) == NST), "");
        wait_vm<out>();
        extra.template operator()<M, 0>();
        bar();
        range.template operator()<have>(std::make_integer_sequence<int, pair_issued(M + 1, NST, NS) - have>{});
        extra.template operator()<M, 1>();
    };
    range.template operator()<0>(std::make_integer_sequence<int, 3>{});
    [&]<int... I>(std::integer_sequence<int, I...>) { (sync.template operator()<I - 1>(), ...); }(std::make_integer_sequence<int, NST / 2 + 1>{});
}
constexpr int G64_STAGE = 24576;
template <int PRE> struct G64Cnt { static constexpr int n(int s) { return s < PRE ? 4 : 6; } };
DI void g64_w(const char* sw, unsigned ldsb, int stage, int slot, int lw, int lane) {
    const unsigned vw = lw * 2048 + lane * 16, mw = ldsb + slot * G64_STAGE + 16384 + lw * 2048;
    const char* s = sw + stage * 8192;
    asm volatile(SETM0(mw) GLDW(vw, s, 0) GLDW(vw, s, 1024)::[mw] "s"(mw), [vw] "v"(vw), [s] "s"(s) : "memory");
}
template <int NST, int NS, int PRE, class EX, int PA, class X>
DI void g64_loader(const char* sa, const char* sw, unsigned ldsb, int lw, int lane, X extra) {
    const unsigned va = lw * 4096 + lane * 16;
    ring_loader<NST, NS, G64Cnt<PRE>, EX, PA>([&]<int S>() {
        const unsigned ma = ldsb + (S % NS) * G64_STAGE + lw * 4096;
        asm volatile(SETM0(ma) GLDA(va, sa, 0) GLDA(va, sa, 1024) GLDA(va, sa, 2048) GLDA(va, sa, 3072)::[ma] "s"(ma), [va] "v"(va), [sa] "s"(sa) : "memory");
        if constexpr (S >= PRE) g64_w(sw, ldsb, S, S % NS, lw, lane);
        sa += 16384;
    }, extra);
}

struct G64Frag { bf16x8 a[4][2], w[4]; };
template <int NST, int NS, class H>
DI void g64_compute(lptr lds, int cw, int lane, f32x16 (&acc)[2], H hook) {
    const LP pa = mk_lp(lds + (lane >> 5) * 2048 + ((cw & 1) * 64 + (lane & 31)) * 16), pw = mk_lp(lds + (cw >> 1) * 1024 + lane * 16);
    G64Frag fr[2];
    auto rd = [&]<int S, int I>() {
        constexpr int so = (S % NS) * G64_STAGE;
        if constexpr (S < NST && I < 12) {
            if constexpr (I % 3 == 0) fr[S & 1].w[I / 3] = lds_at<so + 16384 + (I / 3) * 2048>(pw);
            else fr[S & 1].a[I / 3][I % 3 - 1] = lds_at<so + (I / 3) * 4096 + (I % 3 - 1) * 512>(pa);
        }
    };
    auto step = [&]<int S>() {
        if constexpr (S + 1 < NST) { lgkm_bar(); SBAR(); }
            [&]<int... U>(std::integer_sequence<int, U...>) {
                ((acc[U % 2] = mfma32(fr[S & 1].w[U / 2], fr[S & 1].a[U / 2][U % 2], acc[U % 2]), SBAR(),
                  rd.template operator()<S + 1, 2 * U>(), rd.template operator()<S + 1, 2 * U + 1>(), SBAR()), ...);
            }(std::make_integer_sequence<int, 8>{});
        hook(S);
    };
    bar();
    [&]<int... I>(std::integer_sequence<int, I...>) { (rd.template operator()<0, I>(), ...); }(std::make_integer_sequence<int, 12>{});
    hook(-1);
    [&]<int... S>(std::integer_sequence<int, S...>) { (step.template operator()<S>(), ...); }(std::make_integer_sequence<int, NST>{});
}
DI void tile_map(int b, int& p, int& j) { const int x = b & 7, i = b >> 3; p = (x >> 1) * 4 + (i >> 3); j = (x & 1) * 8 + (i & 7); }

constexpr int K2_W = 98304, K2_MOD = 2 * G64_STAGE, K2_RED = 3 * G64_STAGE;
DI int k2_h(int kc, int tk) { return (kc >> 6) * G64_STAGE + (kc & 63) * 256 + (tk ^ (kc & 7)) * 16; }
DI void ph_ln_kv(const u16* __restrict__ x, const u16* __restrict__ wk, const u16* __restrict__ wv, char* __restrict__ ws, lptr lds, int lane, int wid, int p, int j) {
    const int t = threadIdx.x, tg = p * 8 + (j >> 1), which = j & 1;
    const rsrc_t rws = mk_rsrc(ws);
    const float* modp = (const float*)(ws + WS_MODP);
    u32x4 xr[2][2]; f32x4 mp[4];
    {
        const u16* xp = x + (size_t)(tg * 16 + wid * 2) * D_;
        const unsigned vx = lane * 16, vm = t * 16;
        asm volatile("global_load_dwordx4 %4, %9, %11\n\tglobal_load_dwordx4 %5, %9, %12\n\tglobal_load_dwordx4 %6, %9, %13\n\tglobal_load_dwordx4 %7, %9, %14\n\t"
                     "global_load_dwordx4 %0, %8, %10\n\tglobal_load_dwordx4 %1, %8, %10 offset:1024\n\t"
                     "global_load_dwordx4 %2, %8, %10 offset:2048\n\tglobal_load_dwordx4 %3, %8, %10 offset:3072\n\ts_barrier"
                     : "=&v"(xr[0][0]), "=&v"(xr[0][1]), "=&v"(xr[1][0]), "=&v"(xr[1][1]), "=&v"(mp[0]), "=&v"(mp[1]), "=&v"(mp[2]), "=&v"(mp[3])
                     : "v"(vx), "v"(vm), "s"(xp), "s"(modp), "s"(modp + 6144), "s"(modp + 2 * 6144), "s"(modp + 3 * 6144) : "memory");
    }
    {
        const u16* W = (which ? wv : wk) + wid * 4096;
        const int row = lane >> 2, chn = (lane & 3) ^ (((row >> 3) & 1) << 1);
        const unsigned vo = (unsigned)(row * 64 + chn * 16);
#pragma unroll
        for (int q = 0; q < 2; ++q) {
            const unsigned m = (unsigned)(uintptr_t)lds + K2_W + wid * 8192 + q * 4096;
            const u16* sb_ = W + q * 2048;
            asm volatile(SETM0(m) GLDS(v, s, 0) GLDS(v, s, 1024) GLDS(v, s, 2048) GLDS(v, s, 3072)::[m] "s"(m), [v] "v"(vo), [s] "s"(sb_) : "memory");
        }
    }
    float mf[4] = {0, 0, 0, 0};
    asm volatile("s_waitcnt vmcnt(12)" : "+v"(mp[0]), "+v"(mp[1]), "+v"(mp[2]), "+v"(mp[3])::"memory");
    {
        f32x4 m = (mp[0] + mp[1]) + (mp[2] + mp[3]);
        if (t >= 256) {
#pragma unroll
            for (int e = 0; e < 4; ++e) m[e] = 1.f + rbf(m[e]);
        }
        *(LDSP u32x2*)(lds + K2_MOD + t * 8) = (u32x2){pk2(m[0], m[1]), pk2(m[2], m[3])};
    }
    asm volatile("s_waitcnt vmcnt(8)" : "+v"(xr[0][0]), "+v"(xr[0][1]), "+v"(xr[1][0]), "+v"(xr[1][1])::"memory");
    if (t < 24) {
#pragma unroll
        for (int i = 0; i < 4; ++i) mf[i] = modp[i * 6144 + blockIdx.x * 24 + t];
    }
    f32x2 v[2][8]; float rstd[2];
#pragma unroll
    for (int r = 0; r < 2; ++r) {
#pragma unroll
        for (int j = 0; j < 8; ++j) v[r][j] = (f32x2){bflo(xr[r][j >> 2][j & 3]), bfhi(xr[r][j >> 2][j & 3])};
        const f32x2 s = ((v[r][0] + v[r][1]) + (v[r][2] + v[r][3])) + ((v[r][4] + v[r][5]) + (v[r][6] + v[r][7]));
        const float mean = wave_sum(s[0] + s[1]) * (1.f / 1024.f);
        f32x2 q = {0.f, 0.f};
#pragma unroll
        for (int j = 0; j < 8; ++j) { v[r][j] -= mean; q = __builtin_elementwise_fma(v[r][j], v[r][j], q); }
        rstd[r] = __builtin_amdgcn_rsqf(wave_sum(q[0] + q[1]) * (1.f / 1024.f) + 1e-5f);
    }
    lgkm_bar();
#pragma unroll
    for (int h = 0; h < 2; ++h) {
        const u32x4 ms = *(const LDSP u32x4*)(lds + K2_MOD + h * 1024 + lane * 16), ma = *(const LDSP u32x4*)(lds + K2_MOD + 2048 + h * 1024 + lane * 16);
        u32x4 o[2];
#pragma unroll
        for (int j = 0; j < 4; ++j) {
            const f32x2 sa = {bflo(ms[j]), bfhi(ms[j])}, a1 = {bflo(ma[j]), bfhi(ma[j])};
#pragma unroll
            for (int r = 0; r < 2; ++r) { const f32x2 y = __builtin_elementwise_fma(v[r][h * 4 + j] * rstd[r], a1, sa); o[r][j] = pk2(y[0], y[1]); }
        }
#pragma unroll
        for (int r = 0; r < 2; ++r) *(LDSP u32x4*)(lds + k2_h(h * 64 + lane, wid * 2 + r)) = o[r];
    }
    lgkm_bar();
    {
        const unsigned hAT = WS_H + (tg >> 3) * 262144 + (tg & 7) * 256;
#pragma unroll
        for (int i = 0; i < 2; ++i) {
            const int kc = which * 64 + i * 32 + (t >> 4), tk = t & 15;
            st16(rws, kc * 2048 + tk * 16, hAT, *(const LDSP u32x4*)(lds + k2_h(kc, tk)));
        }
    }
    wait_vm<2>();
    f32x4 acc[2] = {{0, 0, 0, 0}, {0, 0, 0, 0}};
    {
        const int g = lane >> 4, tk = lane & 15, q = (lane & 15) >> 2, p = lane & 3;
#pragma unroll
        for (int s = 0; s < 4; ++s) {
            const int kc = wid * 16 + s * 4 + g;
            const bf16x8 hf = lds_b128(lds + k2_h(kc, tk));
            const int row = kc * 8 + q;
#pragma unroll
            for (int nb = 0; nb < 2; ++nb) {
                const lptr wp = lds + K2_W + row * 64 + ((nb ^ (kc & 1)) * 32) + p * 8;
                const bf16x8 wf = cat(lds_tr(wp), lds_tr(wp + 256));
                acc[nb] = which ? mfma16(hf, wf, acc[nb]) : mfma16(wf, hf, acc[nb]);
            }
        }
    }
#pragma unroll
    for (int nb = 0; nb < 2; ++nb) *(LDSP f32x4*)(lds + K2_RED + ((wid * 2 + nb) * 64 + lane) * 16) = acc[nb];
    wait_vm<0>();
    lgkm_bar();
    if (wid == 3) panel_post(ws, FL_H, p, j, 1u, lane);
    if (wid >= 4) {
#pragma unroll
        for (int i = 0; i < 4; ++i) g64_w(ws + WS_WQ + (size_t)j * 131072, (unsigned)(uintptr_t)lds, i, i, wid - 4, lane);
    }
    if (t < 128) {
        const int nb = t >> 6;
        f32x4 r = *(const LDSP f32x4*)(lds + K2_RED + (nb * 64 + lane) * 16);
#pragma unroll
        for (int w = 1; w < 8; ++w) r += *(const LDSP f32x4*)(lds + K2_RED + ((w * 2 + nb) * 64 + lane) * 16);
        u32x2 o = {pk2(r[0], r[1]), pk2(r[2], r[3])};
        const int g = lane >> 4, c = lane & 15, kt = tg >> 1;
        if (which) {
            __builtin_amdgcn_raw_buffer_store_b64(o, rws, WS_VT + kt * 2048 + ((tg & 1) * 2 + (g & 1)) * 512 + (nb * 16 + c) * 16 + (g >> 1) * 8, 0, 16);
        } else {
            __builtin_amdgcn_raw_buffer_store_b64(o, rws, WS_KT + kt * 2048 + (nb * 2 + (g & 1)) * 512 + ((tg & 1) * 16 + c) * 16 + (g >> 1) * 8, 0, 16);
        }
    }
    if (t < 24) {
        const int col = blockIdx.x * 24 + t, part = col >> 10, k = col & 1023;
        float v = rbf((mf[0] + mf[1]) + (mf[2] + mf[3]));
        if (part == 1 || part == 4) v = rbf(1.f + v);
        __builtin_amdgcn_raw_buffer_store_b32(__float_as_uint(v), rws, WS_MOD + col * 4, 0, 16);
        if (part == 3 || part == 4)
            __builtin_amdgcn_raw_buffer_store_b16((u16)(__float_as_uint(v) >> 16), rws,
                WS_BUT + ((((k >> 5) * 2 + (part == 3)) * 4 + 2 * ((k >> 4) & 1) + ((k >> 2) & 1)) * 8 + (k & 3) + 4 * ((k >> 3) & 1)) * 2, 0, 16);
    }
}

constexpr int QA_NS = 4;
constexpr int QA_KV0 = QA_NS * G64_STAGE;
constexpr int QA_LDS = QA_KV0 + 65536;
constexpr float QSCALE = 0.17677669529663687f * 1.4426950408889634f;

constexpr int OP_NS = 6;
#define OPRE 4
#define KVL 8
#define KVC 11
#define KVN 4
#define KVP 1
#define PROG 1
DI void ph_qattn(const u16* __restrict__ wo, const u16* __restrict__ w1, const u16* __restrict__ w2, char* __restrict__ ws, lptr lds, int lane, int wid, int p, int j) {
    const unsigned ldsb = (unsigned)(uintptr_t)lds;
    const char* KT = ws + WS_KT; const char* VT = ws + WS_VT;
    const unsigned vkv = lane * 16;
    auto kv_piece = [&](int chunk, unsigned buf, int w, int i) {
        const char* s = ((i & 4) ? VT : KT) + chunk * 32768 + w * 4096 + (i & 3) * 1024;
        const unsigned m = ldsb + buf + (i >> 2) * 32768 + w * 4096 + (i & 3) * 1024;
        asm volatile(SETM0(m) GLD1(v, s, 0)::[m] "s"(m), [v] "v"(vkv), [s] "s"(s) : "memory");
    };
    const int b = blockIdx.x;
    const bool big = b < 224;
    const rsrc_t rws = mk_rsrc(ws);
    const lptr stg = lds + 65536 + wid * 4096;
    const int jA = b & 15, gA = big ? (b >> 4) : ((b - 224) >> 4);
    const int gB = big ? (b / 56) : 2 + ((b - 224) >> 4), cB = big ? (b % 56) : jA;
    u32x4 butl, kfl;
    const unsigned vbut = (lane & 1) * 64 + (lane >> 4) * 16;
    const char* sbut = ws + WS_BUT + (gB * 8 + wid) * 128;
    if (wid >= 4) {
        panel_wait(ws, FL_H, p, j, 1u, lane);
        g64_loader<16, QA_NS, QA_NS, NoEx, -1>(ws + WS_H + (size_t)p * 262144, ws + WS_WQ + (size_t)j * 131072, ldsb, wid - 4, lane, []<int I>() {});
        async_ld(butl, vbut, sbut);
        asm volatile("s_waitcnt vmcnt(0)" : "+v"(butl)::"memory");
        bar();
    } else {
        f32x16 r[2] = {};
        g64_compute<16, QA_NS>(lds, wid, lane, r, [&](int i) {
            if (i == KVP) wait_vm<0>();
            if (i == KVP + 1 && wid == 0) grid_post(ws, FL_KV, 1u, lane);
            if (i == KVL) async_ld(kfl, lane * 16, ws + WS_FLAG + FL_KV + (b & 7) * 1024);
            if (i == KVC) {
                asm volatile("s_waitcnt vmcnt(0)" : "+v"(kfl)::"memory");
                if (!grid_ok(kfl, 1u)) grid_wait(ws, FL_KV, 1u, lane);
                async_ld(butl, vbut, sbut);
            }
            if (i >= KVC && i < KVC + 16 / KVN) {
#pragma unroll
                for (int q = 0; q < KVN; ++q) kv_piece(0, QA_KV0, wid + 4 * (q & 1), ((i - KVC) * KVN + q) >> 1);
            }
        });
#pragma unroll
        for (int tb = 0; tb < 2; ++tb)
#pragma unroll
            for (int s = 0; s < 2; ++s) {
                u32x4 o;
#pragma unroll
                for (int i = 0; i < 4; ++i) o[i] = pk2(r[tb][8 * s + 2 * i] * QSCALE, r[tb][8 * s + 2 * i + 1] * QSCALE);
                *(LDSP u32x4*)(lds + 32768 + ((wid * 2 + tb) * 2 + s) * 1024 + lane * 16) = o;
            }
        asm volatile("s_waitcnt vmcnt(0)" : "+v"(butl)::"memory");
        lgkm_bar();
    }
    const int qg = wid & 3, kh = wid >> 2;
    const int lo = kh * 16384 + lane * 16;
    bf16x8 qA0, qA1, qB0, qB1, k0, k1, v0, v1;
    {
        const lptr qp = lds + 32768 + qg * 4096 + lane * 16;
        qA0 = lds_b128(qp); qA1 = lds_b128(qp + 1024); qB0 = lds_b128(qp + 2048); qB1 = lds_b128(qp + 3072);
        k0 = lds_b128(lds + QA_KV0 + lo); k1 = lds_b128(lds + QA_KV0 + lo + 1024);
    }
    bf16x8 sel;
    {
        const unsigned on = ((lane & 15) == ((lane >> 4) & 1)) ? 0x3f803f80u : 0u;
        sel = __builtin_bit_cast(bf16x8, (u32x4){on, on, on, on});
    }
    f32x16 oA = {}, oB = {}, sA, sB;
    f32x4 lA = {}, lB = {};
    const f32x16 zero = {};
    u32x4 pA0, pA1, pB0 = {0, 0, 0, 0}, pB1 = {0, 0, 0, 0};
    bf16x8 but = __builtin_bit_cast(bf16x8, (lane & 15) < 2 ? butl : (u32x4){0, 0, 0, 0});
    PIN(but);
    lgkm_bar();
#define EXP2(E, d, S, i) do { E[d] = __builtin_amdgcn_exp2f(S[i]); E[d + 1] = __builtin_amdgcn_exp2f(S[i + 1]); PIN(E); } while (0)
#define EXP4(E, S, i) do { E[0] = __builtin_amdgcn_exp2f(S[i]); E[1] = __builtin_amdgcn_exp2f(S[i + 1]); E[2] = __builtin_amdgcn_exp2f(S[i + 2]); E[3] = __builtin_amdgcn_exp2f(S[i + 3]); } while (0)
#define PK2(P, d, E) do { P[d] = pk2(E[0], E[1]); P[d + 1] = pk2(E[2], E[3]); PIN(P); } while (0)
#define BF(x) __builtin_bit_cast(bf16x8, x)
#define HALF(SX, PX0, PX1, SY, QY0, QY1, OZ, LZ, PZ0, PZ1, R0, R1, R2, R3) do { \
        SY = mfma32(k0, QY0, zero); SBAR(); PK2(PZ1, 2, e); R0; SBAR(); EXP4(e, SX, 0); PIN(e); SBAR(); \
        SY = mfma32(k1, QY1, SY); SBAR(); PK2(PX0, 0, e); R1; SBAR(); EXP2(e, 0, SX, 4); SBAR(); LZ = mfma16(sel, BF(PZ0), LZ); SBAR(); EXP2(e, 2, SX, 6); SBAR(); \
        OZ = mfma32(v0, BF(PZ0), OZ); SBAR(); PK2(PX0, 2, e); R2; SBAR(); EXP4(e, SX, 8); PIN(e); SBAR(); \
        OZ = mfma32(v1, BF(PZ1), OZ); SBAR(); PK2(PX1, 0, e); R3; SBAR(); EXP2(e, 0, SX, 12); SBAR(); LZ = mfma16(sel, BF(PZ1), LZ); SBAR(); EXP2(e, 2, SX, 14); SBAR(); } while (0)
    sA = mfma32(k0, qA0, zero); sA = mfma32(k1, qA1, sA);
    v0 = k0; v1 = k1;
    f32x4 e = {0, 0, 0, 0};
    bf16x8 w0, w1_, sf[4];
    f32x4 ut[4];
    u32x4 sfl;
    const lptr tp32 = stg + (4 * (lane >> 5) + ((lane & 15) >> 2)) * 128 + ((lane >> 4) & 1) * 32 + (lane & 3) * 8;
    const lptr tp16 = stg + (16 * (lane >> 5) + 4 * ((lane >> 4) & 1) + ((lane & 15) >> 2)) * 128 + (lane & 3) * 8;
    unsigned dstA = big ? WS_W2 + (jA >> 1) * 917504 + (gA * 8 + wid) * 8192 + (jA & 1) * 4096 : WS_WO + jA * 131072 + (gA * 8 + wid) * 4096, dstB[4];
#pragma unroll
    for (int i = 0; i < 4; ++i) {
        const int nbk = cB * 4 + i, jt = nbk / 14;
        dstB[i] = big ? WS_W1 + jt * 458752 + (gB * 8 + wid) * 14336 + (nbk - jt * 14) * 1024 : WS_WO + cB * 131072 + (gB * 8 + wid) * 4096 + i * 1024;
        asm volatile("" : "+s"(dstB[i]));
    }
    const int ldB = big ? F_ : D_;
    const u16* srcA = (big ? w2 : wo) + jA * 64 + (size_t)(gA * 256 + wid * 32) * D_;
    const u16* srcB = (big ? w1 : wo) + cB * 64 + (size_t)(gB * 256 + wid * 32) * ldB;
    asm volatile("" : "+s"(dstA), "+s"(srcA), "+s"(srcB));
    auto side = [&]<int C, int IT, int I>() {
        if constexpr (C < 3 && IT < 2) kv_piece(C + 1, (C & 1) ? QA_KV0 : 0, wid, IT * 4 + I);
        if constexpr ((C == 0 && IT == 2) || (C == 1 && IT == 4)) {
            const int ld = C == 0 ? D_ : ldB;
            const u16* s = (C == 0 ? srcA : srcB) + I * 8 * ld;
            const unsigned vo = (unsigned)((lane >> 3) * ld * 2 + (lane & 7) * 16), m = ldsb + 65536 + wid * 4096 + I * 1024;
            asm volatile(SETM0(m) GLDN(v, s, 0)::[m] "s"(m), [v] "v"(vo), [s] "s"(s) : "memory");
        }
        if constexpr ((C == 1 || C == 2) && IT == 2) {
            if constexpr (I == 0) wait_vm<8>();
            const lptr q = !big ? tp32 + (I >> 1) * 2048 + (I & 1) * 64 : (C == 2 ? tp16 + I * 32 : tp16 + (lane & 3) * 8 + (I >> 1) * 64 + (I & 1) * 8);
            sf[I] = cat(lds_tr(q), lds_tr(q + 1024));
        }
        if constexpr ((C == 1 || C == 2) && IT == 3) {
            const unsigned dst = C == 1 ? dstA + I * 1024 : dstB[I];
            if constexpr (C == 2) ut[I] = mfma16(sf[I], but, (f32x4){0, 0, 0, 0});
            st16(rws, lane * 16, dst, __builtin_bit_cast(u32x4, sf[I]));
        }
        if constexpr (C == 2 && IT == 4) *(LDSP f32x4*)(stg + I * 1024 + lane * 16) = ut[I];
        if constexpr (C == 0 && IT == 3 && I < 2) st16(rws, lane * 16, WS_EX + blockIdx.x * 16384 + wid * 2048 + I * 1024, (u32x4){~0u, ~0u, ~0u, ~0u});
        if constexpr (C == 3 && IT == 0 && I == 0) { if (wid == 0) grid_post(ws, FL_SIDE, 1u, lane); }
        if constexpr (C == 3 && IT == 0) {
            if (wid == 0) {
                const lptr q = lds + 65536 + I * 8192 + (lane >> 4) * 1024 + ((lane >> 2) & 3) * 256 + (lane & 3) * 4;
                ut[I] = (f32x4){*(const LDSP float*)q, *(const LDSP float*)(q + 16), *(const LDSP float*)(q + 4096), *(const LDSP float*)(q + 4096 + 16)};
            }
        }
        if constexpr (C == 3 && IT == 1 && I == 1) {
            if (wid == 0 && big) {
                const f32x4 a = (ut[0] + ut[1]) + (ut[2] + ut[3]);
#pragma unroll
                for (int i = 0; i < 2; ++i) __builtin_amdgcn_raw_buffer_store_b32(__float_as_uint(a[i] + a[2 + i]), rws, WS_UTP + ((gB * 2 + i) * F_ + cB * 64 + lane) * 4, 0, 16);
            }
        }
        if constexpr (C == 3 && IT == 6 && I == 0) {
            if (wid == 0) { wait_vm<0>(); grid_post(ws, FL_SIDE, 2u, lane); }
            async_ld(sfl, lane * 16, ws + WS_FLAG + FL_SIDE + (blockIdx.x & 7) * 1024);
        }
    };
#define SIDE(I) side.template operator()<C, IT, I>()
    auto step = [&]<int C, int IT>() {
        const lptr kp = lds + ((C & 1) ? 0 : QA_KV0) + lo, kn = lds + ((C & 1) ? QA_KV0 : 0) + lo;
        if constexpr (PRIO && IT == 5) __builtin_amdgcn_s_setprio(2);
        if constexpr (PRIO && IT == 6) __builtin_amdgcn_s_setprio(1);
        if constexpr (IT < 7) {
            HALF(sA, pA0, pA1, sB, qB0, qB1, oB, lB, pB0, pB1, SIDE(0), k0 = lds_b128(kp + (IT + 1) * 2048),
                 k1 = lds_b128(kp + (IT + 1) * 2048 + 1024), v0 = lds_b128(kp + 32768 + IT * 2048));
            if constexpr (PRIO && IT == 6) __builtin_amdgcn_s_setprio(0);
            HALF(sB, pB0, pB1, sA, qA0, qA1, oA, lA, pA0, pA1, v1 = lds_b128(kp + 32768 + IT * 2048 + 1024),
                 SIDE(1); if constexpr (IT == 6) w0 = lds_b128(kp + 32768 + 7 * 2048), SIDE(2); if constexpr (IT == 6) w1_ = lds_b128(kp + 32768 + 7 * 2048 + 1024), SIDE(3));
        } else {
            if constexpr (C < 3) {
                wait_vm<C == 2 ? 0 : (C == 1 ? 8 : 6)>();
                lgkm_bar();
                if constexpr (PRIO) __builtin_amdgcn_s_setprio(3);
            }
            HALF(sA, pA0, pA1, sB, qB0, qB1, oB, lB, pB0, pB1, (void)0, if constexpr (C < 3) k0 = lds_b128(kn), if constexpr (C < 3) k1 = lds_b128(kn + 1024), (void)0);
            v0 = w0; v1 = w1_;
            HALF(sB, pB0, pB1, sA, qA0, qA1, oA, lA, pA0, pA1, (void)0, (void)0, (void)0, (void)0);
        }
    };
    if constexpr (PRIO) __builtin_amdgcn_s_setprio(3);
    [&]<int... N>(std::integer_sequence<int, N...>) { (step.template operator()<N / 8, N % 8>(), ...); }(std::make_integer_sequence<int, 32>{});
    PK2(pB1, 2, e);
    oB = mfma32(v0, BF(pB0), oB); oB = mfma32(v1, BF(pB1), oB);
    lB = mfma16(sel, BF(pB0), lB); lB = mfma16(sel, BF(pB1), lB);
    PIN(lA); PIN(lB); PIN(oA); PIN(oB);
    const lptr ex = lds + QA_KV0;
    {
        const f32x16& so = kh ? oA : oB;
        const f32x4& sl = kh ? lA : lB;
#pragma unroll
        for (int i = 0; i < 4; ++i) *(LDSP f32x4*)(ex + wid * 4096 + i * 1024 + lane * 16) = (f32x4){so[4 * i], so[4 * i + 1], so[4 * i + 2], so[4 * i + 3]};
        const f32x4& ml = kh ? lB : lA;
        if (lane < 16) {
            *(LDSP float*)(ex + 32768 + wid * 256 + lane * 4) = sl[0]; *(LDSP float*)(ex + 32768 + wid * 256 + 64 + lane * 4) = sl[1];
            *(LDSP float*)(ex + 32768 + wid * 256 + 128 + lane * 4) = ml[0]; *(LDSP float*)(ex + 32768 + wid * 256 + 192 + lane * 4) = ml[1];
        }
    }
    lgkm_bar();
    asm volatile("s_waitcnt vmcnt(0)" : "+v"(sfl)::"memory");
    if (wid >= 4) {
        if (!grid_ok(sfl, lane >= 56)) grid_wait(ws, FL_SIDE, lane >= 56, lane);
#pragma unroll
        for (int i = 0; i < OPRE; ++i) g64_w(ws + WS_WO + (size_t)j * 131072, ldsb, i, i, wid - 4, lane);
    }
    f32x16 o = kh ? oB : oA;
    float l;
    {
        l = *(const LDSP float*)(ex + 32768 + wid * 256 + 128 + (lane & 31) * 4) + *(const LDSP float*)(ex + 32768 + (wid ^ 4) * 256 + (lane & 31) * 4);
#pragma unroll
        for (int i = 0; i < 4; ++i) {
            const f32x4 v = *(const LDSP f32x4*)(ex + (wid ^ 4) * 4096 + i * 1024 + lane * 16);
#pragma unroll
            for (int e = 0; e < 4; ++e) o[4 * i + e] += v[e];
        }
    }
    float chk = l * 0.f;
#pragma unroll
    for (int i = 0; i < 16; ++i) chk = __builtin_fmaf(o[i], 0.f, chk);
    const bool bad = !(chk == 0.f) || !(l >= 0x1p-90f);
    if (__builtin_expect(__any(bad), 0)) {
        const bf16x8 q0 = kh ? qB0 : qA0, q1 = kh ? qB1 : qA1;
        float m = -INFINITY; l = 0.f; o = zero;
        for (int kt = 0; kt < 64; ++kt) {
            const bf16x8 a0 = BF(ld16(rws, WS_KT + kt * 2048 + lane * 16)), a1 = BF(ld16(rws, WS_KT + kt * 2048 + 1024 + lane * 16));
            const bf16x8 b0 = BF(ld16(rws, WS_VT + kt * 2048 + lane * 16)), b1 = BF(ld16(rws, WS_VT + kt * 2048 + 1024 + lane * 16));
            f32x16 s = mfma32(a0, q0, zero); s = mfma32(a1, q1, s);
            float mx = s[0];
#pragma unroll
            for (int i = 1; i < 16; ++i) mx = fmaxf(mx, s[i]);
            mx = fmaxf(mx, __shfl_xor(mx, 32));
            const float mn = fmaxf(m, mx), al = __builtin_amdgcn_exp2f(m - mn);
            m = mn;
            u32x4 p0, p1; float rs = 0.f;
#pragma unroll
            for (int i = 0; i < 4; ++i) {
                p0[i] = pk2(__builtin_amdgcn_exp2f(s[2 * i] - m), __builtin_amdgcn_exp2f(s[2 * i + 1] - m));
                p1[i] = pk2(__builtin_amdgcn_exp2f(s[8 + 2 * i] - m), __builtin_amdgcn_exp2f(s[9 + 2 * i] - m));
                rs += bflo(p0[i]) + bfhi(p0[i]) + bflo(p1[i]) + bfhi(p1[i]);
            }
            rs += __shfl_xor(rs, 32);
            l = l * al + rs;
#pragma unroll
            for (int i = 0; i < 16; ++i) o[i] *= al;
            o = mfma32(b0, BF(p0), o); o = mfma32(b1, BF(p1), o);
        }
    }
    {
        const float inv = __builtin_amdgcn_rcpf(l);
        const unsigned oAT = WS_O + p * 262144 + (2 * j + (qg >> 1)) * 8192 + ((qg & 1) * 2 + kh) * 512, vo = (lane >> 5) * 2048 + (lane & 31) * 16;
#pragma unroll
        for (int s = 0; s < 2; ++s) {
            u32x4 w;
#pragma unroll
            for (int i = 0; i < 4; ++i) w[i] = pk2(o[8 * s + 2 * i] * inv, o[8 * s + 2 * i + 1] * inv);
            st16(rws, vo, oAT + s * 4096, w);
        }
    }
    wait_vm<0>();
    lgkm_bar();
    if (wid == 0) panel_post(ws, FL_O, p, j, 1u, lane);
}

constexpr int F1_NS = 7, F1_STAGE = 22528, F1_NST = 32, F1_AUX = F1_NS * F1_STAGE;
template <int NW> DI void f1_w(const char* sw, unsigned ldsb, int stage, int pc0, int lane) {
    const unsigned vw = pc0 * 1024 + lane * 16, mw = ldsb + (stage % F1_NS) * F1_STAGE + 8192 + pc0 * 1024;
    const char* s = sw + stage * 14336;
    if constexpr (NW == 4)
        asm volatile(SETM0(mw) GLDW(vw, s, 0) GLDW(vw, s, 1024) GLDW(vw, s, 2048) GLDW(vw, s, 3072)::[mw] "s"(mw), [vw] "v"(vw), [s] "s"(s) : "memory");
    else
        asm volatile(SETM0(mw) GLDW(vw, s, 0) GLDW(vw, s, 1024) GLDW(vw, s, 2048)::[mw] "s"(mw), [vw] "v"(vw), [s] "s"(s) : "memory");
}
template <int NW> struct OpEx { static constexpr int n(int i) { return i < 11 ? 0 : i == 12 ? 2 * NW : NW; } };
template <int NW>
DI void op_loader(char* ws, lptr lds, int lane, int lw, int p, int j) {
    const unsigned ldsb = (unsigned)(uintptr_t)lds;
    const char* sw = ws + WS_WO + (size_t)j * 131072;
    const char* s1 = ws + WS_W1 + (size_t)j * 458752;
    const int pc0 = lw < 2 ? lw * 4 : 8 + (lw - 2) * 3;
#pragma unroll
    for (int i = OPRE; i < OP_NS; ++i) g64_w(sw, ldsb, i, i, lw, lane);
    panel_wait(ws, FL_O, p, j, 1u, lane);
    u32x4 fl;
    g64_loader<16, OP_NS, OP_NS, OpEx<NW>, 8>(ws + WS_O + (size_t)p * 262144, sw, ldsb, lw, lane, [&]<int I>() {
        if constexpr (I == 8) async_ld(fl, lane * 16, ws + WS_FLAG + FL_SIDE + (blockIdx.x & 7) * 1024);
        if constexpr (I == 10) { pin(fl); if (!grid_ok(fl, 2u)) grid_wait(ws, FL_SIDE, 2u, lane); }
        if constexpr (I >= 11) f1_w<NW>(s1, ldsb, I < 13 ? I - 7 : I - 13, pc0, lane);
        if constexpr (I == 12) f1_w<NW>(s1, ldsb, 6, pc0, lane);
    });
}
DI void op_compute(const u16* __restrict__ x, char* __restrict__ ws, lptr lds, int lane, int wid, int p, int j) {
    const int th = wid & 1, nb = wid >> 1, hi = lane >> 5;
    const int n0 = j * 64 + nb * 32 + 4 * hi;
    u32x4 xq[2][2]; f32x4 ga[4], a2[4];
    f32x16 r[2] = {};
    g64_compute<16, OP_NS>(lds, wid, lane, r, [&](int i) {
        if (i == -1) {
#pragma unroll
            for (int tb = 0; tb < 2; ++tb)
#pragma unroll
                for (int h = 0; h < 2; ++h) xq[tb][h] = *(const u32x4*)(x + (size_t)(p * 128 + th * 64 + tb * 32 + (lane & 31)) * D_ + j * 64 + nb * 32 + h * 16 + hi * 8);
#pragma unroll
            for (int q = 0; q < 4; ++q) {
                ga[q] = ld16f(mk_rsrc(ws), WS_MOD + (2048 + n0 + 8 * q) * 4);
                a2[q] = ld16f(mk_rsrc(ws), WS_MOD + (4096 + n0 + 8 * q) * 4);
            }
            SBAR();
        }
    });
    u32x4 xk[2][2];
#pragma unroll
    for (int tb = 0; tb < 2; ++tb) {
        u32x4 (&xo)[2] = xk[tb]; u32x4 yo[2]; float v[16]; float s = 0.f;
        u32x2 xr[2][4];
#pragma unroll
        for (int h = 0; h < 2; ++h) {
            const auto s0 = __builtin_amdgcn_permlane32_swap(xq[tb][h][0], xq[tb][h][2], false, false);
            const auto s1 = __builtin_amdgcn_permlane32_swap(xq[tb][h][1], xq[tb][h][3], false, false);
            xr[tb][2 * h] = (u32x2){s0[0], s1[0]}; xr[tb][2 * h + 1] = (u32x2){s0[1], s1[1]};
        }
#pragma unroll
        for (int i = 0; i < 4; ++i) {
            const float x0 = bflo(xr[tb][i][0]), x1 = bfhi(xr[tb][i][0]), x2 = bflo(xr[tb][i][1]), x3 = bfhi(xr[tb][i][1]);
            const unsigned a = pk2(x0 + ga[i][0] * r[tb][4 * i], x1 + ga[i][1] * r[tb][4 * i + 1]);
            const unsigned b = pk2(x2 + ga[i][2] * r[tb][4 * i + 2], x3 + ga[i][3] * r[tb][4 * i + 3]);
            xo[i >> 1][(i & 1) * 2] = a; xo[i >> 1][(i & 1) * 2 + 1] = b;
            v[4 * i] = bflo(a); v[4 * i + 1] = bfhi(a); v[4 * i + 2] = bflo(b); v[4 * i + 3] = bfhi(b);
            yo[i >> 1][(i & 1) * 2] = pk2(v[4 * i] * a2[i][0], v[4 * i + 1] * a2[i][1]);
            yo[i >> 1][(i & 1) * 2 + 1] = pk2(v[4 * i + 2] * a2[i][2], v[4 * i + 3] * a2[i][3]);
            s += (v[4 * i] + v[4 * i + 1]) + (v[4 * i + 2] + v[4 * i + 3]);
        }
        const unsigned off = p * 262144 + (j * 2 + nb) * 8192 + (th * 64 + tb * 32) * 16, vo = hi * 2048 + (lane & 31) * 16;
#pragma unroll
        for (int h = 0; h < 2; ++h) {
            st16(mk_rsrc(ws), vo, WS_Y + off + h * 4096, yo[h]);
        }
        const float mean = swap32_add(s) * (1.f / 32.f);
        float q = 0.f;
#pragma unroll
        for (int i = 0; i < 16; ++i) { const float d = v[i] - mean; q += d * d; }
        q = swap32_add(q);
        if (hi == 0) __builtin_amdgcn_raw_buffer_store_b64((u32x2){__float_as_uint(mean), __float_as_uint(q)}, mk_rsrc(ws),
                                                           WS_ST + ((j * 2 + nb) * S_ + p * 128 + th * 64 + tb * 32 + lane) * 8, 0, 16);
    }
    wait_vm<0>();
    bar();
    if (wid == 0) panel_post(ws, FL_Y, p, j, 1u, lane);
#pragma unroll
    for (int tb = 0; tb < 2; ++tb)
#pragma unroll
        for (int h = 0; h < 2; ++h) {
            const auto s0 = __builtin_amdgcn_permlane32_swap(xk[tb][h][0], xk[tb][h][2], false, false);
            const auto s1 = __builtin_amdgcn_permlane32_swap(xk[tb][h][1], xk[tb][h][3], false, false);
            st16(mk_rsrc(ws), hi * 2048 + (lane & 31) * 16, WS_X2 + p * 262144 + (j * 2 + nb) * 8192 + (th * 64 + tb * 32) * 16 + h * 4096, (u32x4){s0[0], s1[0], s0[1], s1[1]});
        }
}

#define F2_D 6
#define F2_XP 1
#define F2_MP 4
constexpr int F2_NS = 10, F2_STAGE = 16384, F2_MAIN = 56 - F2_D, F2_NST = 56 + F2_D;
#define F2_EB 4
#define F2_PS 6
#define F2_PA 8
constexpr int F2_LATE = 7 + 7 * F2_EB;
static_assert(F2_LATE <= F2_MAIN, "");
DI unsigned f2_off(int s) {
    const int o = s >= 56, i = o ? s - F2_D : s;
    const int r = i < 7 ? 0 : i < F2_LATE ? 1 + (i - 7) / F2_EB : 1 + (i - F2_LATE) / (7 - F2_EB);
    const int st = i < 7 ? i : i < F2_LATE ? (i - 7) % F2_EB : F2_EB + (i - F2_LATE) % (7 - F2_EB);
    const unsigned b = blockIdx.x, jl = (b >> 3) & 7, q = r ? r - 1 + (r - 1 >= jl) : jl;
    return ((((b & 1) ^ o) * 8 + q) * 7 + st) * 8192;
}
DI void f2_w(char* ws, unsigned ldsb, int i, int lw, int lane) {
    const int b = blockIdx.x;
    const unsigned mw = ldsb + i * F2_STAGE + 8192 + lw * 2048, va = lw * 2048 + lane * 16;
    const char* s = ws + WS_W2 + (size_t)((b >> 3) & 7) * 917504 + f2_off(i);
    asm volatile(SETM0(mw) GLDW(va, s, 0) GLDW(va, s, 1024)::[mw] "s"(mw), [va] "v"(va), [s] "s"(s) : "memory");
}
struct F1Ex { static constexpr int n(int m) { return !PROG || m < 13 ? 0 : m == 14 ? 4 : 6; } };
template <int NW> struct F1Cnt { static constexpr int n(int s) { return (s < F1_NS ? 2 : 2 + NW) + (s == 2 ? 16 : 0); } };
template <int NW>
DI void f1_loader(char* ws, int p, int j, lptr lds, int lw, int lane) {
    const char* sa = ws + WS_Y + (size_t)p * 262144;
    const char* sw = ws + WS_W1 + (size_t)j * 458752;
    const unsigned ldsb = (unsigned)(uintptr_t)lds;
    const int pc0 = lw < 2 ? lw * 4 : 8 + (lw - 2) * 3;
    const unsigned va = lw * 2048 + lane * 16;
#pragma unroll
    for (int i = 0; i < F1_NS; ++i) if (i != 3 && !PROG) f1_w<NW>(sw, ldsb, i, pc0, lane);
    if (NW == 3) {
        if (lane < 56) {
            const unsigned o = WS_UTP + ((lw - 2) * F_ + j * 224 + lane * 4) * 4;
            const rsrc_t r = mk_rsrc(ws);
            const f32x4 u = (ld16f(r, o) + ld16f(r, o + 2 * F_ * 4)) + (ld16f(r, o + 4 * F_ * 4) + ld16f(r, o + 6 * F_ * 4));
            *(LDSP f32x4*)(lds + F1_AUX + (lw - 2) * 1024 + lane * 16) = u * (lw == 2 ? 1.f : -1.4426950408889634f);
        }
    }
    bar();
    f1_w<NW>(sw, ldsb, 3, pc0, lane);
    panel_wait(ws, FL_Y, p, j, 1u, lane);
    f32x2 sp[16];
    unsigned fl;
    pair_loader<F1_NST, F1_NS, F1Cnt<NW>, F1Ex, 8>([&]<int S>() {
        const unsigned ma = ldsb + (S % F1_NS) * F1_STAGE + lw * 2048;
        asm volatile(SETM0(ma) GLDA(va, sa, 0) GLDA(va, sa, 1024)::[ma] "s"(ma), [va] "v"(va), [sa] "s"(sa) : "memory");
        if constexpr (S >= F1_NS) f1_w<NW>(sw, ldsb, S, pc0, lane);
        sa += 8192;
        if constexpr (S == 2) {
            const char* s = ws + WS_ST + (size_t)(p * 128 + lw * 32) * 8;
            const unsigned v0 = (lane >> 5) * 16 * (S_ * 8) + (lane & 31) * 8;
#pragma unroll
            for (int i = 0; i < 16; ++i) async_ld(sp[i], v0 + i * (S_ * 8), s);
        }
    }, [&]<int M, int PH>() {
        if constexpr (M == 0 && PH == 0) {
#pragma unroll
            for (int i = 0; i < 16; ++i) PIN(sp[i]);
        }
        if constexpr (PROG && M == 13 && PH == 1) { f2_w(ws, ldsb, 5, lw, lane); f2_w(ws, ldsb, 6, lw, lane); f2_w(ws, ldsb, 7, lw, lane); }
        if constexpr (PROG && M == 14 && PH == 1) { f2_w(ws, ldsb, 0, lw, lane); f2_w(ws, ldsb, 8, lw, lane); }
        if constexpr (PROG && M == 15 && PH == 1) { f2_w(ws, ldsb, 1, lw, lane); f2_w(ws, ldsb, 2, lw, lane); f2_w(ws, ldsb, 3, lw, lane); }
        if constexpr (M == 8 && PH == 1) async_ld(fl, (lane & 15) * 4, ws + WS_FLAG + FL_Y + (p * 16 + j) * 128);
        if constexpr (M == 10 && PH == 1) { pin(fl); if (!__all(fl >= 2u)) panel_wait(ws, FL_Y, p, j, 2u, lane); }
        if constexpr (M == 2 && PH == 1) {
            float s = 0.f;
#pragma unroll
            for (int i = 0; i < 16; ++i) s += sp[i][0];
            const float mu = swap32_add(s) * (1.f / 32.f);
            float q = 0.f;
#pragma unroll
            for (int i = 0; i < 16; ++i) { const float d = sp[i][0] - mu; q += sp[i][1] + 32.f * d * d; }
            const float rl = -1.4426950408889634f * __builtin_amdgcn_rsqf(swap32_add(q) * (1.f / 1024.f) + 1e-5f);
            if (lane < 32) *(LDSP f32x2*)(lds + F1_AUX + 2048 + (lw * 32 + lane) * 8) = (f32x2){mu, rl};
            asm volatile("s_waitcnt lgkmcnt(0)" ::: "memory");
        }
    });
}
struct F1Frag { bf16x8 a[4], w[7]; };
DI void f1_compute(char* __restrict__ ws, lptr lds, int lane, int wid, int p, int j) {
    const int th = wid & 1, nh = wid >> 1, g = lane >> 4;
    f32x4 acc[7][4] = {};
    const LP pa = mk_lp(lds + g * 2048 + (th * 64 + (lane & 15)) * 16), pw = mk_lp(lds + nh * 1024 + lane * 16);
    F1Frag fr[2];
    auto rd = [&]<int S, int I>() {
        constexpr int so = (S % F1_NS) * F1_STAGE;
        if constexpr (S < F1_NST && I < 11) {
            if constexpr (I >= 1 && I <= 4) fr[S & 1].a[I - 1] = lds_at<so + (I - 1) * 256>(pa);
            else fr[S & 1].w[I ? I - 4 : 0] = lds_at<so + 8192 + (I ? I - 4 : 0) * 2048>(pw);
        }
    };
    auto step = [&]<int S>() {
        if constexpr (S == 6) wait_vm<0>();
        if constexpr (S % 2 == 0) { lgkm_bar(); SBAR(); }
        if constexpr (S == 6) { if (wid == 0) panel_post(ws, FL_Y, p, j, 2u, lane); SBAR(); }
            [&]<int... U>(std::integer_sequence<int, U...>) {
                ((acc[U / 4][U % 4] = mfma16(fr[S & 1].w[U / 4], fr[S & 1].a[U % 4], acc[U / 4][U % 4]), SBAR(), rd.template operator()<S + 1, U>(), SBAR()), ...);
            }(std::make_integer_sequence<int, 28>{});
    };
    bar();
    [&]<int... I>(std::integer_sequence<int, I...>) { (rd.template operator()<0, I>(), ...); }(std::make_integer_sequence<int, 11>{});
    [&]<int... S>(std::integer_sequence<int, S...>) { (step.template operator()<S>(), ...); }(std::make_integer_sequence<int, F1_NST>{});
    f32x2 st[4];
#pragma unroll
    for (int tb = 0; tb < 4; ++tb) st[tb] = *(const LDSP f32x2*)(lds + F1_AUX + 2048 + (th * 64 + tb * 16 + (lane & 15)) * 8);
    const lptr pu = lds + F1_AUX + (nh * 16 + 4 * g) * 4;
    const unsigned gp = WS_G + p * (F_ / 8 * 2048) + j * 57344, vo = nh * 4096 + th * 1024 + (g & 1) * 2048 + ((lane >> 5) * 16 + (lane & 15)) * 16;
    const LP pg = mk_lp(lds + vo);
    f32x4 u = *(const LDSP f32x4*)pu, tt = *(const LDSP f32x4*)(pu + 1024);
    const f32x2 cl = {-1.4426950408889634f, -1.4426950408889634f};
#pragma unroll
    for (int b = 0; b < 7; ++b) {
        f32x4 un, tn;
        if (b < 6) { un = *(const LDSP f32x4*)(pu + (b + 1) * 128); tn = *(const LDSP f32x4*)(pu + 1024 + (b + 1) * 128); }
        unsigned w[4][2];
#pragma unroll
        for (int tb = 0; tb < 4; ++tb)
#pragma unroll
            for (int e = 0; e < 2; ++e) {
                const f32x2 a = __builtin_elementwise_fma((f32x2){u[2 * e], u[2 * e + 1]}, (f32x2){-st[tb][0], -st[tb][0]}, (f32x2){acc[b][tb][2 * e], acc[b][tb][2 * e + 1]});
                const f32x2 z = __builtin_elementwise_fma(a, (f32x2){st[tb][1], st[tb][1]}, (f32x2){tt[2 * e], tt[2 * e + 1]});
                const f32x2 dn = __builtin_elementwise_fma((f32x2){__builtin_amdgcn_exp2f(z[0]), __builtin_amdgcn_exp2f(z[1])}, cl, cl);
                const f32x2 o = z * (f32x2){__builtin_amdgcn_rcpf(dn[0]), __builtin_amdgcn_rcpf(dn[1])};
                w[tb][e] = pk2(o[0], o[1]);
            }
#pragma unroll
        for (int tp = 0; tp < 2; ++tp) {
            const auto s0 = __builtin_amdgcn_permlane32_swap(w[2 * tp][0], w[2 * tp + 1][0], false, false);
            const auto s1 = __builtin_amdgcn_permlane32_swap(w[2 * tp][1], w[2 * tp + 1][1], false, false);
            const u32x4 o = {s0[0], s1[0], s0[1], s1[1]};
            st16(mk_rsrc(ws), vo + tp * 512, gp + b * 8192, o);
            *(LDSP u32x4*)(pg.b[(b * F2_STAGE) >> 16] + ((b * F2_STAGE) & 65535) + tp * 512) = o;
        }
        u = un; tt = tn;
        if (b == 1) bar();
        SBAR();
    }
    wait_vm<2 * (7 - F2_EB)>();
    lgkm_bar();
    if (wid == 0) panel_post(ws, FL_G, p, j, 1u, lane);
}

struct F2Frag { bf16x8 a[4], w[4]; };
constexpr int f2_issued(int m) { return m <= 2 ? F2_NS : cmin(2 * m - 1 + F2_NS, F2_NST); }
constexpr int f2_cnt(int s) { return s < 7 ? 0 : s < F2_NS ? 2 : s < F2_MAIN ? 4 : 3; }
DI void f2_loader(char* ws, lptr lds, int lane, int lw, int p, int j) {
    const int b = blockIdx.x, kh = b & 1, jn = (b >> 3) & 7;
    const unsigned ldsb = (unsigned)(uintptr_t)lds;
    const char* ga = ws + WS_G + (size_t)p * (F_ / 8 * 2048);
    const char* gw = ws + WS_W2 + (size_t)jn * 917504;
    const unsigned va0 = lw * 2048 + lane * 16, vk0 = ((lw >> 1) * 4 + kh * 2 + (lw & 1)) * 1024 + lane * 16;
    bar();
    f2_w(ws, ldsb, 4, lw, lane);
    wait_vm<0>();
    bar();
    f2_w(ws, ldsb, 9, lw, lane);
    panel_wait(ws, FL_G, p, j, ((lane >> 3) & 1) == kh, lane);
    auto issue = [&]<int S>() {
        const unsigned ma = ldsb + (S % F2_NS) * F2_STAGE + lw * 2048, off = f2_off(S), va = va0, vk = vk0;
        const char* sa = ga + off; const char* sw = gw + off;
        if constexpr (S < F2_NS) {
            asm volatile(SETM0(ma) GLDA(va, sa, 0) GLDA(va, sa, 1024)::[ma] "s"(ma), [va] "v"(va), [sa] "s"(sa) : "memory");
        } else if constexpr (S < F2_MAIN) {
            const unsigned mw = ma + 8192;
            asm volatile(SETM0(ma) GLDA(va, sa, 0) GLDA(va, sa, 1024) SETM0(mw) GLDW(va, sw, 0) GLDW(va, sw, 1024)
                         ::[ma] "s"(ma), [mw] "s"(mw), [va] "v"(va), [sa] "s"(sa), [sw] "s"(sw) : "memory");
        } else {
            const unsigned mw = ldsb + (S % F2_NS) * F2_STAGE + 8192 + lw * 1024;
            asm volatile(SETM0(ma) GLDA(va, sa, 0) GLDA(va, sa, 1024) SETM0(mw) GLDW(vk, sw, 0)
                         ::[ma] "s"(ma), [mw] "s"(mw), [va] "v"(va), [vk] "v"(vk), [sa] "s"(sa), [sw] "s"(sw) : "memory");
        }
    };
    auto range = [&]<int S0, int... I>(std::integer_sequence<int, I...>) { (issue.template operator()<S0 + I>(), ...); };
    range.template operator()<7>(std::make_integer_sequence<int, F2_NS - 7>{});
    unsigned fl;
    static_assert(f2_issued(F2_PA + 2) <= F2_LATE, "");
    auto sync = [&]<int M>() {
        constexpr int need = cmin(2 * M + 2, F2_NST - 1), have = f2_issued(M);
        constexpr int from = M == F2_PA + 2 ? f2_issued(F2_PA + 1) : need + 1;
        constexpr int out = [] { int c = M == F2_PA + 1 ? 1 : 0; for (int s = from; s < have; ++s) c += f2_cnt(s); return c; }();
        static_assert(out < 64 && need < have, "");
        wait_vm<out>();
        bar();
        if constexpr (M == F2_PA + 2) {
            pin(fl);
            if (!__all(fl >= 2u)) panel_wait(ws, FL_G, p, j, 2u, lane);
        }
        range.template operator()<have>(std::make_integer_sequence<int, f2_issued(M + 1) - have>{});
        if constexpr (M == F2_PA) async_ld(fl, (lane & 15) * 4, ws + WS_FLAG + FL_G + (p * 16 + j) * 128);
    };
    [&]<int... I>(std::integer_sequence<int, I...>) { (sync.template operator()<I + 2>(), ...); }(std::make_integer_sequence<int, F2_NST / 2 - 2>{});
}
DI void f2_compute(char* __restrict__ ws, u16* __restrict__ out, lptr lds, int lane, int wid, int p, int j) {
    const int b = blockIdx.x, kh = b & 1, jn = (b >> 3) & 7;
    const int th = wid & 1, nh = wid >> 1, g = lane >> 4;
    f32x4 acc[4][4] = {};
    const LP pa = mk_lp(lds + g * 2048 + (th * 64 + (lane & 15)) * 16);
    const LP pk = mk_lp(lds + (nh * 4 + kh * 2) * 1024 + lane * 16), ps = mk_lp(lds + (nh * 4 + (kh ^ 1) * 2) * 1024 + lane * 16);
    F2Frag fr[2];
    LP pt;
    auto rd = [&]<int S, int I>() {
        constexpr int so = (S % F2_NS) * F2_STAGE;
        if constexpr (S < F2_NST && I < (S < F2_MAIN ? 8 : 6)) {
            if constexpr (I >= 1 && I <= 4) fr[S & 1].a[I - 1] = lds_at<so + (I - 1) * 256>(pa);
            else if constexpr (I >= 6) fr[S & 1].w[I - 4] = lds_at<so + 8192 + (I - 6) * 1024>(ps);
            else fr[S & 1].w[I ? 1 : 0] = lds_at<so + 8192 + (I ? 1024 : 0)>(S < F2_MAIN ? pk : pt);
        }
    };
    auto step = [&]<int S>() {
            [&]<int... U>(std::integer_sequence<int, U...>) {
                ((acc[U / 4][U % 4] = mfma16(fr[S & 1].w[U / 4], fr[S & 1].a[U % 4], acc[U / 4][U % 4]), SBAR(), rd.template operator()<S + 1, U>(), SBAR()), ...);
            }(std::make_integer_sequence<int, S < F2_MAIN ? 16 : 8>{});
    };
    [&]<int... I>(std::integer_sequence<int, I...>) { (rd.template operator()<0, I>(), ...); }(std::make_integer_sequence<int, 8>{});
    static_assert(F2_MAIN % 2 == 0 && F2_D >= 4, "");
    const rsrc_t rws = mk_rsrc(ws);
    u32x4 xr[4]; f32x4 gb[2];
    const int c8 = jn * 16 + nh * 8 + kh * 4 + g;
    auto pair = [&]<int M>() {
        if constexpr (2 * M == F2_PS) wait_vm<0>();
        if constexpr (M >= 2) lgkm_bar();
        if constexpr (2 * M == F2_PS) { if (wid == 0) panel_post(ws, FL_G, p, j, 2u, lane); }
        if constexpr (M == F2_XP) {
            asm volatile("" ::: "memory");
#pragma unroll
            for (int tb = 0; tb < 4; ++tb) xr[tb] = ld16(rws, WS_X2 + p * 262144 + c8 * 2048 + (th * 64 + tb * 16 + (lane & 15)) * 16);
            gb[0] = ld16f(rws, WS_MOD + (5120 + c8 * 8) * 4); gb[1] = ld16f(rws, WS_MOD + (5120 + c8 * 8 + 4) * 4);
            SBAR();
        }
        SBAR();
        step.template operator()<2 * M>(); step.template operator()<2 * M + 1>();
    };
    [&]<int... M>(std::integer_sequence<int, M...>) { (pair.template operator()<M>(), ...); }(std::make_integer_sequence<int, F2_MAIN / 2 - 1>{});
    pt = mk_lp(lds + nh * 2048 + lane * 16);
    lgkm_bar(); SBAR();
    step.template operator()<F2_MAIN - 2>(); step.template operator()<F2_MAIN - 1>();
    const unsigned exo = WS_EX + wid * 4096 + lane * 16;
#pragma unroll
    for (int i = 0; i < 4; ++i) {
        u32x4 o = {pk2(acc[2][i][0], acc[2][i][1]), pk2(acc[2][i][2], acc[2][i][3]), pk2(acc[3][i][0], acc[3][i][1]), pk2(acc[3][i][2], acc[3][i][3])};
#pragma unroll
        for (int e = 0; e < 4; ++e) o[e] = o[e] < 0xfffffffeu ? o[e] : 0xfffffffeu;
        st16<16>(rws, exo + i * 1024, b * 16384, o);
    }
    SBAR();
    u32x4 pr[4];
    auto tail = [&]<int M>() {
        lgkm_bar(); SBAR();
        if constexpr (M == F2_MP) {
#pragma unroll
            for (int i = 0; i < 4; ++i) pr[i] = ld16(rws, exo + i * 1024 + (b ^ 1) * 16384);
            SBAR();
        }
        step.template operator()<F2_MAIN + 2 * M>(); step.template operator()<F2_MAIN + 2 * M + 1>();
    };
    [&]<int... I>(std::integer_sequence<int, I...>) { (tail.template operator()<I>(), ...); }(std::make_integer_sequence<int, F2_D>{});
    for (int spin = 0; spin < (1 << 20); ++spin) {
        unsigned m = 0;
#pragma unroll
        for (int i = 0; i < 16; ++i) m = m > pr[i >> 2][i & 3] ? m : pr[i >> 2][i & 3];
        if (!__any(m == 0xffffffffu)) break;
        asm volatile("" ::: "memory");
#pragma unroll
        for (int i = 0; i < 4; ++i) pr[i] = ld16(rws, exo + i * 1024 + (b ^ 1) * 16384);
    }
#pragma unroll
    for (int tb = 0; tb < 4; ++tb) {
        const f32x4 lo = acc[0][tb] + (f32x4){bflo(pr[tb][0]), bfhi(pr[tb][0]), bflo(pr[tb][1]), bfhi(pr[tb][1])};
        const f32x4 hi = acc[1][tb] + (f32x4){bflo(pr[tb][2]), bfhi(pr[tb][2]), bflo(pr[tb][3]), bfhi(pr[tb][3])};
        const u32x4 xv = xr[tb];
        const u32x4 o = {pk2(bflo(xv[0]) + gb[0][0] * lo[0], bfhi(xv[0]) + gb[0][1] * lo[1]), pk2(bflo(xv[1]) + gb[0][2] * lo[2], bfhi(xv[1]) + gb[0][3] * lo[3]),
                         pk2(bflo(xv[2]) + gb[1][0] * hi[0], bfhi(xv[2]) + gb[1][1] * hi[1]), pk2(bflo(xv[3]) + gb[1][2] * hi[2], bfhi(xv[3]) + gb[1][3] * hi[3])};
        st16(mk_rsrc(out), (lane & 15) * (D_ * 2) + c8 * 16, (p * 128 + th * 64 + tb * 16) * (D_ * 2), o);
    }
}

constexpr int MAIN_LDS = 163840;
__global__ void __launch_bounds__(512) k_main(const u16* __restrict__ x, const u16* __restrict__ wk, const u16* __restrict__ wv, const u16* __restrict__ wo, const u16* __restrict__ w1, const u16* __restrict__ w2,
                                              char* __restrict__ ws, u16* __restrict__ out) {
    extern __shared__ __attribute__((aligned(16))) char smem[];
    const lptr lds = (lptr)smem;
    const int lane = threadIdx.x & 63, wid = __builtin_amdgcn_readfirstlane(threadIdx.x >> 6);
    int p, j; tile_map(blockIdx.x, p, j);
    ph_ln_kv(x, wk, wv, ws, lds, lane, wid, p, j);
    ph_qattn(wo, w1, w2, ws, lds, lane, wid, p, j);
    if (wid >= 4) {
        if (wid < 6) { op_loader<4>(ws, lds, lane, wid - 4, p, j); f1_loader<4>(ws, p, j, lds, wid - 4, lane); }
        else { op_loader<3>(ws, lds, lane, wid - 4, p, j); f1_loader<3>(ws, p, j, lds, wid - 4, lane); }
        f2_loader(ws, lds, lane, wid - 4, p, j);
    } else {
        op_compute(x, ws, lds, lane, wid, p, j);
        f1_compute(ws, lds, lane, wid, p, j);
        f2_compute(ws, out, lds, lane, wid, p, j);
    }
}

extern "C" void kernel_launch(void* const* d_in, const int* in_sizes, int n_in, void* d_out, int out_size, void* d_ws, size_t ws_size,
                              hipStream_t stream) {
    const u16* x = (const u16*)d_in[0];
    const u16* wq = (const u16*)d_in[1];
    const u16* wk = (const u16*)d_in[2];
    const u16* wv = (const u16*)d_in[3];
    const u16* wo = (const u16*)d_in[4];
    const u16* w1 = (const u16*)d_in[6];
    const u16* w2 = (const u16*)d_in[7];
    const u16* c = (const u16*)d_in[8];
    const u16* wmod = (const u16*)d_in[9];
    char* ws = (char*)d_ws;
    (void)hipFuncSetAttribute((const void*)k_main, hipFuncAttributeMaxDynamicSharedMemorySize, MAIN_LDS);
    k_prep<<<256, 512, 32768, stream>>>(c, wmod, wq, ws);
    k_main<<<256, 512, MAIN_LDS, stream>>>(x, wk, wv, wo, w1, w2, ws, (u16*)d_out);
}
```
